```python
import torch

def _softmax(x, axis=-1):
    return torch.softmax(x, dim=axis)
def _gelu(x):
    return torch.nn.functional.gelu(x, approximate="tanh")
def _silu(x):
    return torch.nn.functional.silu(x)
def _sigmoid(x):
    return torch.sigmoid(x)
def _layernorm(x, axis=-1, eps=1e-5):
    m = x.mean(dim=axis, keepdim=True)
    v = x.var(dim=axis, keepdim=True, unbiased=False)
    return (x - m) / torch.sqrt(v + eps)
def _scatter_add(tgt, idx, src, axis=0):
    return tgt.clone().index_add_(axis, idx, src)
def _segment_sum(src, seg, n):
    out = torch.zeros((n,) + src.shape[1:], dtype=src.dtype)
    return out.index_add_(0, seg, src)
def _pad(x, pw):
    flat = tuple(v for pair in reversed(pw) for v in pair)
    return torch.nn.functional.pad(x, flat)

kernel_name = 'interleave_layers__linear_kernel_hedgehog__ssm_conv_h3__prompt'

def setup_inputs():

    _w = (lambda a: (lambda t: t.to(torch.bfloat16) if t.is_floating_point() else t)(torch.as_tensor(a)))
    def _A_setup():
        B, S, D, H = 1, 2048, 576, 32
        Dh = D // H
        return {
            "x": _w((lambda *s: torch.randn(*s, dtype=torch.bfloat16) * 0.05)(B, S, D)),
            "wqkv": _w((lambda *s: torch.randn(*s, dtype=torch.bfloat16) * 0.4)(D, 3 * D)),
            "w_phi": _w((lambda *s: torch.randn(*s, dtype=torch.bfloat16) * 0.4)(H, Dh, Dh) * (Dh ** -0.5)),
            "wo": _w((lambda *s: torch.randn(*s, dtype=torch.bfloat16) * 0.05)(D, D)),
        }
    _A_in = _A_setup()

    def _B_setup():
        B, S, D = 1, 2048, 576
        ks = 4
        return {
            "q": _w((lambda *s: torch.randn(*s, dtype=torch.bfloat16) * 0.05)(B, S, D)),
            "k": _w((lambda *s: torch.randn(*s, dtype=torch.bfloat16) * 0.8)(B, S, D)),
            "v": _w((lambda *s: torch.randn(*s, dtype=torch.bfloat16) * 0.8)(B, S, D)),
            "shift_k": _w((lambda *s: torch.randn(*s, dtype=torch.bfloat16) * 0.8)(D, ks) * 0.5),
            "ssm_k": _w((lambda *s: torch.randn(*s, dtype=torch.bfloat16) * 1.6)(D, S) * (1.0 / S ** 0.5)),
            "ssm_D": _w((lambda *s: torch.randn(*s, dtype=torch.bfloat16) * 0.8)(D)),
        }
    _B_in = _B_setup()

    return {
        "A_x": _A_in["x"],
        "A_wqkv": _A_in["wqkv"],
        "A_w_phi": _A_in["w_phi"],
        "A_wo": _A_in["wo"],
        "B_q": _B_in["q"],
        "B_k": _B_in["k"],
        "B_v": _B_in["v"],
        "B_shift_k": _B_in["shift_k"],
        "B_ssm_k": _B_in["ssm_k"],
        "B_ssm_D": _B_in["ssm_D"],
    }

def reference(A_x, A_wqkv, A_w_phi, A_wo, B_q, B_k, B_v, B_shift_k, B_ssm_k, B_ssm_D):
    def _A_ref(x, wqkv, w_phi, wo):
        B, S, D = x.shape
        H = 32; Dh = D // H
        qkv = (lambda x, *d: x.permute(*d))((x @ wqkv).reshape(B, S, 3, H, Dh), 2, 0, 3, 1, 4)
        q, k, v = qkv[0], qkv[1], qkv[2]
        pq = torch.einsum("bhsd,hdf->bhsf", q, w_phi)
        pk = torch.einsum("bhsd,hdf->bhsf", k, w_phi)
        phi_q = torch.concatenate([_softmax(pq, axis=-1), _softmax(-pq, axis=-1)], axis=-1)
        phi_k = torch.concatenate([_softmax(pk, axis=-1), _softmax(-pk, axis=-1)], axis=-1)
        kv = torch.einsum("bhsf,bhsd->bhfd", phi_k, v)
        z  = torch.einsum("bhsf->bhf", phi_k)
        num = torch.einsum("bhqf,bhfd->bhqd", phi_q, kv)
        den = torch.einsum("bhqf,bhf->bhq", phi_q, z)[..., None] + 1e-6
        o = num / den
        return (lambda x, *d: x.permute(*d))(o, 0, 2, 1, 3).reshape(B, S, D) @ wo

    def _B_ref(q, k, v, shift_k, ssm_k, ssm_D):
        B, S, D = q.shape
        ks = shift_k.shape[-1]
        k_pad = _pad(k, ((0, 0), (ks - 1, 0), (0, 0)))
        k_shift = torch.zeros_like(k)
        for t in range(ks):
            k_shift = k_shift + k_pad[:, t:t + S, :] * shift_k[:, ks - 1 - t][None, None, :]
        kv = k_shift * v
        L = 2 * S
        kv_pad = _pad(kv, ((0, 0), (0, S), (0, 0)))
        kf_pad = _pad(ssm_k, ((0, 0), (0, S)))
        kv_f = (lambda x, axes=(-2,-1): torch.fft.rfft2(x.float(), dim=axes))(kv_pad[:, None, :, :], axes=(1, 2))
        kf_f = (lambda x, axes=(-2,-1): torch.fft.rfft2(x.float(), dim=axes))(kf_pad[:, None, :], axes=(1, 2))
        kf_f = (lambda x, *d: x.permute(*d))(kf_f, 1, 2, 0)
        y_f = kv_f * kf_f[None, :, :, :]
        y = (lambda x, s, axes=(-2,-1): torch.fft.irfft2(x, s=s, dim=axes).to(torch.bfloat16))(y_f, (1, L), axes=(1, 2))[:, 0, :S, :]
        y = y + kv * ssm_D[None, None, :]
        return q * y

    x = A_x
    x = x + _A_ref(_layernorm(x, axis=-1), A_wqkv, A_w_phi, A_wo)
    x = x + _B_ref(_layernorm(x, axis=-1), B_k, B_v, B_shift_k, B_ssm_k, B_ssm_D)
    x = x + _A_ref(_layernorm(x, axis=-1), A_wqkv, A_w_phi, A_wo)
    x = x + _B_ref(_layernorm(x, axis=-1), B_k, B_v, B_shift_k, B_ssm_k, B_ssm_D)
    return x
```

```cpp
#include <hip/hip_runtime.h>
#include <hip/hip_bf16.h>
typedef unsigned short u16;
#define S 2048
#define D 576
#define H 32
#define DH 18
__device__ __forceinline__ float b2f(u16 b) { return __uint_as_float((unsigned)b << 16); }
__device__ __forceinline__ u16 f2b(float f) { __hip_bfloat16 h = __float2bfloat16(f); return *(u16*)&h; }

__global__ void k_kv(const u16* k, const u16* v, const u16* sk, u16* kvT) {
  int i = blockIdx.x * 256 + threadIdx.x; if (i >= S * D) return;
  int s = i / D, d = i % D; float a = 0;
  for (int j = 0; j < 4; ++j) if (s - j >= 0) a += b2f(k[(s - j) * D + d]) * b2f(sk[d * 4 + j]);
  kvT[d * S + s] = f2b(a * b2f(v[i]));
}
__global__ void __launch_bounds__(256) k_conv(const u16* kvT, const u16* h, const u16* Dv, u16* y) {
  __shared__ float su[S], sh[S];
  int d = blockIdx.x;
  for (int i = threadIdx.x; i < S; i += 256) { su[i] = b2f(kvT[d * S + i]); sh[i] = b2f(h[d * S + i]); }
  __syncthreads();
  for (int t = threadIdx.x; t < S; t += 256) { float a = 0; for (int j = 0; j <= t; ++j) a += sh[j] * su[t - j]; y[t * D + d] = f2b(a + su[t] * b2f(Dv[d])); }
}
__global__ void __launch_bounds__(64) k_ln(const u16* x, const u16* y, u16* out) {
  int s = blockIdx.x, l = threadIdx.x; float v[9], sum = 0;
  for (int j = 0; j < 9; ++j) { v[j] = b2f(x[s * D + j * 64 + l]); sum += v[j]; }
  for (int o = 32; o; o >>= 1) sum += __shfl_xor(sum, o);
  float mu = sum / D, q = 0;
  for (int j = 0; j < 9; ++j) q += (v[j] - mu) * (v[j] - mu);
  for (int o = 32; o; o >>= 1) q += __shfl_xor(q, o);
  float r = rsqrtf(q / D + 1e-5f);
  for (int j = 0; j < 9; ++j) { float n = (v[j] - mu) * r; out[s * D + j * 64 + l] = f2b(y ? v[j] + n * b2f(y[s * D + j * 64 + l]) : n); }
}
__global__ void __launch_bounds__(256) k_gemm(const u16* A, const u16* B, const u16* R, u16* C, int N, int K) {
  __shared__ float sa[32][65], sb[32][65];
  int bm = blockIdx.y * 64, bn = blockIdx.x * 64, tx = threadIdx.x & 15, ty = threadIdx.x >> 4;
  float acc[4][4] = {};
  for (int k0 = 0; k0 < K; k0 += 32) {
    for (int i = threadIdx.x; i < 2048; i += 256) { int r = i >> 5, c = i & 31; sa[c][r] = b2f(A[(bm + r) * K + k0 + c]); }
    for (int i = threadIdx.x; i < 2048; i += 256) { int r = i >> 6, c = i & 63; sb[r][c] = b2f(B[(k0 + r) * N + bn + c]); }
    __syncthreads();
    for (int k = 0; k < 32; ++k) for (int i = 0; i < 4; ++i) for (int j = 0; j < 4; ++j) acc[i][j] += sa[k][ty * 4 + i] * sb[k][tx * 4 + j];
    __syncthreads();
  }
  for (int i = 0; i < 4; ++i) for (int j = 0; j < 4; ++j) { int r = bm + ty * 4 + i, c = bn + tx * 4 + j; float v = acc[i][j]; if (R) v += b2f(R[r * N + c]); C[r * N + c] = f2b(v); }
}
__global__ void __launch_bounds__(256) k_feat(const u16* qkv, const u16* wphi, u16* phi  ) {
  int i = blockIdx.x * 256 + threadIdx.x; if (i >= 2 * S * H) return;
  int w = i / (S * H), s = (i / H) % S, h = i % H; float p[DH], q[DH];
  for (int d = 0; d < DH; ++d) q[d] = b2f(qkv[s * 3 * D + w * D + h * DH + d]);
  float mx = -1e30f, mn = 1e30f;
  for (int f = 0; f < DH; ++f) { float a = 0; for (int d = 0; d < DH; ++d) a += q[d] * b2f(wphi[(h * DH + d) * DH + f]); p[f] = a; mx = fmaxf(mx, a); mn = fminf(mn, a); }
  float s1 = 0, s2 = 0;
  for (int f = 0; f < DH; ++f) { s1 += __expf(p[f] - mx); s2 += __expf(mn - p[f]); }
  for (int f = 0; f < DH; ++f) { phi[(size_t)i * 36 + f] = f2b(__expf(p[f] - mx) / s1); phi[(size_t)i * 36 + 18 + f] = f2b(__expf(mn - p[f]) / s2); }
}
__global__ void __launch_bounds__(704) k_state(const u16* phik, const u16* qkv, float* st  ) {
  int h = blockIdx.x, t = threadIdx.x; if (t >= 684) return; int f = t / 19, d = t % 19; float a = 0;
  for (int s = 0; s < S; ++s) a += b2f(phik[(s * H + h) * 36 + f]) * (d < 18 ? b2f(qkv[s * 3 * D + 2 * D + h * DH + d]) : 1.0f);
  st[h * 684 + t] = b2f(f2b(a));
}
__global__ void __launch_bounds__(256) k_o(const u16* phiq, const float* st, u16* o) {
  int i = blockIdx.x * 256 + threadIdx.x; if (i >= S * H) return; int s = i / H, h = i % H; float n[19] = {};
  for (int f = 0; f < 36; ++f) { float p = b2f(phiq[(size_t)i * 36 + f]); for (int d = 0; d < 19; ++d) n[d] += p * st[h * 684 + f * 19 + d]; }
  float r = 1.0f / (n[18] + 1e-6f);
  for (int d = 0; d < DH; ++d) o[s * D + h * DH + d] = f2b(n[d] * r);
}
__global__ void k_nop(float* p) { if (threadIdx.x == 0) p[blockIdx.x * 64] = 1.f; }
#define NEXTRA 0
extern "C" void kernel_launch(void* const* d_in, const int* in_sizes, int n_in, void* d_out, int out_size, void* d_ws, size_t ws_size, hipStream_t st) {
  const u16 *x = (const u16*)d_in[0], *wqkv = (const u16*)d_in[1], *wphi = (const u16*)d_in[2], *wo = (const u16*)d_in[3];
  const u16 *bk = (const u16*)d_in[5], *bv = (const u16*)d_in[6], *sk = (const u16*)d_in[7], *ssm = (const u16*)d_in[8], *Dv = (const u16*)d_in[9];
  char* w = (char*)d_ws; const size_t MB = 1 << 20;
  u16 *kvT = (u16*)(w), *y = (u16*)(w + 4 * MB), *xn = (u16*)(w + 8 * MB), *qkv = (u16*)(w + 12 * MB), *phi = (u16*)(w + 20 * MB), *o = (u16*)(w + 32 * MB), *x1 = (u16*)(w + 36 * MB), *x2 = (u16*)(w + 40 * MB);
  float* state = (float*)(w + 44 * MB);
  k_kv<<<S * D / 256, 256, 0, st>>>(bk, bv, sk, kvT);
  k_conv<<<D, 256, 0, st>>>(kvT, ssm, Dv, y);
  const u16* cur = x;
  for (int L = 0; L < 2; ++L) {
    k_ln<<<S, 64, 0, st>>>(cur, nullptr, xn);
    k_gemm<<<dim3(27, 32), 256, 0, st>>>(xn, wqkv, nullptr, qkv, 3 * D, D);
    k_feat<<<2 * S * H / 256, 256, 0, st>>>(qkv, wphi, phi);
    k_state<<<H, 704, 0, st>>>(phi + (size_t)S * H * 36, qkv, state);
    k_o<<<S * H / 256, 256, 0, st>>>(phi, state, o);
    k_gemm<<<dim3(9, 32), 256, 0, st>>>(o, wo, cur, x1, D, D);
    k_ln<<<S, 64, 0, st>>>(x1, y, L == 0 ? x2 : (u16*)d_out);
    cur = x2;
  }
  for (int i = 0; i < NEXTRA; ++i) k_nop<<<256, 256, 0, st>>>((float*)(w + 48 * MB));
}
```

```cpp
#include <hip/hip_runtime.h>
#include <hip/hip_bf16.h>

typedef unsigned u32;
typedef unsigned short u16;
typedef float f2 __attribute__((ext_vector_type(2)));
typedef float f4 __attribute__((ext_vector_type(4)));
typedef u32 u2 __attribute__((ext_vector_type(2)));
typedef u32 u4 __attribute__((ext_vector_type(4)));
typedef u32 u2a __attribute__((ext_vector_type(2), aligned(4)));
typedef __bf16 bf2 __attribute__((ext_vector_type(2)));
typedef __bf16 bf8 __attribute__((ext_vector_type(8)));
typedef short s4v __attribute__((ext_vector_type(4)));
typedef __amdgpu_buffer_rsrc_t rsrc_t;
#define LAS __attribute__((address_space(3)))
#define DEV __device__ __forceinline__
#define UNR _Pragma("unroll")
#define NOUNR _Pragma("unroll 1")
#define FENCE() asm volatile("" ::: "memory")
#define SC1 16

constexpr u32 O_FW = 4096, O_FA = O_FW + 32768, O_FK = O_FA + 32768, O_FO = O_FK + 32768, O_FY = O_FO + 32768, O_FZ = O_FY + 32768, O_FB = O_FZ + 32768;
constexpr u32 O_WT = 262144, WT_G = 124416;
constexpr u32 O_CS = O_WT + 16 * WT_G;
constexpr u32 O_WOT = O_CS + 62464;
constexpr u32 O_KVT = O_WOT + 663552;
constexpr u32 O_YT = O_KVT + 2359296;
constexpr u32 O_SP = O_YT + 2359296, SP_L = 720896, SP_G = 45056, SP_P = 2816;
constexpr u32 O_OB = O_SP + 2 * SP_L, OB_L = 2359296, OB_P = 147456;
constexpr u32 O_ST = O_OB + 2 * OB_L;
constexpr u32 O_X2 = O_ST + 3 * 65536;
constexpr u32 O_STAMP = O_X2 + 2359296;
constexpr u32 WS_BYTES = O_STAMP + 256 * 8 * 32 * 8;
constexpr u32 L_WQ = 82944, L_STG = 46080;
constexpr u32 L_H0 = 124416, L_H1 = 132736, L_U0 = 141056, L_Y = 124416, L_XR = 133632;
constexpr u32 L_TILE = 147456, L_U1 = 147456, L_STB = 152064, L_ZERO = 154832, L_CSL = 155136;
constexpr u32 L_WOTILE = 155648, L_REDA = 155648, L_REDB = 157696;
constexpr u32 L_TK = 160256, L_TK2 = 161696, L_TQ = 161888, L_TQ2 = 163248, L_CNT = 163712, LDS_BYTES = 163840;
enum { C_PROG = 0, C_WPROG = 4, C_WOK, C_TBL, C_KVC, C_PHI, C_STB, C_PU0, C_PU1, C_PA0, C_PA1, C_PB0, C_PB1, C_REDA, C_REDB, C_FYC, C_FAOK, C_FZC, C_GEM, C_PUB };
#define CNT(i) (L_CNT + 4 * (i))
constexpr u32 SENT = 0xFFFFFFFFu;
constexpr int SPIN_G = 1 << 17, SPIN_L = 1 << 21;

#define MAP 1
__device__ __forceinline__ int wg_of(int p, int g) {
  return (p >> 2) * 2 + (g >> 3) + 8 * ((p & 3) * 8 + (g & 7));
}
struct Args {
  const u16 *x, *wqkv, *wphi, *wo, *bk, *bv, *sk, *ssm, *dv;
  u16* out;
  unsigned char* ws;
};

DEV int opq(int v) { asm volatile("" : "+v"(v)); return v; }
DEV u32 rfl(u32 v) { return __builtin_amdgcn_readfirstlane(v); }
DEV rsrc_t mkrs(const void* p, u32 n) { return __builtin_amdgcn_make_buffer_rsrc((void*)p, 0, n, 0x00020000); }
DEV u32 pk(float a, float b) { f2 v = {a, b}; return __builtin_bit_cast(u32, __builtin_convertvector(v, bf2)); }
DEV u32 pk(f2 v) { return __builtin_bit_cast(u32, __builtin_convertvector(v, bf2)); }
DEV f2 up(u32 p) { f2 r = {__uint_as_float(p << 16), __uint_as_float(p & 0xffff0000u)}; return r; }
DEV f2 rnd(f2 v) { return up(pk(v)); }
DEV float rnd1(float v) { return __uint_as_float(pk(v, v) & 0xffff0000u); }
DEV f4 mfma(u4 a, u4 b, f4 c) { return __builtin_amdgcn_mfma_f32_16x16x32_bf16(__builtin_bit_cast(bf8, a), __builtin_bit_cast(bf8, b), c, 0, 0, 0); }
DEV u32 umin(u32 a, u32 b) { return a < b ? a : b; }
DEV u32 umax(u32 a, u32 b) { return a > b ? a : b; }
DEV u32 umax3(u32 a, u32 b, u32 c) { return umax(umax(a, b), c); }
DEV u32 san(u32 v) { return umin(v, 0xFFFFFFFEu); }
DEV u4 san(u4 v) { u4 r = {san(v.x), san(v.y), san(v.z), san(v.w)}; return r; }
DEV u32 vm(u32 m, u4 v) { return umax3(umax3(m, v.x, v.y), v.z, v.w); }
DEV bool anyl(bool c) { return __builtin_amdgcn_ballot_w64(c) != 0; }
DEV float dot2(u32 a, u32 b, float c) { return __builtin_amdgcn_fdot2_f32_bf16(__builtin_bit_cast(bf2, a), __builtin_bit_cast(bf2, b), c, false); }
template <class T> DEV T ld(u32 o) { return *(LAS T*)(uintptr_t)o; }
template <class T> DEV void st(u32 o, T v) { *(LAS T*)(uintptr_t)o = v; }
DEV u2 trd(u32 o) { return __builtin_bit_cast(u2, __builtin_amdgcn_ds_read_tr16_b64_v4i16((LAS s4v*)(uintptr_t)o)); }
DEV u4 trd2(u32 o0, u32 o1) { u2 a = trd(o0), b = trd(o1); u4 r = {a.x, a.y, b.x, b.y}; return r; }
DEV u4 gld4(rsrc_t r, u32 v, u32 s, int aux) { return aux ? __builtin_amdgcn_raw_buffer_load_b128(r, v, s, SC1) : __builtin_amdgcn_raw_buffer_load_b128(r, v, s, 0); }
DEV u2 gld2(rsrc_t r, u32 v, u32 s, int aux) { return aux ? __builtin_amdgcn_raw_buffer_load_b64(r, v, s, SC1) : __builtin_amdgcn_raw_buffer_load_b64(r, v, s, 0); }
DEV u32 gld1(rsrc_t r, u32 v, u32 s, int aux) { return aux ? __builtin_amdgcn_raw_buffer_load_b32(r, v, s, SC1) : __builtin_amdgcn_raw_buffer_load_b32(r, v, s, 0); }
DEV void gst4(u4 d, rsrc_t r, u32 v, u32 s) { __builtin_amdgcn_raw_buffer_store_b128(d, r, v + s, 0, SC1); }
DEV void gst2(u2 d, rsrc_t r, u32 v, u32 s) { __builtin_amdgcn_raw_buffer_store_b64(d, r, v, s, SC1); }
DEV void gst1(u32 d, rsrc_t r, u32 v, u32 s) { __builtin_amdgcn_raw_buffer_store_b32(d, r, v, s, SC1); }
template <int AUX> DEV void dma(rsrc_t r, u32 l, u32 v, u32 s) {
#if defined(__HIP_DEVICE_COMPILE__)
  __builtin_amdgcn_raw_ptr_buffer_load_lds(r, (LAS void*)(uintptr_t)l, 16, v, s, 0, AUX);
#endif
}
DEV void vmwait0() { asm volatile("s_waitcnt vmcnt(0)" ::: "memory"); }
template <int N> DEV void vmwait() { asm volatile("s_waitcnt vmcnt(%0)" ::"n"(N) : "memory"); }
DEV void bar() { asm volatile("s_waitcnt lgkmcnt(0)\n\ts_barrier" ::: "memory"); }
DEV void lset(u32 a, u32 v) { asm volatile("ds_write_b32 %0, %1" ::"v"(a), "v"(v) : "memory"); }
DEV void ladd(u32 a, int lane) { if (lane == 0) { u32 one = 1; asm volatile("ds_add_u32 %0, %1" ::"v"(a), "v"(one) : "memory"); } }
DEV u32 ladd_rtn(u32 a, int lane) {
  u32 r = 0;
  if (lane == 0) { u32 one = 1; asm volatile("ds_add_rtn_u32 %0, %1, %2\n\ts_waitcnt lgkmcnt(0)" : "=v"(r) : "v"(a), "v"(one) : "memory"); }
  return rfl(r);
}
DEV u32 lget(u32 a) { u32 r; asm volatile("ds_read_b32 %0, %1\n\ts_waitcnt lgkmcnt(0)" : "=v"(r) : "v"(a) : "memory"); return rfl(r); }
DEV u32 lwait(u32 a, u32 t, int& dead) {
  u32 r = lget(a);
  if (__builtin_expect(r < t, 0)) {
    NOUNR for (int i = dead ? SPIN_L : 0; i < SPIN_L; ++i) { r = lget(a); if (r >= t) return r; __builtin_amdgcn_s_sleep(1); }
    dead = 1;
  }
  return r;
}
DEV void poll(rsrc_t ws, u32 off, u32 tag, int& dead) {
  NOUNR for (int i = dead ? SPIN_G : 0; i < SPIN_G; ++i) {
    FENCE();
    u32 v = gld1(ws, off, 0, 1);
    if (!anyl(v != tag)) { FENCE(); return; }
    __builtin_amdgcn_s_sleep(1);
  }
  dead = 1;
}
DEV void setflag(rsrc_t ws, u32 off, u32 tag, int lane) { if (lane == 0) gst1(tag, ws, off, 0); }
#define SW16(a, b) __builtin_amdgcn_permlane16_swap(a, b, false, false)
#define SW32(a, b) __builtin_amdgcn_permlane32_swap(a, b, false, false)
DEV float sw16add(float a, float b) { auto r = SW16(__float_as_uint(a), __float_as_uint(b)); return __uint_as_float(r[0]) + __uint_as_float(r[1]); }
DEV float sw32add(float a, float b) { auto r = SW32(__float_as_uint(a), __float_as_uint(b)); return __uint_as_float(r[0]) + __uint_as_float(r[1]); }

#define STAMP(i)
#define STAMPC(i)
#define STARG
#define STPASS

DEV f2 ln_stats(float s, float sq) {
  float mean = s * (1.0f / 576.0f);
  float var = fmaxf(sq * (1.0f / 576.0f) - mean * mean, 0.0f);
  float sd = rnd1(__builtin_amdgcn_sqrtf(rnd1(rnd1(var) + 1e-5f)));
  f2 r = {rnd1(mean), __builtin_amdgcn_rcpf(sd)};
  return r;
}
DEV void stats_from(u4 v, float (&m)[2], float (&rs)[2]) {
  float s = __uint_as_float(v.x) + __uint_as_float(v.z), sq = __uint_as_float(v.y) + __uint_as_float(v.w);
  s = sw16add(s, s); sq = sw16add(sq, sq);
  f2 t = ln_stats(s, sq);
  auto r0 = SW32(__float_as_uint(t.x), __float_as_uint(t.x));
  auto r1 = SW32(__float_as_uint(t.y), __float_as_uint(t.y));
  m[0] = __uint_as_float(r0[0]); m[1] = __uint_as_float(r0[1]); rs[0] = __uint_as_float(r1[0]); rs[1] = __uint_as_float(r1[1]);
}
DEV u4 ld_stats(rsrc_t ws, u32 off) { u2 a = gld2(ws, off, 0, 1), b = gld2(ws, off + 256, 0, 1); return u4{a.x, a.y, b.x, b.y}; }
DEV u4 poll_stats(rsrc_t ws, u32 off, u4 v, int& dead) {
  if (anyl(vm(0, v) == SENT)) {
    NOUNR for (int i = dead ? SPIN_G : 0; i < SPIN_G; ++i) {
      FENCE();
      v = ld_stats(ws, off);
      if (!anyl(vm(0, v) == SENT)) { FENCE(); return v; }
    }
    dead = 1;
  }
  FENCE();
  return v;
}

DEV void pub_stats(rsrc_t ws, float s0, float s1, float q0, float q1, u32 red, u32 cnt, u32 last, u32 dst, int w, int lane) {
  float t = sw16add(sw32add(s0, s1), sw32add(q0, q1));
  st<float>(red + w * 256 + (16 * (lane >> 5) + (lane & 15)) * 8 + ((lane >> 4) & 1) * 4, t);
  FENCE();
  if (ladd_rtn(cnt, lane) == last) {
    f4 r = {0, 0, 0, 0};
    UNR for (int i = 0; i < 8; ++i) r += ld<f4>(red + i * 256 + (lane & 15) * 16);
    gst4(san(__builtin_bit_cast(u4, r)), ws, lane < 16 ? dst + lane * 16 : 0x80000000u, 0);
  }
}

struct KVin { u32 k[11], v[8]; u4 w; };
DEV void kv_load(const Args& a, int T, int rt, int c, KVin& in) {
  rsrc_t rk = mkrs(a.bk, 2359296), rv = mkrs(a.bv, 2359296), rw = mkrs(a.sk, 4608);
  int cp = T >> 2, sub = T & 3, ch = 144 * c + 2 * cp, s0 = 32 * rt + 8 * sub;
  u32 base = s0 * 1152 + ch * 2;
  UNR for (int i = 0; i < 3; ++i) in.k[i] = gld1(rk, s0 == 0 ? 0x80000000u : base - (3 - i) * 1152, 0, 0);
  UNR for (int i = 0; i < 8; ++i) in.k[3 + i] = gld1(rk, base, i * 1152, 0);
  UNR for (int i = 0; i < 8; ++i) in.v[i] = gld1(rv, base, i * 1152, 0);
  in.w = gld4(rw, ch * 8, 0, 0);
}
DEV void kv_compute(rsrc_t ws, int T, int rt, int c, const KVin& in) {
  int cp = T >> 2, sub = T & 3, ch = 144 * c + 2 * cp, s0 = 32 * rt + 8 * sub;
  f2 w0 = {__uint_as_float(in.w.x << 16), __uint_as_float(in.w.z << 16)}, w1 = {__uint_as_float(in.w.x & 0xffff0000u), __uint_as_float(in.w.z & 0xffff0000u)};
  f2 w2 = {__uint_as_float(in.w.y << 16), __uint_as_float(in.w.w << 16)}, w3 = {__uint_as_float(in.w.y & 0xffff0000u), __uint_as_float(in.w.w & 0xffff0000u)};
  f2 kf[11];
  UNR for (int i = 0; i < 11; ++i) kf[i] = up(in.k[i]);
  u32 P[8];
  UNR for (int s = 0; s < 8; ++s) {
    f2 t = rnd(kf[s] * w3);
    t = rnd(t + rnd(kf[s + 1] * w2));
    t = rnd(t + rnd(kf[s + 2] * w1));
    t = rnd(t + rnd(kf[s + 3] * w0));
    P[s] = pk(t * up(in.v[s]));
  }
  u4 o0, o1;
  UNR for (int d = 0; d < 4; ++d) {
    o0[d] = __builtin_amdgcn_perm(P[6 - 2 * d], P[7 - 2 * d], 0x05040100u);
    o1[d] = __builtin_amdgcn_perm(P[6 - 2 * d], P[7 - 2 * d], 0x07060302u);
  }
  u32 off = O_KVT + ch * 4096 + (2040 - s0) * 2;
  gst4(o0, ws, off, 0);
  gst4(o1, ws, off, 4096);
}

struct Hin { u4 h[4]; u32 n[4]; };
DEV void h_load(const Args& a, int ch, int lane, Hin& in) {
  rsrc_t rh = mkrs(a.ssm, 2359296);
  UNR for (int m = 0; m < 4; ++m) { in.h[m] = gld4(rh, ch * 4096 + lane * 16, m * 1024, 0); in.n[m] = gld1(rh, ch * 4096 + lane * 16 + 16, m * 1024, 0); }
}
DEV void h_stage(u32 lH, u32 lU, int lane, const Hin& in) {
  UNR for (int m = 0; m < 4; ++m) {
    u4 h = in.h[m];
    u4 s = {__builtin_amdgcn_alignbit(h.y, h.x, 16), __builtin_amdgcn_alignbit(h.z, h.y, 16), __builtin_amdgcn_alignbit(h.w, h.z, 16), __builtin_amdgcn_alignbit(in.n[m], h.w, 16)};
    st<u4>(lH + 32 + m * 1024 + lane * 16, h);
    st<u4>(lH + 4160 + 32 + m * 1024 + lane * 16, s);
  }
  u32 h0 = rfl(in.h[0].x) << 16;
  u4 z = {0, 0, 0, lane == 3 ? h0 : 0u};
  if (lane < 4) st<u4>(lH + (lane >> 1) * 4160 + (lane & 1) * 16, z);
  u4 zz = {0, 0, 0, 0};
  if (lane < 30) st<u4>(lU + 4096 + lane * 16, zz);
}
DEV void u_stage(rsrc_t ws, u32 lU, int ch, int lane) {
  UNR for (int m = 0; m < 4; ++m) dma<SC1>(ws, lU + m * 1024, O_KVT + ch * 4096 + lane * 16, m * 1024);
}
DEV void conv_pair(const Args& a, rsrc_t ws, u32 lH, u32 lU, int ch, int e, u32 cA, u32 cB, u32 tgt, int lane, int& dead) {
  const int q = lane >> 4, i16 = lane & 15;
  f4 acc[8];
  UNR for (int i = 0; i < 8; ++i) acc[i] = f4{0, 0, 0, 0};
  const u32 aB = lU + 2 * (2032 - 16 * i16 + 8 * q) - 3584 + 256 * e;
  const u32 aA = lH + ((i16 & 1) ? 0 : 4160) + 4 * (((i16 + 1) >> 1) + 4 * q) + 256 * e;
  const u32 aA4[4] = {aA, aA + 1024, aA + 2048, aA + 3072};
  float dv = __uint_as_float((u32)a.dv[ch] << 16);
  u4 B[8];
  u2 Al[8], Ah[8];
#define CLD(t, bb)                                                                                                              \
  asm volatile("ds_read2_b32 %0, %3 offset0:%5 offset1:%6\n\tds_read2_b32 %1, %3 offset0:%7 offset1:%8\n\tds_read_b128 %2, %4 offset:%9" \
               : "=&v"(Al[t]), "=&v"(Ah[t]), "=&v"(B[7 - (t)])                                                                    \
               : "v"(aA4[(t) >> 1]), "v"(aB), "n"(16 * (bb) + 128 * ((t) & 1)), "n"(16 * (bb) + 128 * ((t) & 1) + 1),              \
                 "n"(16 * (bb) + 128 * ((t) & 1) + 2), "n"(16 * (bb) + 128 * ((t) & 1) + 3), "n"(512 * (t) + 64 * (bb))           \
               : "memory")
  UNR for (int t = 0; t < 8; ++t) CLD(t, 0);
  UNR for (int bb = 0; bb < 4; ++bb) {
    asm volatile("s_waitcnt lgkmcnt(6)" : "+v"(Al[0]), "+v"(Ah[0]), "+v"(Al[1]), "+v"(Ah[1]), "+v"(Al[2]), "+v"(Ah[2]), "+v"(Al[3]), "+v"(Ah[3]), "+v"(Al[4]),
                 "+v"(Ah[4]), "+v"(Al[5]), "+v"(Ah[5]), "+v"(B[7]), "+v"(B[6]), "+v"(B[5]), "+v"(B[4]), "+v"(B[3]), "+v"(B[2]));
    UNR for (int t = 0; t < 8; ++t) {
      u4 A = {Al[t].x, Al[t].y, Ah[t].x, Ah[t].y};
      UNR for (int v = 7 - t; v >= 0; --v) {
        if (t == 0 && v == 1) {
          __builtin_amdgcn_sched_barrier(0);
          asm volatile("s_waitcnt lgkmcnt(0)" : "+v"(Al[6]), "+v"(Ah[6]), "+v"(Al[7]), "+v"(Ah[7]), "+v"(B[1]), "+v"(B[0]));
        }
        acc[t + v] = mfma(A, B[v], acc[t + v]);
      }
      __builtin_amdgcn_sched_barrier(0);
      if (bb < 3) CLD(t, bb + 1);
    }
  }
  FENCE();
  ladd(cA, lane);
  lwait(cA, tgt, dead);
  f4 keep[4];
  UNR for (int i = 0; i < 4; ++i) {
    keep[i] = e ? acc[4 + i] : acc[i];
    f4 snd = e ? acc[i] : acc[4 + i];
    st<f4>(lH + e * 4096 + i * 1024 + lane * 16, snd);
  }
  FENCE();
  ladd(cB, lane);
  lwait(cB, tgt, dead);
  u2 Y[4];
  UNR for (int i = 0; i < 4; ++i) {
    f4 s = keep[i] + ld<f4>(lH + (1 - e) * 4096 + i * 1024 + lane * 16);
    u2 uu = ld<u2>(lU + 2 * (2044 - 256 * (4 * e + i) - 16 * i16 - 4 * q));
    f2 u32_ = up(uu.x), u10 = up(uu.y);
    f2 g01 = {u10.y * dv, u10.x * dv}, g23 = {u32_.y * dv, u32_.x * dv};
    f2 s01 = {s.x, s.y}, s23 = {s.z, s.w};
    Y[i].x = pk(rnd(s01) + rnd(g01));
    Y[i].y = pk(rnd(s23) + rnd(g23));
  }
  UNR for (int i = 0; i < 2; ++i) {
    auto r0 = SW16(Y[2 * i].x, Y[2 * i + 1].x);
    auto r1 = SW16(Y[2 * i].y, Y[2 * i + 1].y);
    u4 o = {r0[0], r1[0], r0[1], r1[1]};
    gst4(o, ws, O_YT + ch * 4096 + 2 * (256 * (4 * e + 2 * i + (q & 1)) + 16 * i16 + 8 * (q >> 1)), 0);
  }
}

template <bool HELP, int L>
DEV void tail(const Args& a, rsrc_t ws, int b, int w, int lane0, int g, int rt, int c, int rot, u32 tag, const u4 (&wo)[18], const u4 (&wo8)[18], int& dead STARG) {
  const int lane = opq(lane0), q = lane >> 4, i16 = lane & 15;
  const bool ex = HELP && w < 6;
  const int tb8 = w & 1;
  u2 xr[3];
  if (L == 0) {
    rsrc_t rx = mkrs(a.x, 2359296);
    u32 o = (32 * rt + i16) * 1152 + (144 * c + 4 * q) * 2;
    xr[0] = gld2(rx, o, w * 32, 0); xr[1] = gld2(rx, o, w * 32 + 16 * 1152, 0);
    xr[2] = gld2(rx, o, 256 + tb8 * 16 * 1152, 0);
  } else {
    xr[0] = ld<u2>(L_XR + w * 1024 + lane * 8); xr[1] = ld<u2>(L_XR + w * 1024 + 512 + lane * 8);
    xr[2] = ld<u2>(L_XR + 8192 + tb8 * 512 + lane * 8);
  }
  {
    const u32 pb = L * OB_L + (rt >> 2) * OB_P, tbp = 2 * (rt & 3);
    u4 v[5];
    if (HELP) lwait(CNT(C_PUB), 8 * L + 8, dead);
    NOUNR for (int it = dead ? SPIN_G - 1 : 0;; ++it) {
      FENCE();
      UNR for (int i = 0; i < 4; ++i) v[i] = gld4(ws, O_OB + lane * 16, pb + (tbp + (i >> 1)) * 16384 + (w + 8 * (i & 1)) * 1024, 1);
      if (!HELP) {
        u32 o = O_OB + 131072 + q * 2048 + i16 * 8, so = pb + (w & 1) * 8192 + (tbp + (w >> 1)) * 128;
        u2 l0 = gld2(ws, o, so, 1), l1 = gld2(ws, o + 1024, so, 1);
        v[4] = u4{l0.x, l0.y, l1.x, l1.y};
      }
      u32 m = 0;
      UNR for (int i = 0; i < (HELP ? 4 : 5); ++i) m = vm(m, v[i]);
      if (!anyl(m == SENT)) break;
      if (it >= SPIN_G) { dead = 1; break; }
    }
    FENCE();
    UNR for (int i = 0; i < 4; ++i) st<u4>(L_STG + (i >> 1) * 18432 + (w + 8 * (i & 1)) * 1024 + lane * 16, v[i]);
    if (!HELP) st<u4>(L_STG + (w >> 1) * 18432 + (16 + (w & 1)) * 1024 + lane * 16, v[4]);
  }
  STAMP(16 + L * 8);
  bar();
  STAMP(17 + L * 8);
  u32 fyoff = 0, fy = tag;
  const int chl = 144 * c + ((lane & 16) ? 128 : 16 * w) + i16;
  if (L == 0) { fyoff = chl < 512 ? O_FY + (chl >> 1) * 128 : O_FZ + (chl - 512) * 128; fy = gld1(ws, fyoff, 0, 1); }
  f4 acc[3] = {f4{0, 0, 0, 0}, f4{0, 0, 0, 0}, f4{0, 0, 0, 0}};
  if (ex) {
    UNR for (int ks = 0; ks < 18; ++ks) {
      u4 b0 = ld<u4>(L_STG + ks * 1024 + lane * 16), b1 = ld<u4>(L_STG + 18432 + ks * 1024 + lane * 16);
      u4 b2 = ld<u4>(L_STG + tb8 * 18432 + ks * 1024 + lane * 16);
      acc[0] = mfma(wo[ks], b0, acc[0]); acc[1] = mfma(wo[ks], b1, acc[1]); acc[2] = mfma(wo8[ks], b2, acc[2]);
    }
  } else {
    UNR for (int ks = 0; ks < 18; ++ks) {
      u4 b0 = ld<u4>(L_STG + ks * 1024 + lane * 16), b1 = ld<u4>(L_STG + 18432 + ks * 1024 + lane * 16);
      acc[0] = mfma(wo[ks], b0, acc[0]); acc[1] = mfma(wo[ks], b1, acc[1]);
    }
  }
  u4 yv, yv8;
  if (L == 0) {
    if (anyl(fy != tag)) poll(ws, fyoff, tag, dead);
    FENCE();
    yv = gld4(ws, O_YT + (lane >> 2) * 4096 + (lane & 3) * 16, (144 * c + 16 * w) * 4096 + rt * 64, 1);
    yv8 = gld4(ws, O_YT + (lane >> 2) * 4096 + (lane & 3) * 16, (144 * c + 128) * 4096 + rt * 64, 1);
  }
  constexpr int NU = HELP ? 3 : 2;
  f2 x1[NU][2];
  float S[NU], Q[NU];
  UNR for (int u = 0; u < NU; ++u) {
    f2 lo = {acc[u].x, acc[u].y}, hi = {acc[u].z, acc[u].w};
    x1[u][0] = rnd(rnd(lo) + up(xr[u].x)); x1[u][1] = rnd(rnd(hi) + up(xr[u].y));
    f2 s = x1[u][0] + x1[u][1], sq = x1[u][0] * x1[u][0] + x1[u][1] * x1[u][1];
    S[u] = s.x + s.y; Q[u] = sq.x + sq.y;
  }
  if (HELP) { if (w == 4) { S[0] += S[2]; Q[0] += Q[2]; } if (w == 5) { S[1] += S[2]; Q[1] += Q[2]; } }
  const u32 stoff = O_ST + L * 2 * 65536 + rt * 1024 + (q & 1) * 512 + (16 * (q >> 1) + i16) * 8;
  pub_stats(ws, S[0], S[1], Q[0], Q[1], L_REDA, CNT(C_REDA), 8 * L + 7, O_ST + L * 2 * 65536 + rt * 1024 + c * 256, w, lane);
  STAMP(18 + L * 8);
  if (HELP && L == 0 && w == 4) {
    lwait(CNT(C_REDA), 8, dead);
    UNR for (int j = 5; j < 9; ++j) {
      int kr = j + rot; kr -= kr >= 9 ? 9 : 0;
      UNR for (int i = 0; i < 9; ++i) dma<SC1>(ws, j * 9216 + i * 1024, O_WT + lane * 16, g * WT_G + kr * 9216 + i * 1024);
    }
  }
  u4 sv = ld_stats(ws, stoff);
  if (L == 0) {
    st<u4>(L_Y + w * 1024 + lane * 16, yv);
    if (ex) st<u4>(L_Y + 8192 + lane * 16, yv8);
    FENCE();
  }
  u2 y[NU];
  {
    u32 ya = L_Y + (4 * q + (i16 >> 2)) * 64 + (i16 & 3) * 8;
    y[0] = trd(ya + w * 1024); y[1] = trd(ya + w * 1024 + 32);
    if (HELP) y[2] = trd(ya + 8192 + tb8 * 32);
  }
  sv = poll_stats(ws, stoff, sv, dead);
  STAMP(19 + L * 8);
  if (HELP && L == 0 && w == 4) { vmwait0(); lset(CNT(C_WPROG), 11); }
  float m[2], rs[2];
  stats_from(sv, m, rs);
  u2 x2[NU];
  UNR for (int u = 0; u < NU; ++u) {
    float mu = u < 2 ? m[u] : (tb8 ? m[1] : m[0]), ru = u < 2 ? rs[u] : (tb8 ? rs[1] : rs[0]);
    UNR for (int h = 0; h < 2; ++h) {
      f2 xn = rnd(rnd(x1[u][h] - mu) * ru);
      x2[u][h] = pk(x1[u][h] + rnd(xn * up(y[u][h])));
    }
  }
  auto r0 = SW16(x2[0].x, x2[1].x);
  auto r1 = SW16(x2[0].y, x2[1].y);
  u4 o = {r0[0], r1[0], r0[1], r1[1]};
  if (L == 0) {
    gst4(san(o), ws, O_X2 + (q & 1) * 18432 + (q >> 1) * 256 + i16 * 16, 2 * rt * 18432 + (9 * c + w) * 512);
    if (ex) {
      auto t0 = SW16(x2[NU - 1].x, x2[NU - 1].x);
      auto t1 = SW16(x2[NU - 1].y, x2[NU - 1].y);
      gst4(san(u4{t0[0], t1[0], t0[1], t1[1]}), ws, (q & 1) ? 0x80000000u : O_X2 + (q >> 1) * 256 + i16 * 16, (2 * rt + tb8) * 18432 + (9 * c + 8) * 512);
    }
    st<u2>(L_XR + w * 1024 + lane * 8, x2[0]); st<u2>(L_XR + w * 1024 + 512 + lane * 8, x2[1]);
    if (ex) st<u2>(L_XR + 8192 + tb8 * 512 + lane * 8, x2[NU - 1]);
    UNR for (int u = 0; u < NU; ++u) {
      f2 lo = up(x2[u].x), hi = up(x2[u].y);
      f2 s = lo + hi, sq = lo * lo + hi * hi;
      S[u] = s.x + s.y; Q[u] = sq.x + sq.y;
    }
    if (HELP) { if (w == 4) { S[0] += S[2]; Q[0] += Q[2]; } if (w == 5) { S[1] += S[2]; Q[1] += Q[2]; } }
    pub_stats(ws, S[0], S[1], Q[0], Q[1], L_REDB, CNT(C_REDB), 7, O_ST + 65536 + rt * 1024 + c * 256, w, lane);
  } else {
    rsrc_t ro = mkrs(a.out, 2359296);
    __builtin_amdgcn_raw_buffer_store_b128(o, ro, (16 * (q & 1) + i16) * 1152 + (q >> 1) * 16 + 32 * rt * 1152 + 288 * c + 32 * w, 0, 0);
    if (ex) __builtin_amdgcn_raw_buffer_store_b64(x2[NU - 1], ro, i16 * 1152 + q * 8, (32 * rt + 16 * tb8) * 1152 + 288 * c + 256, 0);
  }
  STAMP(20 + L * 8);
}

template <int L>
DEV void main_layer(const Args& a, rsrc_t ws, int b, int w, int lane0, int p, int g, int rt, int c, int rot, u32 tag, u4 (&B)[36], float (&mS)[2], float (&mQ)[2], u32& wp, int& dead STARG) {
    constexpr float LOG2E = 1.4426950408889634f;
    int lane = opq(lane0), q = lane >> 4, i16 = lane & 15;
    u32 aF[2], a4[2], a7[2];
    UNR for (int u = 0; u < 2; ++u) {
      aF[u] = i16 * 128 + (((4 * u + q) ^ (i16 >> 1)) << 4);
      a4[u] = (64 + 2 * (i16 >> 2) + (i16 & 1)) * 128 + (((4 * u + q) ^ (2 * (i16 >> 2))) << 4);
      a7[u] = L_WQ + (32 + (i16 >> 2)) * 128 + ((4 * u + q) << 4);
    }
    {
      f4 acc[5][2];
      u4 sv = {0, 0, 0, 0};
      const u32 stoff = O_ST + 65536 + (4 * p + w) * 1024 + (q & 1) * 512 + (16 * (q >> 1) + i16) * 8;
      if (L == 1) {
        u32 G = lane < 36 ? lane : (lane < 40 ? 9 * (lane - 36) + 8 : 0);
        u32 po = O_X2 + (8 * p + 2 * w + (lane >= 36 && lane < 40 ? 1 : 0)) * 18432 + G * 512;
        NOUNR for (int i = dead ? SPIN_G : 0; i < SPIN_G; ++i) {
          FENCE();
          u32 v = gld1(ws, po, 0, 1);
          if (!anyl(v == SENT)) break;
        }
        FENCE();
        STAMP(21);
      }
      for (int pass = 0;; ++pass) {
        if (L == 1) {
          UNR for (int j = 0; j < 9; ++j) {
            int kr = j + rot; kr -= kr >= 9 ? 9 : 0;
            UNR for (int i = 0; i < 4; ++i) B[4 * j + i] = gld4(ws, O_X2 + lane * 16 + (i >> 1) * 1024, (8 * p + 2 * w + (i & 1)) * 18432 + kr * 2048, 1);
          }
          sv = ld_stats(ws, stoff);
        }
        UNR for (int i = 0; i < 5; ++i) acc[i][0] = acc[i][1] = f4{0, 0, 0, 0};
        u32 vmx = 0;
        if (wp < (L ? 0u : 1u)) wp = lwait(CNT(C_WPROG), 1, dead);
        if (L == 0) STAMP(3);
        u4 A[2][5];
        UNR for (int i = 0; i < 4; ++i) A[0][i] = ld<u4>(aF[0] + i * 2048);
        A[0][4] = ld<u4>(a4[0]);
        u32 fl = *(volatile LAS u32*)(uintptr_t)CNT(C_WPROG);
        UNR for (int s = 0; s < 18; ++s) {
          if (s < 17) {
            const int j = (s + 1) >> 1, u = (s + 1) & 1;
            if (u == 0) {
              u32 need = L ? (j < 5 ? 0u : 11u) : (u32)(j + 1);
              wp = umax(wp, rfl(fl));
              if (wp < need) wp = lwait(CNT(C_WPROG), need, dead);
            }
            UNR for (int i = 0; i < 4; ++i) A[(s + 1) & 1][i] = ld<u4>(aF[u] + j * 9216 + i * 2048);
            A[(s + 1) & 1][4] = ld<u4>(a4[u] + j * 9216);
            if (u == 0) fl = *(volatile LAS u32*)(uintptr_t)CNT(C_WPROG);
          }
          vmx = vm(vm(vmx, B[2 * s]), B[2 * s + 1]);
          UNR for (int i = 0; i < 5; ++i) {
            acc[i][0] = mfma(A[s & 1][i], B[2 * s], acc[i][0]);
            acc[i][1] = mfma(A[s & 1][i], B[2 * s + 1], acc[i][1]);
          }
        }
        if (L == 0 || !anyl(vmx == SENT) || dead) break;
        if (pass >= 20000) { dead = 1; break; }
        FENCE();
      }
      FENCE();
      ladd(CNT(C_GEM), lane);
      STAMP(4 + L * 6);
      if (L == 0) lwait(CNT(C_TBL), 1, dead);
      else { sv = poll_stats(ws, stoff, sv, dead); stats_from(sv, mS, mQ); }
      {
        u32 kv[2][9];
        UNR for (int i = 0; i < 5; ++i) {
          f4 cs = ld<f4>(L_CSL + i * 64 + q * 16);
          f2 c01 = {cs.x, cs.y}, c23 = {cs.z, cs.w};
          UNR for (int tb = 0; tb < 2; ++tb) {
            f2 lo = {acc[i][tb].x, acc[i][tb].y}, hi = {acc[i][tb].z, acc[i][tb].w};
            kv[tb][i < 4 ? 2 * i : 8] = pk((lo - c01 * mS[tb]) * mQ[tb]);
            if (i < 4) kv[tb][2 * i + 1] = pk((hi - c23 * mS[tb]) * mQ[tb]);
          }
        }
        u32 K[9], V[9];
        UNR for (int i = 0; i < 9; ++i) { auto t = SW32(kv[0][i], kv[1][i]); K[i] = t[0]; V[i] = t[1]; }
        const bool val = (i16 >> 2) == q;
        const u32 tk = L_TK + (val ? ((q & 1) * 4 + (i16 & 3)) * 16 : 128), tk2 = L_TK2 + (val ? ((q & 1) * 4 + (i16 & 3)) * 4 : 32);
        const u4 k0 = {K[0], K[1], K[2], K[3]}, k1 = {K[4], K[5], K[6], K[7]}, k2 = {K[8], 0, 0, 0};
        f4 P[5];
        UNR for (int m = 0; m < 5; ++m) {
          u4 t2 = {ld<u32>(tk2 + m * 36), 0, 0, 0};
          P[m] = mfma(ld<u4>(tk + m * 288), k0, f4{0, 0, 0, 0});
          P[m] = mfma(ld<u4>(tk + m * 288 + 144), k1, P[m]);
          P[m] = mfma(t2, k2, P[m]);
        }
        const u32 row = L_STG + (32 * w + 16 * (q >> 1) + i16) * 232;
        f2 z[9];
        UNR for (int i = 0; i < 9; ++i) z[i] = f2{P[i >> 1][2 * (i & 1)], P[i >> 1][2 * (i & 1) + 1]};
        float mx = fmaxf(z[0].x, z[0].y), mn = fminf(z[0].x, z[0].y);
        UNR for (int i = 1; i < 9; ++i) { mx = fmaxf(fmaxf(mx, z[i].x), z[i].y); mn = fminf(fminf(mn, z[i].x), z[i].y); }
        const float ma = -mx * LOG2E, mb = mn * LOG2E;
        f2 e1[9], e2[9], s1 = {0, 0}, s2 = {0, 0};
        UNR for (int i = 0; i < 9; ++i) {
          f2 t1 = z[i] * LOG2E + ma, t2 = mb - z[i] * LOG2E;
          e1[i] = f2{__builtin_amdgcn_exp2f(t1.x), __builtin_amdgcn_exp2f(t1.y)};
          e2[i] = f2{__builtin_amdgcn_exp2f(t2.x), __builtin_amdgcn_exp2f(t2.y)};
          s1 += e1[i]; s2 += e2[i];
        }
        const float r1 = __builtin_amdgcn_rcpf(s1.x + s1.y), r2 = __builtin_amdgcn_rcpf(s2.x + s2.y);
        lwait(CNT(C_GEM), 4 * (L + 1), dead);
        {
          u32 vo = row + 144 + (q & 1) * 40;
          UNR for (int i = 0; i < 4; ++i) st<u2>(vo + 8 * i, u2{V[2 * i], V[2 * i + 1]});
          st<u2>(vo + 32, u2{V[8], 0x00003F80u});
        }
        u32 fo = row + (q & 1) * 72;
        UNR for (int i = 0; i < 4; ++i) {
          st<u2>(fo + 8 * i, u2{pk(e1[2 * i] * r1), pk(e1[2 * i + 1] * r1)});
          st<u2>(fo + 32 + 8 * i, u2{pk(e2[2 * i] * r2), pk(e2[2 * i + 1] * r2)});
        }
        st<u2>(fo + 64, u2{pk(e1[8] * r1), pk(e2[8] * r2)});
        FENCE();
        ladd(CNT(C_PHI), lane);
        lwait(CNT(C_PHI), 4 * (L + 1), dead);
        STAMP(5 + L * 6);
        const int hd = w >> 1, db = w & 1;
        const u32 ta = L_STG + (16 * (q & 1) + 4 * (i16 >> 2) + (q >> 1)) * 232 + (i16 & 3) * 8;
        f4 Dd[3] = {f4{0, 0, 0, 0}, f4{0, 0, 0, 0}, f4{0, 0, 0, 0}};
        const u32 tf = ta + 72 * hd, tv = ta + 144 + 40 * hd + 32 * db;
        UNR for (int ks = 0; ks < 4; ++ks) {
          u4 bv = trd2(tv + ks * 7424, tv + ks * 7424 + 464);
          UNR for (int fb = 0; fb < 3; ++fb) Dd[fb] = mfma(trd2(tf + ks * 7424 + fb * 32, tf + ks * 7424 + fb * 32 + 464), bv, Dd[fb]);
        }
        auto s0 = SW16(pk(Dd[0].x, Dd[0].y), pk(Dd[1].x, Dd[1].y));
        auto s1b = SW16(pk(Dd[0].z, Dd[0].w), pk(Dd[1].z, Dd[1].w));
        u4 o = {s0[0], s1b[0], s0[1], s1b[1]};
        const int d = 16 * db + i16;
        const u32 so = L * SP_L + g * SP_G + p * SP_P;
        gst4(san(o), ws, d < 19 ? O_SP + (hd * 19 + d) * 64 + (q & 1) * 32 + (q >> 1) * 16 : 0x80000000u, so);
        gst2(u2{san(pk(Dd[2].x, Dd[2].y)), san(pk(Dd[2].z, Dd[2].w))}, ws, (d < 19 && q == 0) ? O_SP + 2432 + (hd * 19 + d) * 8 : 0x80000000u, so);
      }
      ladd(CNT(C_PUB), lane);
      STAMP(6 + L * 6);
      u4 wo[18];
      u32 fq[2][9];
      {
        if (wp < 10) wp = lwait(CNT(C_WPROG), 10, dead);
        if (L == 0) lwait(CNT(C_FAOK), 1, dead);
        f4 aq[3][2];
        UNR for (int i = 0; i < 3; ++i) aq[i][0] = aq[i][1] = f4{0, 0, 0, 0};
        u4 A[2][3];
        A[0][0] = ld<u4>(L_WQ + aF[0]); A[0][1] = ld<u4>(L_WQ + aF[0] + 2048); A[0][2] = ld<u4>(a7[0]);
        UNR for (int s = 0; s < 18; ++s) {
          if (s < 17) {
            const int j = (s + 1) >> 1, u = (s + 1) & 1;
            A[(s + 1) & 1][0] = ld<u4>(L_WQ + aF[u] + j * 4608); A[(s + 1) & 1][1] = ld<u4>(L_WQ + aF[u] + j * 4608 + 2048);
            A[(s + 1) & 1][2] = ld<u4>(a7[u] + j * 4608);
          }
          UNR for (int i = 0; i < 3; ++i) {
            aq[i][0] = mfma(A[s & 1][i], B[2 * s], aq[i][0]);
            aq[i][1] = mfma(A[s & 1][i], B[2 * s + 1], aq[i][1]);
          }
          if (s < 9) {
            wo[2 * s] = gld4(ws, O_WOT + lane * 16, (9 * c + w) * 18432 + 2 * s * 1024, 1);
            wo[2 * s + 1] = gld4(ws, O_WOT + lane * 16, (9 * c + w) * 18432 + (2 * s + 1) * 1024, 1);
          }
          __builtin_amdgcn_sched_barrier(0);
        }
        STAMP(7 + L * 6);
        const f4 c5 = ld<f4>(L_CSL + 5 * 64 + q * 16), c6 = ld<f4>(L_CSL + 6 * 64 + q * 16);
        const float c7 = ld<float>(L_CSL + 7 * 64 + q * 16);
        const bool val = (i16 >> 3) == (q >> 1);
        const u32 tq = L_TQ + (val ? (q * 4 + (i16 & 3)) * 16 : 256), tq2 = L_TQ2 + (val ? (q * 4 + (i16 & 3)) * 4 : 64);
        u4 q0[2], q1[2];
        UNR for (int tb = 0; tb < 2; ++tb) {
          q0[tb].x = pk((f2{aq[0][tb].x, aq[0][tb].y} - f2{c5.x, c5.y} * mS[tb]) * mQ[tb]);
          q0[tb].y = pk((f2{aq[0][tb].z, aq[0][tb].w} - f2{c5.z, c5.w} * mS[tb]) * mQ[tb]);
          q0[tb].z = pk((f2{aq[1][tb].x, aq[1][tb].y} - f2{c6.x, c6.y} * mS[tb]) * mQ[tb]);
          q0[tb].w = pk((f2{aq[1][tb].z, aq[1][tb].w} - f2{c6.z, c6.w} * mS[tb]) * mQ[tb]);
          q1[tb] = u4{pk((aq[2][tb].x - c7 * mS[tb]) * mQ[tb], 0.0f), 0, 0, 0};
        }
        f4 P[2][5];
        UNR for (int m = 0; m < 5; ++m) {
          u4 t0 = ld<u4>(tq + m * 272), t1 = {ld<u32>(tq2 + m * 68), 0, 0, 0};
          UNR for (int tb = 0; tb < 2; ++tb) P[tb][m] = mfma(t1, q1[tb], mfma(t0, q0[tb], f4{0, 0, 0, 0}));
        }
        const float ls = (q & 1) ? -LOG2E : LOG2E;
        UNR for (int tb = 0; tb < 2; ++tb) {
          f2 z[9];
          UNR for (int i = 0; i < 9; ++i) z[i] = f2{P[tb][i >> 1][2 * (i & 1)], P[tb][i >> 1][2 * (i & 1) + 1]} * ls;
          float mx = fmaxf(z[0].x, z[0].y);
          UNR for (int i = 1; i < 9; ++i) mx = fmaxf(fmaxf(mx, z[i].x), z[i].y);
          f2 s1 = {0, 0};
          UNR for (int i = 0; i < 9; ++i) { f2 t = z[i] - mx; z[i] = f2{__builtin_amdgcn_exp2f(t.x), __builtin_amdgcn_exp2f(t.y)}; s1 += z[i]; }
          const float r1 = __builtin_amdgcn_rcpf(s1.x + s1.y);
          UNR for (int i = 0; i < 9; ++i) fq[tb][i] = pk(z[i] * r1);
        }
      }
      STAMP(8 + L * 6);
      lwait(CNT(C_STB), 4 * (L + 1), dead);
      STAMP(9 + L * 6);
      {
        const bool val = (i16 >> 3) == (q >> 1);
        const int d01 = (q >> 1) * 19 + 8 * ((i16 >> 2) & 1) + (i16 & 3), d2 = (q >> 1) * 19 + 16 + ((i16 & 3) < 2 ? (i16 & 3) : 2);
        const u32 m01 = val ? L_STB + d01 * 64 + (q & 1) * 32 : L_ZERO, m2 = val ? L_STB + d2 * 64 + (q & 1) * 32 : L_ZERO;
        const u32 l01 = val ? L_STB + 2432 + d01 * 8 + (q & 1) * 4 : L_ZERO, l2 = val ? L_STB + 2432 + d2 * 8 + (q & 1) * 4 : L_ZERO;
        f4 O[2][3];
        UNR for (int m = 0; m < 3; ++m) {
          u4 t0 = ld<u4>(m < 2 ? m01 + m * 256 : m2), t1 = ld<u4>((m < 2 ? m01 + m * 256 : m2) + 16), t2 = {ld<u32>(m < 2 ? l01 + m * 32 : l2), 0, 0, 0};
          UNR for (int tb = 0; tb < 2; ++tb) {
            u4 b0 = {fq[tb][0], fq[tb][1], fq[tb][2], fq[tb][3]}, b1 = {fq[tb][4], fq[tb][5], fq[tb][6], fq[tb][7]}, b2 = {fq[tb][8], 0, 0, 0};
            O[tb][m] = mfma(t2, b2, mfma(t1, b1, mfma(t0, b0, f4{0, 0, 0, 0})));
          }
        }
        u32 lo[2];
        UNR for (int tb = 0; tb < 2; ++tb) {
          const float inv = __builtin_amdgcn_rcpf(O[tb][2].z + 1e-6f);
          u4 o = {pk(f2{O[tb][0].x, O[tb][0].y} * inv), pk(f2{O[tb][0].z, O[tb][0].w} * inv), pk(f2{O[tb][1].x, O[tb][1].y} * inv), pk(f2{O[tb][1].z, O[tb][1].w} * inv)};
          gst4(san(o), ws, O_OB + lane * 16, L * OB_L + p * OB_P + (2 * w + tb) * 16384 + g * 1024);
          lo[tb] = pk(f2{O[tb][2].x, O[tb][2].y} * inv);
        }
        const u32 lv = san((q & 1) ? lo[1] : lo[0]);
        auto hh = SW32(lv, lv);
        const u32 n0 = __builtin_amdgcn_update_dpp(0u, hh[0], 0xB1, 0xF, 0xF, true), n1 = __builtin_amdgcn_update_dpp(0u, hh[1], 0xB1, 0xF, 0xF, true);
        gst4(u4{hh[0], hh[1], n0, n1}, ws, (q < 2 && !(i16 & 1)) ? O_OB + 131072 + (16 * q + i16) * 8 : 0x80000000u, L * OB_L + p * OB_P + g * 1024 + w * 256);
        ladd(CNT(C_PUB), lane);
      }
      STAMP(22 + L);
      tail<false, L>(a, ws, b, w, lane, g, rt, c, rot, tag, wo, wo, dead STPASS);
    }
}

template <int L>
DEV void state_sum(const Args& a, rsrc_t ws, int b, int w, int lane0, int g, int& dead STARG) {
    const int lane = opq(lane0), q = lane >> 4, i16 = lane & 15;
    u4 sel;
    UNR for (int d = 0; d < 4; ++d) sel[d] = ((i16 & 7) >> 1) == d ? ((i16 & 1) ? 0x3F800000u : 0x00003F80u) : 0u;
      {
        const int h = w - 4;
        u4 v[3][4];
        u32 off[3];
        UNR for (int gi = 0; gi < 3; ++gi) off[gi] = O_SP + q * SP_P + umin(16 * umin(h + 4 * gi, 10) + i16, 170) * 16;
        lwait(CNT(C_PUB), 8 * L + 4, dead);
        NOUNR for (int it = dead ? SPIN_G - 1 : 0;; ++it) {
          FENCE();
          u32 m = 0;
          UNR for (int gi = 0; gi < 3; ++gi) UNR for (int pp = 0; pp < 4; ++pp) v[gi][pp] = gld4(ws, off[gi], L * SP_L + g * SP_G + 4 * pp * SP_P, 1);
          UNR for (int gi = 0; gi < 3; ++gi) UNR for (int pp = 0; pp < 4; ++pp) m = vm(m, v[gi][pp]);
          if (!anyl(m == SENT)) break;
          if (it >= SPIN_G) { dead = 1; break; }
        }
        FENCE();
        STAMP(6 + L * 6);
        UNR for (int gi = 0; gi < 3; ++gi) {
          f4 s = {0, 0, 0, 0};
          UNR for (int pp = 0; pp < 4; ++pp) s = mfma(sel, v[gi][pp], s);
          int ck = 16 * umin(h + 4 * gi, 10) + i16;
          if (q < 2 && ck < 171) st<u2>(L_STB + ck * 16 + q * 8, u2{pk(s.x, s.y), pk(s.z, s.w)});
        }
        FENCE();
        ladd(CNT(C_STB), lane);
        STAMP(7 + L * 6);
      }
}

__global__ void __launch_bounds__(512, 2) fused_kernel(Args a) {
  const int tid = threadIdx.x, lane = tid & 63, w = rfl(tid >> 6), b = blockIdx.x;
  const int q = lane >> 4, i16 = lane & 15;
  const int xcd = b & 7, r = b >> 3;
  const int p = 4 * (xcd >> 1) + (r >> 3), g = 8 * (xcd & 1) + (r & 7), rot = ((r & 7) + 2 * (r >> 3)) % 9;
  const int rt = 4 * p + (g >> 2), c = g & 3;
  rsrc_t ws = mkrs(a.ws, WS_BYTES);
  u32 tag = *(const u32*)a.ws + 1;
  int dead = 0;
  STAMP(0); STAMPC(29);

  if (w < 4) {
    __builtin_amdgcn_s_setprio(2);
    u4 B[36];
    float mS[2], mQ[2];
    {
      rsrc_t rx = mkrs(a.x, 2359296);
      u32 vo[2];
      UNR for (int par = 0; par < 2; ++par) vo[par] = (8 * par + (lane >> 3)) * 1152 + (((lane & 7) ^ (4 * par + (lane >> 4))) << 4);
      UNR for (int j = 0; j < 9; ++j) {
        int kr = j + rot; kr -= kr >= 9 ? 9 : 0;
        UNR for (int rg = 0; rg < 4; ++rg) dma<0>(rx, j * 16384 + w * 4096 + rg * 1024, vo[rg & 1], (128 * p + 32 * w + 16 * (rg >> 1)) * 1152 + kr * 128);
      }
      FENCE();
      __builtin_amdgcn_sched_barrier(0);
      asm volatile("s_waitcnt lgkmcnt(0)\n\ts_barrier" : "+s"(tag)::"memory");
      __builtin_amdgcn_sched_barrier(0);
      STAMP(1);
      u32 a0 = w * 4096 + (i16 >> 3) * 1024 + (i16 & 7) * 128 + ((q ^ (i16 >> 1)) << 4), a1 = a0 ^ 64, pa = CNT(C_PROG) + 4 * w;
      f4 gS[2] = {f4{0, 0, 0, 0}, f4{0, 0, 0, 0}}, gQ[2] = {f4{0, 0, 0, 0}, f4{0, 0, 0, 0}};
      const u4 ones = {0x3F803F80u, 0x3F803F80u, 0x3F803F80u, 0x3F803F80u};
#define X0STEP(j)                                                                                                                      \
  {                                                                                                                                    \
    u32 pv = j + 1;                                                                                                                    \
    asm volatile("s_waitcnt vmcnt(%8)\n\tds_read_b128 %0, %4\n\tds_read_b128 %1, %4 offset:2048\n\tds_read_b128 %2, %5\n\t"            \
                 "ds_read_b128 %3, %5 offset:2048\n\ts_waitcnt lgkmcnt(0)\n\tds_write_b32 %6, %7"                                       \
                 : "=&v"(B[4 * j]), "=&v"(B[4 * j + 1]), "=&v"(B[4 * j + 2]), "=&v"(B[4 * j + 3])                                        \
                 : "v"(a0 + j * 16384), "v"(a1 + j * 16384), "v"(pa), "v"(pv), "n"(32 - 4 * j)                                          \
                 : "memory");                                                                                                          \
    UNR for (int i = 0; i < 4; ++i) { gS[i & 1] = mfma(ones, B[4 * j + i], gS[i & 1]); gQ[i & 1] = mfma(B[4 * j + i], B[4 * j + i], gQ[i & 1]); } \
  }
      X0STEP(0) X0STEP(1) X0STEP(2) X0STEP(3) X0STEP(4) X0STEP(5) X0STEP(6) X0STEP(7) X0STEP(8)
      STAMP(2);
      UNR for (int tb = 0; tb < 2; ++tb) {
        float d = (i16 & 2) ? ((i16 & 1) ? gQ[tb].w : gQ[tb].z) : ((i16 & 1) ? gQ[tb].y : gQ[tb].x);
        d = (q == (i16 >> 2)) ? d : 0.0f;
        d = sw16add(d, d); d = sw32add(d, d);
        f2 t = ln_stats(gS[tb].x, d);
        mS[tb] = t.x; mQ[tb] = t.y;
      }
    }
    u32 wp = 0;
    main_layer<0>(a, ws, b, w, lane, p, g, rt, c, rot, tag, B, mS, mQ, wp, dead STPASS);
    main_layer<1>(a, ws, b, w, lane, p, g, rt, c, rot, tag, B, mS, mQ, wp, dead STPASS);
  } else {
    const int pr = (w - 4) >> 1, e = w & 1;
    const u32 lH = pr ? L_H1 : L_H0, lU = pr ? L_U1 : L_U0;
    if (w == 4) {
      const int cl = b % 27, kr0 = b / 27;
      if (b < 243) {
        rsrc_t rw = mkrs(a.wqkv, 576 * 3456);
        UNR for (int rg = 0; rg < 8; ++rg)
          dma<0>(rw, L_TILE + rg * 1024, ((lane >> 3) ^ (rg & 1)) * 3456 + (((lane & 7) ^ (4 * ((rg >> 1) & 1))) << 4), (64 * kr0 + 8 * rg) * 3456 + cl * 128);
      }
      FENCE();
      asm volatile("s_waitcnt lgkmcnt(0)\n\ts_barrier" : "+s"(tag)::"memory");
      if (b < 243) {
        const int cg = lane >> 3, rg = lane & 7, sec = cl / 9;
        u32 dst[8];
        UNR for (int j = 0; j < 8; ++j) {
          int cc = 64 * (cl - 9 * sec) + 8 * cg + j, h = cc / 18, d = cc - 18 * h, hl = h & 1;
          u32 o;
          if (sec == 0) {
            int hi = d >= 9, qq = 2 * hl + hi, s = d - 9 * hi;
            int row = s < 8 ? 16 * (s >> 2) + 4 * qq + (s & 3) : 32 + qq, key = s < 8 ? (row >> 1) & 7 : 0;
            o = L_WQ + kr0 * 4608 + row * 128 + ((rg ^ key) << 4);
          } else {
            int qq = 2 * (sec - 1) + hl;
            int row = d < 16 ? 16 * (d >> 2) + 4 * qq + (d & 3) : 48 + 2 * qq + d, key = d < 16 ? (row >> 1) & 7 : 2 * qq;
            o = kr0 * 9216 + row * 128 + ((rg ^ key) << 4);
          }
          dst[j] = O_WT + (h >> 1) * WT_G + o;
        }
        vmwait0();
        u4 R[8];
        const u32 rb = L_TILE + rg * 1024 + ((cg ^ (4 * ((rg >> 1) & 1))) << 4);
        UNR for (int i = 0; i < 8; ++i) R[i] = ld<u4>(rb + ((i ^ (rg & 1)) << 7));
        float cs[8];
        UNR for (int j = 0; j < 8; ++j) {
          u4 o;
          UNR for (int d = 0; d < 4; ++d) o[d] = __builtin_amdgcn_perm(R[2 * d + 1][j >> 1], R[2 * d][j >> 1], (j & 1) ? 0x07060302u : 0x05040100u);
          gst4(o, ws, dst[j], 0);
          cs[j] = dot2(o.x, 0x3F803F80u, dot2(o.y, 0x3F803F80u, 0.0f)) + dot2(o.z, 0x3F803F80u, dot2(o.w, 0x3F803F80u, 0.0f));
        }
        float sel = 0;
        UNR for (int j = 0; j < 8; ++j) {
          float v = cs[j];
          v += __uint_as_float(__builtin_amdgcn_update_dpp(0, __float_as_uint(v), 0xB1, 0xF, 0xF, true));
          v += __uint_as_float(__builtin_amdgcn_update_dpp(0, __float_as_uint(v), 0x4E, 0xF, 0xF, true));
          v += __uint_as_float(__builtin_amdgcn_update_dpp(0, __float_as_uint(v), 0x141, 0xF, 0xF, true));
          sel = rg == j ? v : sel;
        }
        gst1(__float_as_uint(sel), ws, O_CS + lane * 4, kr0 * 6912 + cl * 256);
        vmwait0();
        setflag(ws, O_FW + b * 128, tag, lane);
      }
      STAMP(1);
      {
        int l6 = lane < 54 ? lane : 0, kr = l6 / 6, i = l6 - 6 * kr;
        int ln = 9 * (i >> 1) + ((36 * g + 35 * (i & 1)) >> 6);
        poll(ws, O_FW + (kr * 27 + ln) * 128, tag, dead);
      }
      lset(CNT(C_WOK), 1);
      STAMP(2);
      u32 xp = 0;
      auto need_x = [&](u32 n) {
        if (xp < n) {
          NOUNR for (int i = dead ? SPIN_L : 0; i < SPIN_L; ++i) {
            u4 v;
            asm volatile("ds_read_b128 %0, %1\n\ts_waitcnt lgkmcnt(0)" : "=v"(v) : "v"(CNT(C_PROG)) : "memory");
            xp = rfl(umin(umin(v.x, v.y), umin(v.z, v.w)));
            if (xp >= n) return;
          }
          dead = 1;
        }
      };
      UNR for (int j = 0; j < 9; ++j) {
        need_x(((j + 1) * 9216 - 1) / 16384 + 1);
        int kr = j + rot; kr -= kr >= 9 ? 9 : 0;
        UNR for (int i = 0; i < 9; ++i) dma<SC1>(ws, j * 9216 + i * 1024, O_WT + lane * 16, g * WT_G + kr * 9216 + i * 1024);
        if (j >= 3) { vmwait<27>(); lset(CNT(C_WPROG), j - 2); }
      }
      need_x(8);
      UNR for (int i = 0; i < 41; ++i) {
        u32 o = i * 1024 + lane * 16 + rot * 4608;
        o -= o >= 41472 ? 41472 : 0;
        if (i < 40 || lane < 32) dma<SC1>(ws, L_WQ + i * 1024, O_WT + L_WQ + o, g * WT_G);
        if (i == 8) { vmwait<27>(); lset(CNT(C_WPROG), 7); }
        if (i == 17) { vmwait<27>(); lset(CNT(C_WPROG), 8); }
        if (i == 26) { vmwait<27>(); lset(CNT(C_WPROG), 9); }
      }
      vmwait0();
      lset(CNT(C_WPROG), 10);
      STAMP(3);
    } else if (w < 7) {
      KVin in0, in1;
      Hin hin;
      const int T = (w - 5) * 128 + lane, chn = 2 * b + (w - 5);
      const u32 mH = w == 5 ? L_H0 : L_H1, mU = w == 5 ? L_U0 : L_U1;
      kv_load(a, T, rt, c, in0);
      kv_load(a, T + 64, rt, c, in1);
      h_load(a, chn, lane, hin);
      FENCE();
      __builtin_amdgcn_sched_barrier(0);
      asm volatile("s_waitcnt lgkmcnt(0)\n\ts_barrier" : "+s"(tag)::"memory");
      __builtin_amdgcn_sched_barrier(0);
      kv_compute(ws, T, rt, c, in0);
      kv_compute(ws, T + 64, rt, c, in1);
      STAMP(1);
      vmwait0();
      if (ladd_rtn(CNT(C_KVC), lane) == 2) setflag(ws, O_FK + b * 128, tag, lane);
      UNR for (int i = 0; i < 4; ++i) lwait(CNT(C_PROG) + 4 * i, 9, dead);
      lwait(CNT(C_WOK), 1, dead);
      h_stage(mH, mU, lane, hin);
      poll(ws, O_FK + wg_of(lane >> 2, 4 * (lane & 3) + (2 * b) / 144) * 128, tag, dead);
      STAMP(2);
      u_stage(ws, mU, chn, lane);
      vmwait0();
      ladd(w == 5 ? CNT(C_PU0) : CNT(C_PU1), lane);
      STAMP(3);
    } else {
      if (lane < 32) lset(L_CNT + lane * 4, 0);
      const int cl = b % 9, gg = b / 9;
      if (b < 144) {
        rsrc_t rw = mkrs(a.wo, 576 * 1152);
        UNR for (int m = 0; m < 5; ++m)
          if (m < 4 || lane < 32) dma<0>(rw, L_WOTILE + m * 1024, (lane >> 3) * 1152 + (lane & 7) * 16, (36 * gg + 8 * m) * 1152 + cl * 128);
      }
      rsrc_t rp = mkrs(a.wphi, 32 * 648);
      u4 p0 = gld4(rp, lane * 16, g * 1296, 0), p1 = gld4(rp, lane < 17 ? 1024 + lane * 16 : 0x80000000u, g * 1296, 0);
      KVin in;
      if (lane < 32) kv_load(a, 256 + lane, rt, c, in);
      FENCE();
      __builtin_amdgcn_sched_barrier(0);
      asm volatile("s_waitcnt lgkmcnt(0)\n\ts_barrier" : "+s"(tag)::"memory");
      __builtin_amdgcn_sched_barrier(0);
      const u4 s4 = {SENT, SENT, SENT, SENT};
      UNR for (int i = 0; i < 3; ++i) gst4(s4, ws, (i < 2 || lane < 48) ? O_SP + i * 1024 + lane * 16 : 0x80000000u, g * SP_G + p * SP_P);
      UNR for (int i = 0; i < 9; ++i) gst4(s4, ws, O_OB + lane * 16, p * OB_P + i * 16384 + g * 1024);
      gst4(s4, ws, lane < 48 ? O_ST + q * 65536 + i16 * 16 : 0x80000000u, rt * 1024 + c * 256);
      if (lane < 32) kv_compute(ws, 256 + lane, rt, c, in);
      if (b < 144) {
        const u32 ta = L_WOTILE + (18 * (q >> 1) + 8 * (q & 1) + (i16 >> 2)) * 128 + (i16 & 3) * 8;
        UNR for (int G = 0; G < 4; ++G) gst4(trd2(ta + G * 32, ta + G * 32 + 512), ws, O_WOT + lane * 16, (4 * cl + G) * 18432 + gg * 1024);
        u2 lf = trd(L_WOTILE + (16 + ((i16 >> 2) & 1) + 18 * (i16 >> 3)) * 128 + q * 32 + (i16 & 3) * 8);
        gst2(lf, ws, O_WOT + q * 18432 + i16 * 16, 4 * cl * 18432 + (16 + (gg >> 3)) * 1024 + ((gg >> 1) & 3) * 256 + (gg & 1) * 8);
      }
      FENCE();
      asm volatile("s_waitcnt lgkmcnt(0)" ::: "memory");
      st<u4>(L_WOTILE + lane * 16, p0);
      if (lane < 17) st<u4>(L_WOTILE + 1024 + lane * 16, p1);
      FENCE();
      auto wp16 = [&](int hd, int d, int f) -> u32 { u32 v = ld<u16>(L_WOTILE + (hd * 324 + d * 18 + (f < 18 ? f : 0)) * 2); return f < 18 ? v : 0u; };
      UNR for (int rd = 0; rd < 2; ++rd) {
        int t = rd * 64 + lane, tt = t < 80 ? t : 79, m = tt >> 4, cc = (tt >> 3) & 1, hd = (tt >> 2) & 1, f = 4 * m + (tt & 3);
        u4 v;
        UNR for (int d = 0; d < 4; ++d) v[d] = wp16(hd, 8 * cc + 2 * d, f) | (wp16(hd, 8 * cc + 2 * d + 1, f) << 16);
        st<u4>(L_TK + (tt >> 3) * 144 + (tt & 7) * 16, v);
        int hq = (tt >> 3) & 1, dh = (tt >> 2) & 1;
        UNR for (int d = 0; d < 4; ++d) v[d] = wp16(hq, 9 * dh + 2 * d, f) | (wp16(hq, 9 * dh + 2 * d + 1, f) << 16);
        st<u4>(L_TQ + m * 272 + (tt & 15) * 16, v);
        st<u32>(L_TQ2 + m * 68 + (tt & 15) * 4, wp16(hq, 9 * dh + 8, f));
      }
      {
        int tt = lane < 40 ? lane : 39, m = tt >> 3, hd = (tt >> 2) & 1, f = 4 * m + (tt & 3);
        st<u32>(L_TK2 + m * 36 + (tt & 7) * 4, wp16(hd, 16, f) | (wp16(hd, 17, f) << 16));
        u4 z = {0, 0, 0, 0};
        if (lane < 10) st<u4>(L_TK + lane * 144 + 128, z);
        if (lane < 5) { st<u4>(L_TQ + lane * 272 + 256, z); st<u32>(L_TK2 + lane * 36 + 32, 0u); st<u32>(L_TQ2 + lane * 68 + 64, 0u); }
      }
      FENCE();
      vmwait0();
      setflag(ws, O_FA + b * 128, tag, lane);
      if (ladd_rtn(CNT(C_KVC), lane) == 2) setflag(ws, O_FK + b * 128, tag, lane);
      STAMP(1);
      lwait(CNT(C_WOK), 1, dead);
      UNR for (int rd = 0; rd < 2; ++rd) {
        int blk = rd * 4 + q, qq = i16 >> 2, ee = i16 & 3;
        int col;
        if (rd == 0) col = 576 + 576 * (qq >> 1) + 36 * g + 18 * (qq & 1) + 4 * blk + ee;
        else col = blk == 4 ? 576 + 576 * (qq >> 1) + 36 * g + 18 * (qq & 1) + 16 + (ee & 1) : 36 * g + 9 * qq + (blk == 7 ? 8 : 4 * (blk - 5) + ee);
        float s = 0;
        UNR for (int k = 0; k < 9; ++k) s += __uint_as_float(gld1(ws, O_CS + col * 4, k * 6912, 1));
        st<float>(L_CSL + rd * 256 + lane * 4, s);
      }
      if (lane < 18) st<u4>(L_ZERO + lane * 16, u4{0, 0, 0, 0});
      FENCE();
      ladd(CNT(C_TBL), lane);
      STAMP(2);
      {
        poll(ws, O_FA + wg_of(lane < 16 ? lane : p, lane < 16 ? g : (lane & 15)) * 128, tag, dead);
        int ln = (144 * c + 48 * q + (q == 3 ? -1 : 0)) >> 6;
        poll(ws, O_FA + (i16 * 9 + ln) * 128, tag, dead);
      }
      lset(CNT(C_FAOK), 1);
      lwait(CNT(C_WPROG), 10, dead);
      UNR for (int i = 0; i < 9; ++i) {
        u32 o = i * 1024 + lane * 16;
        o += o >= 4608 ? 18432 - 4608 : 0;
        gst4(s4, ws, O_X2 + o, 2 * rt * 18432 + c * 4608);
      }
      UNR for (int i = 0; i < 3; ++i) gst4(s4, ws, (i < 2 || lane < 48) ? O_SP + i * 1024 + lane * 16 : 0x80000000u, SP_L + g * SP_G + p * SP_P);
      UNR for (int i = 0; i < 9; ++i) gst4(s4, ws, O_OB + lane * 16, OB_L + p * OB_P + i * 16384 + g * 1024);
      STAMP(3);
    }
    u4 wo[18], wo8[18];
    lwait(CNT(C_WPROG), 10, dead);
    lwait(CNT(C_FAOK), 1, dead);
    UNR for (int i = 0; i < 18; ++i) wo[i] = gld4(ws, O_WOT + lane * 16, (9 * c + w) * 18432 + i * 1024, 1);
    lwait(pr ? CNT(C_PU1) : CNT(C_PU0), 1, dead);
    STAMP(4);
    conv_pair(a, ws, lH, lU, 2 * b + pr, e, pr ? CNT(C_PA1) : CNT(C_PA0), pr ? CNT(C_PB1) : CNT(C_PB0), 2, lane, dead);
    STAMP(5);
    vmwait0();
    if (w == 7) setflag(ws, O_FB + b * 128, tag, lane);
    if (ladd_rtn(CNT(C_FYC), lane) == 3) setflag(ws, O_FY + b * 128, tag, lane);
    state_sum<0>(a, ws, b, w, lane, g, dead STPASS);
      {
        if (c == 0 && w < 6) {
          const int ch = 512 + rt;
          lwait(CNT(C_FYC), 4, dead);
          if (w == 5) {
            Hin hin;
            h_load(a, ch, lane, hin);
            poll(ws, O_FK + wg_of(lane >> 2, 4 * (lane & 3) + 3) * 128, tag, dead);
            u_stage(ws, L_U0, ch, lane);
            h_stage(L_H0, L_U0, lane, hin);
            vmwait0();
            ladd(CNT(C_PU0), lane);
          }
          lwait(CNT(C_PU0), 2, dead);
          conv_pair(a, ws, L_H0, L_U0, ch, e, CNT(C_PA0), CNT(C_PB0), 4, lane, dead);
          vmwait0();
          if (ladd_rtn(CNT(C_FZC), lane) == 1) setflag(ws, O_FZ + (ch - 512) * 128, tag, lane);
        }
        if (w < 6) UNR for (int i = 0; i < 18; ++i) wo8[i] = gld4(ws, O_WOT + lane * 16, (9 * c + 8) * 18432 + i * 1024, 1);
        if (w == 7) poll(ws, O_FB + wg_of(lane < 16 ? lane : p, lane < 16 ? g : (lane & 15)) * 128, tag, dead);
        STAMP(8);
      }
    tail<true, 0>(a, ws, b, w, lane, g, rt, c, rot, tag, wo, wo8, dead STPASS);
    state_sum<1>(a, ws, b, w, lane, g, dead STPASS);
    tail<true, 1>(a, ws, b, w, lane, g, rt, c, rot, tag, wo, wo8, dead STPASS);
    if (b == 0 && w == 7) {
      UNR for (int i = 0; i < 4; ++i) poll(ws, O_FA + (i * 64 + lane) * 128, tag, dead);
      setflag(ws, 0, tag, lane);
    }
  }
  STAMP(31); STAMPC(30);
}

extern "C" void kernel_launch(void* const* d_in, const int* in_sizes, int n_in, void* d_out, int out_size, void* d_ws, size_t ws_size, hipStream_t stream) {
  Args a;
  a.x = (const u16*)d_in[0]; a.wqkv = (const u16*)d_in[1]; a.wphi = (const u16*)d_in[2]; a.wo = (const u16*)d_in[3];
  a.bk = (const u16*)d_in[5]; a.bv = (const u16*)d_in[6]; a.sk = (const u16*)d_in[7]; a.ssm = (const u16*)d_in[8]; a.dv = (const u16*)d_in[9];
  a.out = (u16*)d_out; a.ws = (unsigned char*)d_ws;
  (void)hipFuncSetAttribute((const void*)fused_kernel, hipFuncAttributeMaxDynamicSharedMemorySize, LDS_BYTES);
  fused_kernel<<<256, 512, LDS_BYTES, stream>>>(a);
}
```

```cpp
#define PADAT 0
#include <hip/hip_runtime.h>
#include <hip/hip_bf16.h>

typedef unsigned u32;
typedef unsigned short u16;
typedef float f2 __attribute__((ext_vector_type(2)));
typedef float f4 __attribute__((ext_vector_type(4)));
typedef u32 u2 __attribute__((ext_vector_type(2)));
typedef u32 u4 __attribute__((ext_vector_type(4)));
typedef u32 u2a __attribute__((ext_vector_type(2), aligned(4)));
typedef __bf16 bf2 __attribute__((ext_vector_type(2)));
typedef __bf16 bf8 __attribute__((ext_vector_type(8)));
typedef short s4v __attribute__((ext_vector_type(4)));
typedef __amdgpu_buffer_rsrc_t rsrc_t;
#define LAS __attribute__((address_space(3)))
#define DEV __device__ __forceinline__
#define UNR _Pragma("unroll")
#define NOUNR _Pragma("unroll 1")
#define FENCE() asm volatile("" ::: "memory")
#define SC1 16

constexpr u32 O_FW = 4096, O_FA = O_FW + 32768, O_FK = O_FA + 32768, O_FO = O_FK + 32768, O_FY = O_FO + 32768, O_FZ = O_FY + 32768, O_FB = O_FZ + 32768;
constexpr u32 O_WT = 262144, WT_G = 124416;
constexpr u32 O_CS = O_WT + 16 * WT_G;
constexpr u32 O_WOT = O_CS + 62464;
constexpr u32 O_KVT = O_WOT + 663552;
constexpr u32 O_YT = O_KVT + 2359296;
constexpr u32 O_SP = O_YT + 2359296, SP_L = 720896, SP_G = 45056, SP_P = 2816;
constexpr u32 O_OB = O_SP + 2 * SP_L, OB_L = 2359296, OB_P = 147456;
constexpr u32 O_ST = O_OB + 2 * OB_L;
constexpr u32 O_X2 = O_ST + 3 * 65536;
constexpr u32 O_STAMP = O_X2 + 2359296;
constexpr u32 WS_BYTES = O_STAMP + 256 * 8 * 32 * 8;
constexpr u32 L_WQ = 82944, L_STG = 46080;
constexpr u32 L_H0 = 124416, L_H1 = 132736, L_U0 = 141056, L_Y = 124416, L_XR = 133632;
constexpr u32 L_TILE = 147456, L_U1 = 147456, L_STB = 152064, L_ZERO = 154832, L_CSL = 155136;
constexpr u32 L_WOTILE = 155648, L_REDA = 155648, L_REDB = 157696;
constexpr u32 L_TK = 160256, L_TK2 = 161696, L_TQ = 161888, L_TQ2 = 163248, L_CNT = 163712, LDS_BYTES = 163840;
enum { C_PROG = 0, C_WPROG = 4, C_WOK, C_TBL, C_KVC, C_PHI, C_STB, C_PU0, C_PU1, C_PA0, C_PA1, C_PB0, C_PB1, C_REDA, C_REDB, C_FYC, C_FAOK, C_FZC, C_GEM, C_PUB };
#define CNT(i) (L_CNT + 4 * (i))
constexpr u32 SENT = 0xFFFFFFFFu;
constexpr int SPIN_G = 1 << 17, SPIN_L = 1 << 21;

#define MAP 1
__device__ __forceinline__ int wg_of(int p, int g) {
  return (p >> 2) * 2 + (g >> 3) + 8 * ((p & 3) * 8 + (g & 7));
}
struct Args {
  const u16 *x, *wqkv, *wphi, *wo, *bk, *bv, *sk, *ssm, *dv;
  u16* out;
  unsigned char* ws;
};

DEV int opq(int v) { asm volatile("" : "+v"(v)); return v; }
DEV u32 rfl(u32 v) { return __builtin_amdgcn_readfirstlane(v); }
DEV rsrc_t mkrs(const void* p, u32 n) { return __builtin_amdgcn_make_buffer_rsrc((void*)p, 0, n, 0x00020000); }
DEV u32 pk(float a, float b) { f2 v = {a, b}; return __builtin_bit_cast(u32, __builtin_convertvector(v, bf2)); }
DEV u32 pk(f2 v) { return __builtin_bit_cast(u32, __builtin_convertvector(v, bf2)); }
DEV f2 up(u32 p) { f2 r = {__uint_as_float(p << 16), __uint_as_float(p & 0xffff0000u)}; return r; }
DEV f2 rnd(f2 v) { return up(pk(v)); }
DEV float rnd1(float v) { return __uint_as_float(pk(v, v) & 0xffff0000u); }
DEV f4 mfma(u4 a, u4 b, f4 c) { return __builtin_amdgcn_mfma_f32_16x16x32_bf16(__builtin_bit_cast(bf8, a), __builtin_bit_cast(bf8, b), c, 0, 0, 0); }
DEV u32 umin(u32 a, u32 b) { return a < b ? a : b; }
DEV u32 umax(u32 a, u32 b) { return a > b ? a : b; }
DEV u32 umax3(u32 a, u32 b, u32 c) { return umax(umax(a, b), c); }
DEV u32 san(u32 v) { return umin(v, 0xFFFFFFFEu); }
DEV u4 san(u4 v) { u4 r = {san(v.x), san(v.y), san(v.z), san(v.w)}; return r; }
DEV u32 vm(u32 m, u4 v) { return umax3(umax3(m, v.x, v.y), v.z, v.w); }
DEV bool anyl(bool c) { return __builtin_amdgcn_ballot_w64(c) != 0; }
DEV float dot2(u32 a, u32 b, float c) { return __builtin_amdgcn_fdot2_f32_bf16(__builtin_bit_cast(bf2, a), __builtin_bit_cast(bf2, b), c, false); }
template <class T> DEV T ld(u32 o) { return *(LAS T*)(uintptr_t)o; }
template <class T> DEV void st(u32 o, T v) { *(LAS T*)(uintptr_t)o = v; }
DEV u2 trd(u32 o) { return __builtin_bit_cast(u2, __builtin_amdgcn_ds_read_tr16_b64_v4i16((LAS s4v*)(uintptr_t)o)); }
DEV u4 trd2(u32 o0, u32 o1) { u2 a = trd(o0), b = trd(o1); u4 r = {a.x, a.y, b.x, b.y}; return r; }
DEV u4 gld4(rsrc_t r, u32 v, u32 s, int aux) { return aux ? __builtin_amdgcn_raw_buffer_load_b128(r, v, s, SC1) : __builtin_amdgcn_raw_buffer_load_b128(r, v, s, 0); }
DEV u2 gld2(rsrc_t r, u32 v, u32 s, int aux) { return aux ? __builtin_amdgcn_raw_buffer_load_b64(r, v, s, SC1) : __builtin_amdgcn_raw_buffer_load_b64(r, v, s, 0); }
DEV u32 gld1(rsrc_t r, u32 v, u32 s, int aux) { return aux ? __builtin_amdgcn_raw_buffer_load_b32(r, v, s, SC1) : __builtin_amdgcn_raw_buffer_load_b32(r, v, s, 0); }
DEV void gst4(u4 d, rsrc_t r, u32 v, u32 s) { __builtin_amdgcn_raw_buffer_store_b128(d, r, v + s, 0, SC1); }
DEV void gst2(u2 d, rsrc_t r, u32 v, u32 s) { __builtin_amdgcn_raw_buffer_store_b64(d, r, v, s, SC1); }
DEV void gst1(u32 d, rsrc_t r, u32 v, u32 s) { __builtin_amdgcn_raw_buffer_store_b32(d, r, v, s, SC1); }
template <int AUX> DEV void dma(rsrc_t r, u32 l, u32 v, u32 s) {
#if defined(__HIP_DEVICE_COMPILE__)
  __builtin_amdgcn_raw_ptr_buffer_load_lds(r, (LAS void*)(uintptr_t)l, 16, v, s, 0, AUX);
#endif
}
DEV void vmwait0() { asm volatile("s_waitcnt vmcnt(0)" ::: "memory"); }
template <int N> DEV void vmwait() { asm volatile("s_waitcnt vmcnt(%0)" ::"n"(N) : "memory"); }
DEV void bar() { asm volatile("s_waitcnt lgkmcnt(0)\n\ts_barrier" ::: "memory"); }
DEV void lset(u32 a, u32 v) { asm volatile("ds_write_b32 %0, %1" ::"v"(a), "v"(v) : "memory"); }
DEV void ladd(u32 a, int lane) { if (lane == 0) { u32 one = 1; asm volatile("ds_add_u32 %0, %1" ::"v"(a), "v"(one) : "memory"); } }
DEV u32 ladd_rtn(u32 a, int lane) {
  u32 r = 0;
  if (lane == 0) { u32 one = 1; asm volatile("ds_add_rtn_u32 %0, %1, %2\n\ts_waitcnt lgkmcnt(0)" : "=v"(r) : "v"(a), "v"(one) : "memory"); }
  return rfl(r);
}
DEV u32 lget(u32 a) { u32 r; asm volatile("ds_read_b32 %0, %1\n\ts_waitcnt lgkmcnt(0)" : "=v"(r) : "v"(a) : "memory"); return rfl(r); }
DEV u32 lwait(u32 a, u32 t, int& dead) {
  u32 r = lget(a);
  if (__builtin_expect(r < t, 0)) {
    NOUNR for (int i = dead ? SPIN_L : 0; i < SPIN_L; ++i) { r = lget(a); if (r >= t) return r; __builtin_amdgcn_s_sleep(1); }
    dead = 1;
  }
  return r;
}
DEV void poll(rsrc_t ws, u32 off, u32 tag, int& dead) {
  NOUNR for (int i = dead ? SPIN_G : 0; i < SPIN_G; ++i) {
    FENCE();
    u32 v = gld1(ws, off, 0, 1);
    if (!anyl(v != tag)) { FENCE(); return; }
    __builtin_amdgcn_s_sleep(1);
  }
  dead = 1;
}
DEV void setflag(rsrc_t ws, u32 off, u32 tag, int lane) { if (lane == 0) gst1(tag, ws, off, 0); }
#define SW16(a, b) __builtin_amdgcn_permlane16_swap(a, b, false, false)
#define SW32(a, b) __builtin_amdgcn_permlane32_swap(a, b, false, false)
DEV float sw16add(float a, float b) { auto r = SW16(__float_as_uint(a), __float_as_uint(b)); return __uint_as_float(r[0]) + __uint_as_float(r[1]); }
DEV float sw32add(float a, float b) { auto r = SW32(__float_as_uint(a), __float_as_uint(b)); return __uint_as_float(r[0]) + __uint_as_float(r[1]); }

#define STAMP(i) do { if ((i) == PADAT && w < 4) { __builtin_amdgcn_sched_barrier(0); stv = (u32)__builtin_amdgcn_s_memrealtime(); __builtin_amdgcn_sched_barrier(0);} if ((i) == 31 && w < 4) { for (int z_ = 0; z_ < 100000; ++z_) { if ((u32)__builtin_amdgcn_s_memrealtime() - stv >= 6000u) break; __builtin_amdgcn_s_sleep(2); } } } while (0)
#define STAMPC(i)
#define STARG , u32& stv
#define STPASS , stv

DEV f2 ln_stats(float s, float sq) {
  float mean = s * (1.0f / 576.0f);
  float var = fmaxf(sq * (1.0f / 576.0f) - mean * mean, 0.0f);
  float sd = rnd1(__builtin_amdgcn_sqrtf(rnd1(rnd1(var) + 1e-5f)));
  f2 r = {rnd1(mean), __builtin_amdgcn_rcpf(sd)};
  return r;
}
DEV void stats_from(u4 v, float (&m)[2], float (&rs)[2]) {
  float s = __uint_as_float(v.x) + __uint_as_float(v.z), sq = __uint_as_float(v.y) + __uint_as_float(v.w);
  s = sw16add(s, s); sq = sw16add(sq, sq);
  f2 t = ln_stats(s, sq);
  auto r0 = SW32(__float_as_uint(t.x), __float_as_uint(t.x));
  auto r1 = SW32(__float_as_uint(t.y), __float_as_uint(t.y));
  m[0] = __uint_as_float(r0[0]); m[1] = __uint_as_float(r0[1]); rs[0] = __uint_as_float(r1[0]); rs[1] = __uint_as_float(r1[1]);
}
DEV u4 ld_stats(rsrc_t ws, u32 off) { u2 a = gld2(ws, off, 0, 1), b = gld2(ws, off + 256, 0, 1); return u4{a.x, a.y, b.x, b.y}; }
DEV u4 poll_stats(rsrc_t ws, u32 off, u4 v, int& dead) {
  if (anyl(vm(0, v) == SENT)) {
    NOUNR for (int i = dead ? SPIN_G : 0; i < SPIN_G; ++i) {
      FENCE();
      v = ld_stats(ws, off);
      if (!anyl(vm(0, v) == SENT)) { FENCE(); return v; }
    }
    dead = 1;
  }
  FENCE();
  return v;
}

DEV void pub_stats(rsrc_t ws, float s0, float s1, float q0, float q1, u32 red, u32 cnt, u32 last, u32 dst, int w, int lane) {
  float t = sw16add(sw32add(s0, s1), sw32add(q0, q1));
  st<float>(red + w * 256 + (16 * (lane >> 5) + (lane & 15)) * 8 + ((lane >> 4) & 1) * 4, t);
  FENCE();
  if (ladd_rtn(cnt, lane) == last) {
    f4 r = {0, 0, 0, 0};
    UNR for (int i = 0; i < 8; ++i) r += ld<f4>(red + i * 256 + (lane & 15) * 16);
    gst4(san(__builtin_bit_cast(u4, r)), ws, lane < 16 ? dst + lane * 16 : 0x80000000u, 0);
  }
}

struct KVin { u32 k[11], v[8]; u4 w; };
DEV void kv_load(const Args& a, int T, int rt, int c, KVin& in) {
  rsrc_t rk = mkrs(a.bk, 2359296), rv = mkrs(a.bv, 2359296), rw = mkrs(a.sk, 4608);
  int cp = T >> 2, sub = T & 3, ch = 144 * c + 2 * cp, s0 = 32 * rt + 8 * sub;
  u32 base = s0 * 1152 + ch * 2;
  UNR for (int i = 0; i < 3; ++i) in.k[i] = gld1(rk, s0 == 0 ? 0x80000000u : base - (3 - i) * 1152, 0, 0);
  UNR for (int i = 0; i < 8; ++i) in.k[3 + i] = gld1(rk, base, i * 1152, 0);
  UNR for (int i = 0; i < 8; ++i) in.v[i] = gld1(rv, base, i * 1152, 0);
  in.w = gld4(rw, ch * 8, 0, 0);
}
DEV void kv_compute(rsrc_t ws, int T, int rt, int c, const KVin& in) {
  int cp = T >> 2, sub = T & 3, ch = 144 * c + 2 * cp, s0 = 32 * rt + 8 * sub;
  f2 w0 = {__uint_as_float(in.w.x << 16), __uint_as_float(in.w.z << 16)}, w1 = {__uint_as_float(in.w.x & 0xffff0000u), __uint_as_float(in.w.z & 0xffff0000u)};
  f2 w2 = {__uint_as_float(in.w.y << 16), __uint_as_float(in.w.w << 16)}, w3 = {__uint_as_float(in.w.y & 0xffff0000u), __uint_as_float(in.w.w & 0xffff0000u)};
  f2 kf[11];
  UNR for (int i = 0; i < 11; ++i) kf[i] = up(in.k[i]);
  u32 P[8];
  UNR for (int s = 0; s < 8; ++s) {
    f2 t = rnd(kf[s] * w3);
    t = rnd(t + rnd(kf[s + 1] * w2));
    t = rnd(t + rnd(kf[s + 2] * w1));
    t = rnd(t + rnd(kf[s + 3] * w0));
    P[s] = pk(t * up(in.v[s]));
  }
  u4 o0, o1;
  UNR for (int d = 0; d < 4; ++d) {
    o0[d] = __builtin_amdgcn_perm(P[6 - 2 * d], P[7 - 2 * d], 0x05040100u);
    o1[d] = __builtin_amdgcn_perm(P[6 - 2 * d], P[7 - 2 * d], 0x07060302u);
  }
  u32 off = O_KVT + ch * 4096 + (2040 - s0) * 2;
  gst4(o0, ws, off, 0);
  gst4(o1, ws, off, 4096);
}

struct Hin { u4 h[4]; u32 n[4]; };
DEV void h_load(const Args& a, int ch, int lane, Hin& in) {
  rsrc_t rh = mkrs(a.ssm, 2359296);
  UNR for (int m = 0; m < 4; ++m) { in.h[m] = gld4(rh, ch * 4096 + lane * 16, m * 1024, 0); in.n[m] = gld1(rh, ch * 4096 + lane * 16 + 16, m * 1024, 0); }
}
DEV void h_stage(u32 lH, u32 lU, int lane, const Hin& in) {
  UNR for (int m = 0; m < 4; ++m) {
    u4 h = in.h[m];
    u4 s = {__builtin_amdgcn_alignbit(h.y, h.x, 16), __builtin_amdgcn_alignbit(h.z, h.y, 16), __builtin_amdgcn_alignbit(h.w, h.z, 16), __builtin_amdgcn_alignbit(in.n[m], h.w, 16)};
    st<u4>(lH + 32 + m * 1024 + lane * 16, h);
    st<u4>(lH + 4160 + 32 + m * 1024 + lane * 16, s);
  }
  u32 h0 = rfl(in.h[0].x) << 16;
  u4 z = {0, 0, 0, lane == 3 ? h0 : 0u};
  if (lane < 4) st<u4>(lH + (lane >> 1) * 4160 + (lane & 1) * 16, z);
  u4 zz = {0, 0, 0, 0};
  if (lane < 30) st<u4>(lU + 4096 + lane * 16, zz);
}
DEV void u_stage(rsrc_t ws, u32 lU, int ch, int lane) {
  UNR for (int m = 0; m < 4; ++m) dma<SC1>(ws, lU + m * 1024, O_KVT + ch * 4096 + lane * 16, m * 1024);
}
DEV void conv_pair(const Args& a, rsrc_t ws, u32 lH, u32 lU, int ch, int e, u32 cA, u32 cB, u32 tgt, int lane, int& dead) {
  const int q = lane >> 4, i16 = lane & 15;
  f4 acc[8];
  UNR for (int i = 0; i < 8; ++i) acc[i] = f4{0, 0, 0, 0};
  const u32 aB = lU + 2 * (2032 - 16 * i16 + 8 * q) - 3584 + 256 * e;
  const u32 aA = lH + ((i16 & 1) ? 0 : 4160) + 4 * (((i16 + 1) >> 1) + 4 * q) + 256 * e;
  const u32 aA4[4] = {aA, aA + 1024, aA + 2048, aA + 3072};
  float dv = __uint_as_float((u32)a.dv[ch] << 16);
  u4 B[8];
  u2 Al[8], Ah[8];
#define CLD(t, bb)                                                                                                              \
  asm volatile("ds_read2_b32 %0, %3 offset0:%5 offset1:%6\n\tds_read2_b32 %1, %3 offset0:%7 offset1:%8\n\tds_read_b128 %2, %4 offset:%9" \
               : "=&v"(Al[t]), "=&v"(Ah[t]), "=&v"(B[7 - (t)])                                                                    \
               : "v"(aA4[(t) >> 1]), "v"(aB), "n"(16 * (bb) + 128 * ((t) & 1)), "n"(16 * (bb) + 128 * ((t) & 1) + 1),              \
                 "n"(16 * (bb) + 128 * ((t) & 1) + 2), "n"(16 * (bb) + 128 * ((t) & 1) + 3), "n"(512 * (t) + 64 * (bb))           \
               : "memory")
  UNR for (int t = 0; t < 8; ++t) CLD(t, 0);
  UNR for (int bb = 0; bb < 4; ++bb) {
    asm volatile("s_waitcnt lgkmcnt(6)" : "+v"(Al[0]), "+v"(Ah[0]), "+v"(Al[1]), "+v"(Ah[1]), "+v"(Al[2]), "+v"(Ah[2]), "+v"(Al[3]), "+v"(Ah[3]), "+v"(Al[4]),
                 "+v"(Ah[4]), "+v"(Al[5]), "+v"(Ah[5]), "+v"(B[7]), "+v"(B[6]), "+v"(B[5]), "+v"(B[4]), "+v"(B[3]), "+v"(B[2]));
    UNR for (int t = 0; t < 8; ++t) {
      u4 A = {Al[t].x, Al[t].y, Ah[t].x, Ah[t].y};
      UNR for (int v = 7 - t; v >= 0; --v) {
        if (t == 0 && v == 1) {
          __builtin_amdgcn_sched_barrier(0);
          asm volatile("s_waitcnt lgkmcnt(0)" : "+v"(Al[6]), "+v"(Ah[6]), "+v"(Al[7]), "+v"(Ah[7]), "+v"(B[1]), "+v"(B[0]));
        }
        acc[t + v] = mfma(A, B[v], acc[t + v]);
      }
      __builtin_amdgcn_sched_barrier(0);
      if (bb < 3) CLD(t, bb + 1);
    }
  }
  FENCE();
  ladd(cA, lane);
  lwait(cA, tgt, dead);
  f4 keep[4];
  UNR for (int i = 0; i < 4; ++i) {
    keep[i] = e ? acc[4 + i] : acc[i];
    f4 snd = e ? acc[i] : acc[4 + i];
    st<f4>(lH + e * 4096 + i * 1024 + lane * 16, snd);
  }
  FENCE();
  ladd(cB, lane);
  lwait(cB, tgt, dead);
  u2 Y[4];
  UNR for (int i = 0; i < 4; ++i) {
    f4 s = keep[i] + ld<f4>(lH + (1 - e) * 4096 + i * 1024 + lane * 16);
    u2 uu = ld<u2>(lU + 2 * (2044 - 256 * (4 * e + i) - 16 * i16 - 4 * q));
    f2 u32_ = up(uu.x), u10 = up(uu.y);
    f2 g01 = {u10.y * dv, u10.x * dv}, g23 = {u32_.y * dv, u32_.x * dv};
    f2 s01 = {s.x, s.y}, s23 = {s.z, s.w};
    Y[i].x = pk(rnd(s01) + rnd(g01));
    Y[i].y = pk(rnd(s23) + rnd(g23));
  }
  UNR for (int i = 0; i < 2; ++i) {
    auto r0 = SW16(Y[2 * i].x, Y[2 * i + 1].x);
    auto r1 = SW16(Y[2 * i].y, Y[2 * i + 1].y);
    u4 o = {r0[0], r1[0], r0[1], r1[1]};
    gst4(o, ws, O_YT + ch * 4096 + 2 * (256 * (4 * e + 2 * i + (q & 1)) + 16 * i16 + 8 * (q >> 1)), 0);
  }
}

template <bool HELP, int L>
DEV void tail(const Args& a, rsrc_t ws, int b, int w, int lane0, int g, int rt, int c, int rot, u32 tag, const u4 (&wo)[18], const u4 (&wo8)[18], int& dead STARG) {
  const int lane = opq(lane0), q = lane >> 4, i16 = lane & 15;
  const bool ex = HELP && w < 6;
  const int tb8 = w & 1;
  u2 xr[3];
  if (L == 0) {
    rsrc_t rx = mkrs(a.x, 2359296);
    u32 o = (32 * rt + i16) * 1152 + (144 * c + 4 * q) * 2;
    xr[0] = gld2(rx, o, w * 32, 0); xr[1] = gld2(rx, o, w * 32 + 16 * 1152, 0);
    xr[2] = gld2(rx, o, 256 + tb8 * 16 * 1152, 0);
  } else {
    xr[0] = ld<u2>(L_XR + w * 1024 + lane * 8); xr[1] = ld<u2>(L_XR + w * 1024 + 512 + lane * 8);
    xr[2] = ld<u2>(L_XR + 8192 + tb8 * 512 + lane * 8);
  }
  {
    const u32 pb = L * OB_L + (rt >> 2) * OB_P, tbp = 2 * (rt & 3);
    u4 v[5];
    if (HELP) lwait(CNT(C_PUB), 8 * L + 8, dead);
    NOUNR for (int it = dead ? SPIN_G - 1 : 0;; ++it) {
      FENCE();
      UNR for (int i = 0; i < 4; ++i) v[i] = gld4(ws, O_OB + lane * 16, pb + (tbp + (i >> 1)) * 16384 + (w + 8 * (i & 1)) * 1024, 1);
      if (!HELP) {
        u32 o = O_OB + 131072 + q * 2048 + i16 * 8, so = pb + (w & 1) * 8192 + (tbp + (w >> 1)) * 128;
        u2 l0 = gld2(ws, o, so, 1), l1 = gld2(ws, o + 1024, so, 1);
        v[4] = u4{l0.x, l0.y, l1.x, l1.y};
      }
      u32 m = 0;
      UNR for (int i = 0; i < (HELP ? 4 : 5); ++i) m = vm(m, v[i]);
      if (!anyl(m == SENT)) break;
      if (it >= SPIN_G) { dead = 1; break; }
    }
    FENCE();
    UNR for (int i = 0; i < 4; ++i) st<u4>(L_STG + (i >> 1) * 18432 + (w + 8 * (i & 1)) * 1024 + lane * 16, v[i]);
    if (!HELP) st<u4>(L_STG + (w >> 1) * 18432 + (16 + (w & 1)) * 1024 + lane * 16, v[4]);
  }
  STAMP(16 + L * 8);
  bar();
  STAMP(17 + L * 8);
  u32 fyoff = 0, fy = tag;
  const int chl = 144 * c + ((lane & 16) ? 128 : 16 * w) + i16;
  if (L == 0) { fyoff = chl < 512 ? O_FY + (chl >> 1) * 128 : O_FZ + (chl - 512) * 128; fy = gld1(ws, fyoff, 0, 1); }
  f4 acc[3] = {f4{0, 0, 0, 0}, f4{0, 0, 0, 0}, f4{0, 0, 0, 0}};
  if (ex) {
    UNR for (int ks = 0; ks < 18; ++ks) {
      u4 b0 = ld<u4>(L_STG + ks * 1024 + lane * 16), b1 = ld<u4>(L_STG + 18432 + ks * 1024 + lane * 16);
      u4 b2 = ld<u4>(L_STG + tb8 * 18432 + ks * 1024 + lane * 16);
      acc[0] = mfma(wo[ks], b0, acc[0]); acc[1] = mfma(wo[ks], b1, acc[1]); acc[2] = mfma(wo8[ks], b2, acc[2]);
    }
  } else {
    UNR for (int ks = 0; ks < 18; ++ks) {
      u4 b0 = ld<u4>(L_STG + ks * 1024 + lane * 16), b1 = ld<u4>(L_STG + 18432 + ks * 1024 + lane * 16);
      acc[0] = mfma(wo[ks], b0, acc[0]); acc[1] = mfma(wo[ks], b1, acc[1]);
    }
  }
  u4 yv, yv8;
  if (L == 0) {
    if (anyl(fy != tag)) poll(ws, fyoff, tag, dead);
    FENCE();
    yv = gld4(ws, O_YT + (lane >> 2) * 4096 + (lane & 3) * 16, (144 * c + 16 * w) * 4096 + rt * 64, 1);
    yv8 = gld4(ws, O_YT + (lane >> 2) * 4096 + (lane & 3) * 16, (144 * c + 128) * 4096 + rt * 64, 1);
  }
  constexpr int NU = HELP ? 3 : 2;
  f2 x1[NU][2];
  float S[NU], Q[NU];
  UNR for (int u = 0; u < NU; ++u) {
    f2 lo = {acc[u].x, acc[u].y}, hi = {acc[u].z, acc[u].w};
    x1[u][0] = rnd(rnd(lo) + up(xr[u].x)); x1[u][1] = rnd(rnd(hi) + up(xr[u].y));
    f2 s = x1[u][0] + x1[u][1], sq = x1[u][0] * x1[u][0] + x1[u][1] * x1[u][1];
    S[u] = s.x + s.y; Q[u] = sq.x + sq.y;
  }
  if (HELP) { if (w == 4) { S[0] += S[2]; Q[0] += Q[2]; } if (w == 5) { S[1] += S[2]; Q[1] += Q[2]; } }
  const u32 stoff = O_ST + L * 2 * 65536 + rt * 1024 + (q & 1) * 512 + (16 * (q >> 1) + i16) * 8;
  pub_stats(ws, S[0], S[1], Q[0], Q[1], L_REDA, CNT(C_REDA), 8 * L + 7, O_ST + L * 2 * 65536 + rt * 1024 + c * 256, w, lane);
  STAMP(18 + L * 8);
  if (HELP && L == 0 && w == 4) {
    lwait(CNT(C_REDA), 8, dead);
    UNR for (int j = 5; j < 9; ++j) {
      int kr = j + rot; kr -= kr >= 9 ? 9 : 0;
      UNR for (int i = 0; i < 9; ++i) dma<SC1>(ws, j * 9216 + i * 1024, O_WT + lane * 16, g * WT_G + kr * 9216 + i * 1024);
    }
  }
  u4 sv = ld_stats(ws, stoff);
  if (L == 0) {
    st<u4>(L_Y + w * 1024 + lane * 16, yv);
    if (ex) st<u4>(L_Y + 8192 + lane * 16, yv8);
    FENCE();
  }
  u2 y[NU];
  {
    u32 ya = L_Y + (4 * q + (i16 >> 2)) * 64 + (i16 & 3) * 8;
    y[0] = trd(ya + w * 1024); y[1] = trd(ya + w * 1024 + 32);
    if (HELP) y[2] = trd(ya + 8192 + tb8 * 32);
  }
  sv = poll_stats(ws, stoff, sv, dead);
  STAMP(19 + L * 8);
  if (HELP && L == 0 && w == 4) { vmwait0(); lset(CNT(C_WPROG), 11); }
  float m[2], rs[2];
  stats_from(sv, m, rs);
  u2 x2[NU];
  UNR for (int u = 0; u < NU; ++u) {
    float mu = u < 2 ? m[u] : (tb8 ? m[1] : m[0]), ru = u < 2 ? rs[u] : (tb8 ? rs[1] : rs[0]);
    UNR for (int h = 0; h < 2; ++h) {
      f2 xn = rnd(rnd(x1[u][h] - mu) * ru);
      x2[u][h] = pk(x1[u][h] + rnd(xn * up(y[u][h])));
    }
  }
  auto r0 = SW16(x2[0].x, x2[1].x);
  auto r1 = SW16(x2[0].y, x2[1].y);
  u4 o = {r0[0], r1[0], r0[1], r1[1]};
  if (L == 0) {
    gst4(san(o), ws, O_X2 + (q & 1) * 18432 + (q >> 1) * 256 + i16 * 16, 2 * rt * 18432 + (9 * c + w) * 512);
    if (ex) {
      auto t0 = SW16(x2[NU - 1].x, x2[NU - 1].x);
      auto t1 = SW16(x2[NU - 1].y, x2[NU - 1].y);
      gst4(san(u4{t0[0], t1[0], t0[1], t1[1]}), ws, (q & 1) ? 0x80000000u : O_X2 + (q >> 1) * 256 + i16 * 16, (2 * rt + tb8) * 18432 + (9 * c + 8) * 512);
    }
    st<u2>(L_XR + w * 1024 + lane * 8, x2[0]); st<u2>(L_XR + w * 1024 + 512 + lane * 8, x2[1]);
    if (ex) st<u2>(L_XR + 8192 + tb8 * 512 + lane * 8, x2[NU - 1]);
    UNR for (int u = 0; u < NU; ++u) {
      f2 lo = up(x2[u].x), hi = up(x2[u].y);
      f2 s = lo + hi, sq = lo * lo + hi * hi;
      S[u] = s.x + s.y; Q[u] = sq.x + sq.y;
    }
    if (HELP) { if (w == 4) { S[0] += S[2]; Q[0] += Q[2]; } if (w == 5) { S[1] += S[2]; Q[1] += Q[2]; } }
    pub_stats(ws, S[0], S[1], Q[0], Q[1], L_REDB, CNT(C_REDB), 7, O_ST + 65536 + rt * 1024 + c * 256, w, lane);
  } else {
    rsrc_t ro = mkrs(a.out, 2359296);
    __builtin_amdgcn_raw_buffer_store_b128(o, ro, (16 * (q & 1) + i16) * 1152 + (q >> 1) * 16 + 32 * rt * 1152 + 288 * c + 32 * w, 0, 0);
    if (ex) __builtin_amdgcn_raw_buffer_store_b64(x2[NU - 1], ro, i16 * 1152 + q * 8, (32 * rt + 16 * tb8) * 1152 + 288 * c + 256, 0);
  }
  STAMP(20 + L * 8);
}

template <int L>
DEV void main_layer(const Args& a, rsrc_t ws, int b, int w, int lane0, int p, int g, int rt, int c, int rot, u32 tag, u4 (&B)[36], float (&mS)[2], float (&mQ)[2], u32& wp, int& dead STARG) {
    constexpr float LOG2E = 1.4426950408889634f;
    int lane = opq(lane0), q = lane >> 4, i16 = lane & 15;
    u32 aF[2], a4[2], a7[2];
    UNR for (int u = 0; u < 2; ++u) {
      aF[u] = i16 * 128 + (((4 * u + q) ^ (i16 >> 1)) << 4);
      a4[u] = (64 + 2 * (i16 >> 2) + (i16 & 1)) * 128 + (((4 * u + q) ^ (2 * (i16 >> 2))) << 4);
      a7[u] = L_WQ + (32 + (i16 >> 2)) * 128 + ((4 * u + q) << 4);
    }
    {
      f4 acc[5][2];
      u4 sv = {0, 0, 0, 0};
      const u32 stoff = O_ST + 65536 + (4 * p + w) * 1024 + (q & 1) * 512 + (16 * (q >> 1) + i16) * 8;
      if (L == 1) {
        u32 G = lane < 36 ? lane : (lane < 40 ? 9 * (lane - 36) + 8 : 0);
        u32 po = O_X2 + (8 * p + 2 * w + (lane >= 36 && lane < 40 ? 1 : 0)) * 18432 + G * 512;
        NOUNR for (int i = dead ? SPIN_G : 0; i < SPIN_G; ++i) {
          FENCE();
          u32 v = gld1(ws, po, 0, 1);
          if (!anyl(v == SENT)) break;
        }
        FENCE();
        STAMP(21);
      }
      for (int pass = 0;; ++pass) {
        if (L == 1) {
          UNR for (int j = 0; j < 9; ++j) {
            int kr = j + rot; kr -= kr >= 9 ? 9 : 0;
            UNR for (int i = 0; i < 4; ++i) B[4 * j + i] = gld4(ws, O_X2 + lane * 16 + (i >> 1) * 1024, (8 * p + 2 * w + (i & 1)) * 18432 + kr * 2048, 1);
          }
          sv = ld_stats(ws, stoff);
        }
        UNR for (int i = 0; i < 5; ++i) acc[i][0] = acc[i][1] = f4{0, 0, 0, 0};
        u32 vmx = 0;
        if (wp < (L ? 0u : 1u)) wp = lwait(CNT(C_WPROG), 1, dead);
        if (L == 0) STAMP(3);
        u4 A[2][5];
        UNR for (int i = 0; i < 4; ++i) A[0][i] = ld<u4>(aF[0] + i * 2048);
        A[0][4] = ld<u4>(a4[0]);
        u32 fl = *(volatile LAS u32*)(uintptr_t)CNT(C_WPROG);
        UNR for (int s = 0; s < 18; ++s) {
          if (s < 17) {
            const int j = (s + 1) >> 1, u = (s + 1) & 1;
            if (u == 0) {
              u32 need = L ? (j < 5 ? 0u : 11u) : (u32)(j + 1);
              wp = umax(wp, rfl(fl));
              if (wp < need) wp = lwait(CNT(C_WPROG), need, dead);
            }
            UNR for (int i = 0; i < 4; ++i) A[(s + 1) & 1][i] = ld<u4>(aF[u] + j * 9216 + i * 2048);
            A[(s + 1) & 1][4] = ld<u4>(a4[u] + j * 9216);
            if (u == 0) fl = *(volatile LAS u32*)(uintptr_t)CNT(C_WPROG);
          }
          vmx = vm(vm(vmx, B[2 * s]), B[2 * s + 1]);
          UNR for (int i = 0; i < 5; ++i) {
            acc[i][0] = mfma(A[s & 1][i], B[2 * s], acc[i][0]);
            acc[i][1] = mfma(A[s & 1][i], B[2 * s + 1], acc[i][1]);
          }
        }
        if (L == 0 || !anyl(vmx == SENT) || dead) break;
        if (pass >= 20000) { dead = 1; break; }
        FENCE();
      }
      FENCE();
      ladd(CNT(C_GEM), lane);
      STAMP(4 + L * 6);
      if (L == 0) lwait(CNT(C_TBL), 1, dead);
      else { sv = poll_stats(ws, stoff, sv, dead); stats_from(sv, mS, mQ); }
      {
        u32 kv[2][9];
        UNR for (int i = 0; i < 5; ++i) {
          f4 cs = ld<f4>(L_CSL + i * 64 + q * 16);
          f2 c01 = {cs.x, cs.y}, c23 = {cs.z, cs.w};
          UNR for (int tb = 0; tb < 2; ++tb) {
            f2 lo = {acc[i][tb].x, acc[i][tb].y}, hi = {acc[i][tb].z, acc[i][tb].w};
            kv[tb][i < 4 ? 2 * i : 8] = pk((lo - c01 * mS[tb]) * mQ[tb]);
            if (i < 4) kv[tb][2 * i + 1] = pk((hi - c23 * mS[tb]) * mQ[tb]);
          }
        }
        u32 K[9], V[9];
        UNR for (int i = 0; i < 9; ++i) { auto t = SW32(kv[0][i], kv[1][i]); K[i] = t[0]; V[i] = t[1]; }
        const bool val = (i16 >> 2) == q;
        const u32 tk = L_TK + (val ? ((q & 1) * 4 + (i16 & 3)) * 16 : 128), tk2 = L_TK2 + (val ? ((q & 1) * 4 + (i16 & 3)) * 4 : 32);
        const u4 k0 = {K[0], K[1], K[2], K[3]}, k1 = {K[4], K[5], K[6], K[7]}, k2 = {K[8], 0, 0, 0};
        f4 P[5];
        UNR for (int m = 0; m < 5; ++m) {
          u4 t2 = {ld<u32>(tk2 + m * 36), 0, 0, 0};
          P[m] = mfma(ld<u4>(tk + m * 288), k0, f4{0, 0, 0, 0});
          P[m] = mfma(ld<u4>(tk + m * 288 + 144), k1, P[m]);
          P[m] = mfma(t2, k2, P[m]);
        }
        const u32 row = L_STG + (32 * w + 16 * (q >> 1) + i16) * 232;
        f2 z[9];
        UNR for (int i = 0; i < 9; ++i) z[i] = f2{P[i >> 1][2 * (i & 1)], P[i >> 1][2 * (i & 1) + 1]};
        float mx = fmaxf(z[0].x, z[0].y), mn = fminf(z[0].x, z[0].y);
        UNR for (int i = 1; i < 9; ++i) { mx = fmaxf(fmaxf(mx, z[i].x), z[i].y); mn = fminf(fminf(mn, z[i].x), z[i].y); }
        const float ma = -mx * LOG2E, mb = mn * LOG2E;
        f2 e1[9], e2[9], s1 = {0, 0}, s2 = {0, 0};
        UNR for (int i = 0; i < 9; ++i) {
          f2 t1 = z[i] * LOG2E + ma, t2 = mb - z[i] * LOG2E;
          e1[i] = f2{__builtin_amdgcn_exp2f(t1.x), __builtin_amdgcn_exp2f(t1.y)};
          e2[i] = f2{__builtin_amdgcn_exp2f(t2.x), __builtin_amdgcn_exp2f(t2.y)};
          s1 += e1[i]; s2 += e2[i];
        }
        const float r1 = __builtin_amdgcn_rcpf(s1.x + s1.y), r2 = __builtin_amdgcn_rcpf(s2.x + s2.y);
        lwait(CNT(C_GEM), 4 * (L + 1), dead);
        {
          u32 vo = row + 144 + (q & 1) * 40;
          UNR for (int i = 0; i < 4; ++i) st<u2>(vo + 8 * i, u2{V[2 * i], V[2 * i + 1]});
          st<u2>(vo + 32, u2{V[8], 0x00003F80u});
        }
        u32 fo = row + (q & 1) * 72;
        UNR for (int i = 0; i < 4; ++i) {
          st<u2>(fo + 8 * i, u2{pk(e1[2 * i] * r1), pk(e1[2 * i + 1] * r1)});
          st<u2>(fo + 32 + 8 * i, u2{pk(e2[2 * i] * r2), pk(e2[2 * i + 1] * r2)});
        }
        st<u2>(fo + 64, u2{pk(e1[8] * r1), pk(e2[8] * r2)});
        FENCE();
        ladd(CNT(C_PHI), lane);
        lwait(CNT(C_PHI), 4 * (L + 1), dead);
        STAMP(5 + L * 6);
        const int hd = w >> 1, db = w & 1;
        const u32 ta = L_STG + (16 * (q & 1) + 4 * (i16 >> 2) + (q >> 1)) * 232 + (i16 & 3) * 8;
        f4 Dd[3] = {f4{0, 0, 0, 0}, f4{0, 0, 0, 0}, f4{0, 0, 0, 0}};
        const u32 tf = ta + 72 * hd, tv = ta + 144 + 40 * hd + 32 * db;
        UNR for (int ks = 0; ks < 4; ++ks) {
          u4 bv = trd2(tv + ks * 7424, tv + ks * 7424 + 464);
          UNR for (int fb = 0; fb < 3; ++fb) Dd[fb] = mfma(trd2(tf + ks * 7424 + fb * 32, tf + ks * 7424 + fb * 32 + 464), bv, Dd[fb]);
        }
        auto s0 = SW16(pk(Dd[0].x, Dd[0].y), pk(Dd[1].x, Dd[1].y));
        auto s1b = SW16(pk(Dd[0].z, Dd[0].w), pk(Dd[1].z, Dd[1].w));
        u4 o = {s0[0], s1b[0], s0[1], s1b[1]};
        const int d = 16 * db + i16;
        const u32 so = L * SP_L + g * SP_G + p * SP_P;
        gst4(san(o), ws, d < 19 ? O_SP + (hd * 19 + d) * 64 + (q & 1) * 32 + (q >> 1) * 16 : 0x80000000u, so);
        gst2(u2{san(pk(Dd[2].x, Dd[2].y)), san(pk(Dd[2].z, Dd[2].w))}, ws, (d < 19 && q == 0) ? O_SP + 2432 + (hd * 19 + d) * 8 : 0x80000000u, so);
      }
      ladd(CNT(C_PUB), lane);
      STAMP(6 + L * 6);
      u4 wo[18];
      u32 fq[2][9];
      {
        if (wp < 10) wp = lwait(CNT(C_WPROG), 10, dead);
        if (L == 0) lwait(CNT(C_FAOK), 1, dead);
        f4 aq[3][2];
        UNR for (int i = 0; i < 3; ++i) aq[i][0] = aq[i][1] = f4{0, 0, 0, 0};
        u4 A[2][3];
        A[0][0] = ld<u4>(L_WQ + aF[0]); A[0][1] = ld<u4>(L_WQ + aF[0] + 2048); A[0][2] = ld<u4>(a7[0]);
        UNR for (int s = 0; s < 18; ++s) {
          if (s < 17) {
            const int j = (s + 1) >> 1, u = (s + 1) & 1;
            A[(s + 1) & 1][0] = ld<u4>(L_WQ + aF[u] + j * 4608); A[(s + 1) & 1][1] = ld<u4>(L_WQ + aF[u] + j * 4608 + 2048);
            A[(s + 1) & 1][2] = ld<u4>(a7[u] + j * 4608);
          }
          UNR for (int i = 0; i < 3; ++i) {
            aq[i][0] = mfma(A[s & 1][i], B[2 * s], aq[i][0]);
            aq[i][1] = mfma(A[s & 1][i], B[2 * s + 1], aq[i][1]);
          }
          if (s < 9) {
            wo[2 * s] = gld4(ws, O_WOT + lane * 16, (9 * c + w) * 18432 + 2 * s * 1024, 1);
            wo[2 * s + 1] = gld4(ws, O_WOT + lane * 16, (9 * c + w) * 18432 + (2 * s + 1) * 1024, 1);
          }
          __builtin_amdgcn_sched_barrier(0);
        }
        STAMP(7 + L * 6);
        const f4 c5 = ld<f4>(L_CSL + 5 * 64 + q * 16), c6 = ld<f4>(L_CSL + 6 * 64 + q * 16);
        const float c7 = ld<float>(L_CSL + 7 * 64 + q * 16);
        const bool val = (i16 >> 3) == (q >> 1);
        const u32 tq = L_TQ + (val ? (q * 4 + (i16 & 3)) * 16 : 256), tq2 = L_TQ2 + (val ? (q * 4 + (i16 & 3)) * 4 : 64);
        u4 q0[2], q1[2];
        UNR for (int tb = 0; tb < 2; ++tb) {
          q0[tb].x = pk((f2{aq[0][tb].x, aq[0][tb].y} - f2{c5.x, c5.y} * mS[tb]) * mQ[tb]);
          q0[tb].y = pk((f2{aq[0][tb].z, aq[0][tb].w} - f2{c5.z, c5.w} * mS[tb]) * mQ[tb]);
          q0[tb].z = pk((f2{aq[1][tb].x, aq[1][tb].y} - f2{c6.x, c6.y} * mS[tb]) * mQ[tb]);
          q0[tb].w = pk((f2{aq[1][tb].z, aq[1][tb].w} - f2{c6.z, c6.w} * mS[tb]) * mQ[tb]);
          q1[tb] = u4{pk((aq[2][tb].x - c7 * mS[tb]) * mQ[tb], 0.0f), 0, 0, 0};
        }
        f4 P[2][5];
        UNR for (int m = 0; m < 5; ++m) {
          u4 t0 = ld<u4>(tq + m * 272), t1 = {ld<u32>(tq2 + m * 68), 0, 0, 0};
          UNR for (int tb = 0; tb < 2; ++tb) P[tb][m] = mfma(t1, q1[tb], mfma(t0, q0[tb], f4{0, 0, 0, 0}));
        }
        const float ls = (q & 1) ? -LOG2E : LOG2E;
        UNR for (int tb = 0; tb < 2; ++tb) {
          f2 z[9];
          UNR for (int i = 0; i < 9; ++i) z[i] = f2{P[tb][i >> 1][2 * (i & 1)], P[tb][i >> 1][2 * (i & 1) + 1]} * ls;
          float mx = fmaxf(z[0].x, z[0].y);
          UNR for (int i = 1; i < 9; ++i) mx = fmaxf(fmaxf(mx, z[i].x), z[i].y);
          f2 s1 = {0, 0};
          UNR for (int i = 0; i < 9; ++i) { f2 t = z[i] - mx; z[i] = f2{__builtin_amdgcn_exp2f(t.x), __builtin_amdgcn_exp2f(t.y)}; s1 += z[i]; }
          const float r1 = __builtin_amdgcn_rcpf(s1.x + s1.y);
          UNR for (int i = 0; i < 9; ++i) fq[tb][i] = pk(z[i] * r1);
        }
      }
      STAMP(8 + L * 6);
      lwait(CNT(C_STB), 4 * (L + 1), dead);
      STAMP(9 + L * 6);
      {
        const bool val = (i16 >> 3) == (q >> 1);
        const int d01 = (q >> 1) * 19 + 8 * ((i16 >> 2) & 1) + (i16 & 3), d2 = (q >> 1) * 19 + 16 + ((i16 & 3) < 2 ? (i16 & 3) : 2);
        const u32 m01 = val ? L_STB + d01 * 64 + (q & 1) * 32 : L_ZERO, m2 = val ? L_STB + d2 * 64 + (q & 1) * 32 : L_ZERO;
        const u32 l01 = val ? L_STB + 2432 + d01 * 8 + (q & 1) * 4 : L_ZERO, l2 = val ? L_STB + 2432 + d2 * 8 + (q & 1) * 4 : L_ZERO;
        f4 O[2][3];
        UNR for (int m = 0; m < 3; ++m) {
          u4 t0 = ld<u4>(m < 2 ? m01 + m * 256 : m2), t1 = ld<u4>((m < 2 ? m01 + m * 256 : m2) + 16), t2 = {ld<u32>(m < 2 ? l01 + m * 32 : l2), 0, 0, 0};
          UNR for (int tb = 0; tb < 2; ++tb) {
            u4 b0 = {fq[tb][0], fq[tb][1], fq[tb][2], fq[tb][3]}, b1 = {fq[tb][4], fq[tb][5], fq[tb][6], fq[tb][7]}, b2 = {fq[tb][8], 0, 0, 0};
            O[tb][m] = mfma(t2, b2, mfma(t1, b1, mfma(t0, b0, f4{0, 0, 0, 0})));
          }
        }
        u32 lo[2];
        UNR for (int tb = 0; tb < 2; ++tb) {
          const float inv = __builtin_amdgcn_rcpf(O[tb][2].z + 1e-6f);
          u4 o = {pk(f2{O[tb][0].x, O[tb][0].y} * inv), pk(f2{O[tb][0].z, O[tb][0].w} * inv), pk(f2{O[tb][1].x, O[tb][1].y} * inv), pk(f2{O[tb][1].z, O[tb][1].w} * inv)};
          gst4(san(o), ws, O_OB + lane * 16, L * OB_L + p * OB_P + (2 * w + tb) * 16384 + g * 1024);
          lo[tb] = pk(f2{O[tb][2].x, O[tb][2].y} * inv);
        }
        const u32 lv = san((q & 1) ? lo[1] : lo[0]);
        auto hh = SW32(lv, lv);
        const u32 n0 = __builtin_amdgcn_update_dpp(0u, hh[0], 0xB1, 0xF, 0xF, true), n1 = __builtin_amdgcn_update_dpp(0u, hh[1], 0xB1, 0xF, 0xF, true);
        gst4(u4{hh[0], hh[1], n0, n1}, ws, (q < 2 && !(i16 & 1)) ? O_OB + 131072 + (16 * q + i16) * 8 : 0x80000000u, L * OB_L + p * OB_P + g * 1024 + w * 256);
        ladd(CNT(C_PUB), lane);
      }
      STAMP(22 + L);
      tail<false, L>(a, ws, b, w, lane, g, rt, c, rot, tag, wo, wo, dead STPASS);
    }
}

template <int L>
DEV void state_sum(const Args& a, rsrc_t ws, int b, int w, int lane0, int g, int& dead STARG) {
    const int lane = opq(lane0), q = lane >> 4, i16 = lane & 15;
    u4 sel;
    UNR for (int d = 0; d < 4; ++d) sel[d] = ((i16 & 7) >> 1) == d ? ((i16 & 1) ? 0x3F800000u : 0x00003F80u) : 0u;
      {
        const int h = w - 4;
        u4 v[3][4];
        u32 off[3];
        UNR for (int gi = 0; gi < 3; ++gi) off[gi] = O_SP + q * SP_P + umin(16 * umin(h + 4 * gi, 10) + i16, 170) * 16;
        lwait(CNT(C_PUB), 8 * L + 4, dead);
        NOUNR for (int it = dead ? SPIN_G - 1 : 0;; ++it) {
          FENCE();
          u32 m = 0;
          UNR for (int gi = 0; gi < 3; ++gi) UNR for (int pp = 0; pp < 4; ++pp) v[gi][pp] = gld4(ws, off[gi], L * SP_L + g * SP_G + 4 * pp * SP_P, 1);
          UNR for (int gi = 0; gi < 3; ++gi) UNR for (int pp = 0; pp < 4; ++pp) m = vm(m, v[gi][pp]);
          if (!anyl(m == SENT)) break;
          if (it >= SPIN_G) { dead = 1; break; }
        }
        FENCE();
        STAMP(6 + L * 6);
        UNR for (int gi = 0; gi < 3; ++gi) {
          f4 s = {0, 0, 0, 0};
          UNR for (int pp = 0; pp < 4; ++pp) s = mfma(sel, v[gi][pp], s);
          int ck = 16 * umin(h + 4 * gi, 10) + i16;
          if (q < 2 && ck < 171) st<u2>(L_STB + ck * 16 + q * 8, u2{pk(s.x, s.y), pk(s.z, s.w)});
        }
        FENCE();
        ladd(CNT(C_STB), lane);
        STAMP(7 + L * 6);
      }
}

__global__ void __launch_bounds__(512, 2) fused_kernel(Args a) {
  const int tid = threadIdx.x, lane = tid & 63, w = rfl(tid >> 6), b = blockIdx.x;
  const int q = lane >> 4, i16 = lane & 15;
  const int xcd = b & 7, r = b >> 3;
  const int p = 4 * (xcd >> 1) + (r >> 3), g = 8 * (xcd & 1) + (r & 7), rot = ((r & 7) + 2 * (r >> 3)) % 9;
  const int rt = 4 * p + (g >> 2), c = g & 3;
  rsrc_t ws = mkrs(a.ws, WS_BYTES);
  u32 tag = *(const u32*)a.ws + 1;
  int dead = 0;
  u32 stv = 0;
  STAMP(0); STAMPC(29);

  if (w < 4) {
    __builtin_amdgcn_s_setprio(2);
    u4 B[36];
    float mS[2], mQ[2];
    {
      rsrc_t rx = mkrs(a.x, 2359296);
      u32 vo[2];
      UNR for (int par = 0; par < 2; ++par) vo[par] = (8 * par + (lane >> 3)) * 1152 + (((lane & 7) ^ (4 * par + (lane >> 4))) << 4);
      UNR for (int j = 0; j < 9; ++j) {
        int kr = j + rot; kr -= kr >= 9 ? 9 : 0;
        UNR for (int rg = 0; rg < 4; ++rg) dma<0>(rx, j * 16384 + w * 4096 + rg * 1024, vo[rg & 1], (128 * p + 32 * w + 16 * (rg >> 1)) * 1152 + kr * 128);
      }
      FENCE();
      __builtin_amdgcn_sched_barrier(0);
      asm volatile("s_waitcnt lgkmcnt(0)\n\ts_barrier" : "+s"(tag)::"memory");
      __builtin_amdgcn_sched_barrier(0);
      STAMP(1);
      u32 a0 = w * 4096 + (i16 >> 3) * 1024 + (i16 & 7) * 128 + ((q ^ (i16 >> 1)) << 4), a1 = a0 ^ 64, pa = CNT(C_PROG) + 4 * w;
      f4 gS[2] = {f4{0, 0, 0, 0}, f4{0, 0, 0, 0}}, gQ[2] = {f4{0, 0, 0, 0}, f4{0, 0, 0, 0}};
      const u4 ones = {0x3F803F80u, 0x3F803F80u, 0x3F803F80u, 0x3F803F80u};
#define X0STEP(j)                                                                                                                      \
  {                                                                                                                                    \
    u32 pv = j + 1;                                                                                                                    \
    asm volatile("s_waitcnt vmcnt(%8)\n\tds_read_b128 %0, %4\n\tds_read_b128 %1, %4 offset:2048\n\tds_read_b128 %2, %5\n\t"            \
                 "ds_read_b128 %3, %5 offset:2048\n\ts_waitcnt lgkmcnt(0)\n\tds_write_b32 %6, %7"                                       \
                 : "=&v"(B[4 * j]), "=&v"(B[4 * j + 1]), "=&v"(B[4 * j + 2]), "=&v"(B[4 * j + 3])                                        \
                 : "v"(a0 + j * 16384), "v"(a1 + j * 16384), "v"(pa), "v"(pv), "n"(32 - 4 * j)                                          \
                 : "memory");                                                                                                          \
    UNR for (int i = 0; i < 4; ++i) { gS[i & 1] = mfma(ones, B[4 * j + i], gS[i & 1]); gQ[i & 1] = mfma(B[4 * j + i], B[4 * j + i], gQ[i & 1]); } \
  }
      X0STEP(0) X0STEP(1) X0STEP(2) X0STEP(3) X0STEP(4) X0STEP(5) X0STEP(6) X0STEP(7) X0STEP(8)
      STAMP(2);
      UNR for (int tb = 0; tb < 2; ++tb) {
        float d = (i16 & 2) ? ((i16 & 1) ? gQ[tb].w : gQ[tb].z) : ((i16 & 1) ? gQ[tb].y : gQ[tb].x);
        d = (q == (i16 >> 2)) ? d : 0.0f;
        d = sw16add(d, d); d = sw32add(d, d);
        f2 t = ln_stats(gS[tb].x, d);
        mS[tb] = t.x; mQ[tb] = t.y;
      }
    }
    u32 wp = 0;
    main_layer<0>(a, ws, b, w, lane, p, g, rt, c, rot, tag, B, mS, mQ, wp, dead STPASS);
    main_layer<1>(a, ws, b, w, lane, p, g, rt, c, rot, tag, B, mS, mQ, wp, dead STPASS);
  } else {
    const int pr = (w - 4) >> 1, e = w & 1;
    const u32 lH = pr ? L_H1 : L_H0, lU = pr ? L_U1 : L_U0;
    if (w == 4) {
      const int cl = b % 27, kr0 = b / 27;
      if (b < 243) {
        rsrc_t rw = mkrs(a.wqkv, 576 * 3456);
        UNR for (int rg = 0; rg < 8; ++rg)
          dma<0>(rw, L_TILE + rg * 1024, ((lane >> 3) ^ (rg & 1)) * 3456 + (((lane & 7) ^ (4 * ((rg >> 1) & 1))) << 4), (64 * kr0 + 8 * rg) * 3456 + cl * 128);
      }
      FENCE();
      asm volatile("s_waitcnt lgkmcnt(0)\n\ts_barrier" : "+s"(tag)::"memory");
      if (b < 243) {
        const int cg = lane >> 3, rg = lane & 7, sec = cl / 9;
        u32 dst[8];
        UNR for (int j = 0; j < 8; ++j) {
          int cc = 64 * (cl - 9 * sec) + 8 * cg + j, h = cc / 18, d = cc - 18 * h, hl = h & 1;
          u32 o;
          if (sec == 0) {
            int hi = d >= 9, qq = 2 * hl + hi, s = d - 9 * hi;
            int row = s < 8 ? 16 * (s >> 2) + 4 * qq + (s & 3) : 32 + qq, key = s < 8 ? (row >> 1) & 7 : 0;
            o = L_WQ + kr0 * 4608 + row * 128 + ((rg ^ key) << 4);
          } else {
            int qq = 2 * (sec - 1) + hl;
            int row = d < 16 ? 16 * (d >> 2) + 4 * qq + (d & 3) : 48 + 2 * qq + d, key = d < 16 ? (row >> 1) & 7 : 2 * qq;
            o = kr0 * 9216 + row * 128 + ((rg ^ key) << 4);
          }
          dst[j] = O_WT + (h >> 1) * WT_G + o;
        }
        vmwait0();
        u4 R[8];
        const u32 rb = L_TILE + rg * 1024 + ((cg ^ (4 * ((rg >> 1) & 1))) << 4);
        UNR for (int i = 0; i < 8; ++i) R[i] = ld<u4>(rb + ((i ^ (rg & 1)) << 7));
        float cs[8];
        UNR for (int j = 0; j < 8; ++j) {
          u4 o;
          UNR for (int d = 0; d < 4; ++d) o[d] = __builtin_amdgcn_perm(R[2 * d + 1][j >> 1], R[2 * d][j >> 1], (j & 1) ? 0x07060302u : 0x05040100u);
          gst4(o, ws, dst[j], 0);
          cs[j] = dot2(o.x, 0x3F803F80u, dot2(o.y, 0x3F803F80u, 0.0f)) + dot2(o.z, 0x3F803F80u, dot2(o.w, 0x3F803F80u, 0.0f));
        }
        float sel = 0;
        UNR for (int j = 0; j < 8; ++j) {
          float v = cs[j];
          v += __uint_as_float(__builtin_amdgcn_update_dpp(0, __float_as_uint(v), 0xB1, 0xF, 0xF, true));
          v += __uint_as_float(__builtin_amdgcn_update_dpp(0, __float_as_uint(v), 0x4E, 0xF, 0xF, true));
          v += __uint_as_float(__builtin_amdgcn_update_dpp(0, __float_as_uint(v), 0x141, 0xF, 0xF, true));
          sel = rg == j ? v : sel;
        }
        gst1(__float_as_uint(sel), ws, O_CS + lane * 4, kr0 * 6912 + cl * 256);
        vmwait0();
        setflag(ws, O_FW + b * 128, tag, lane);
      }
      STAMP(1);
      {
        int l6 = lane < 54 ? lane : 0, kr = l6 / 6, i = l6 - 6 * kr;
        int ln = 9 * (i >> 1) + ((36 * g + 35 * (i & 1)) >> 6);
        poll(ws, O_FW + (kr * 27 + ln) * 128, tag, dead);
      }
      lset(CNT(C_WOK), 1);
      STAMP(2);
      u32 xp = 0;
      auto need_x = [&](u32 n) {
        if (xp < n) {
          NOUNR for (int i = dead ? SPIN_L : 0; i < SPIN_L; ++i) {
            u4 v;
            asm volatile("ds_read_b128 %0, %1\n\ts_waitcnt lgkmcnt(0)" : "=v"(v) : "v"(CNT(C_PROG)) : "memory");
            xp = rfl(umin(umin(v.x, v.y), umin(v.z, v.w)));
            if (xp >= n) return;
          }
          dead = 1;
        }
      };
      UNR for (int j = 0; j < 9; ++j) {
        need_x(((j + 1) * 9216 - 1) / 16384 + 1);
        int kr = j + rot; kr -= kr >= 9 ? 9 : 0;
        UNR for (int i = 0; i < 9; ++i) dma<SC1>(ws, j * 9216 + i * 1024, O_WT + lane * 16, g * WT_G + kr * 9216 + i * 1024);
        if (j >= 3) { vmwait<27>(); lset(CNT(C_WPROG), j - 2); }
      }
      need_x(8);
      UNR for (int i = 0; i < 41; ++i) {
        u32 o = i * 1024 + lane * 16 + rot * 4608;
        o -= o >= 41472 ? 41472 : 0;
        if (i < 40 || lane < 32) dma<SC1>(ws, L_WQ + i * 1024, O_WT + L_WQ + o, g * WT_G);
        if (i == 8) { vmwait<27>(); lset(CNT(C_WPROG), 7); }
        if (i == 17) { vmwait<27>(); lset(CNT(C_WPROG), 8); }
        if (i == 26) { vmwait<27>(); lset(CNT(C_WPROG), 9); }
      }
      vmwait0();
      lset(CNT(C_WPROG), 10);
      STAMP(3);
    } else if (w < 7) {
      KVin in0, in1;
      Hin hin;
      const int T = (w - 5) * 128 + lane, chn = 2 * b + (w - 5);
      const u32 mH = w == 5 ? L_H0 : L_H1, mU = w == 5 ? L_U0 : L_U1;
      kv_load(a, T, rt, c, in0);
      kv_load(a, T + 64, rt, c, in1);
      h_load(a, chn, lane, hin);
      FENCE();
      __builtin_amdgcn_sched_barrier(0);
      asm volatile("s_waitcnt lgkmcnt(0)\n\ts_barrier" : "+s"(tag)::"memory");
      __builtin_amdgcn_sched_barrier(0);
      kv_compute(ws, T, rt, c, in0);
      kv_compute(ws, T + 64, rt, c, in1);
      STAMP(1);
      vmwait0();
      if (ladd_rtn(CNT(C_KVC), lane) == 2) setflag(ws, O_FK + b * 128, tag, lane);
      UNR for (int i = 0; i < 4; ++i) lwait(CNT(C_PROG) + 4 * i, 9, dead);
      lwait(CNT(C_WOK), 1, dead);
      h_stage(mH, mU, lane, hin);
      poll(ws, O_FK + wg_of(lane >> 2, 4 * (lane & 3) + (2 * b) / 144) * 128, tag, dead);
      STAMP(2);
      u_stage(ws, mU, chn, lane);
      vmwait0();
      ladd(w == 5 ? CNT(C_PU0) : CNT(C_PU1), lane);
      STAMP(3);
    } else {
      if (lane < 32) lset(L_CNT + lane * 4, 0);
      const int cl = b % 9, gg = b / 9;
      if (b < 144) {
        rsrc_t rw = mkrs(a.wo, 576 * 1152);
        UNR for (int m = 0; m < 5; ++m)
          if (m < 4 || lane < 32) dma<0>(rw, L_WOTILE + m * 1024, (lane >> 3) * 1152 + (lane & 7) * 16, (36 * gg + 8 * m) * 1152 + cl * 128);
      }
      rsrc_t rp = mkrs(a.wphi, 32 * 648);
      u4 p0 = gld4(rp, lane * 16, g * 1296, 0), p1 = gld4(rp, lane < 17 ? 1024 + lane * 16 : 0x80000000u, g * 1296, 0);
      KVin in;
      if (lane < 32) kv_load(a, 256 + lane, rt, c, in);
      FENCE();
      __builtin_amdgcn_sched_barrier(0);
      asm volatile("s_waitcnt lgkmcnt(0)\n\ts_barrier" : "+s"(tag)::"memory");
      __builtin_amdgcn_sched_barrier(0);
      const u4 s4 = {SENT, SENT, SENT, SENT};
      UNR for (int i = 0; i < 3; ++i) gst4(s4, ws, (i < 2 || lane < 48) ? O_SP + i * 1024 + lane * 16 : 0x80000000u, g * SP_G + p * SP_P);
      UNR for (int i = 0; i < 9; ++i) gst4(s4, ws, O_OB + lane * 16, p * OB_P + i * 16384 + g * 1024);
      gst4(s4, ws, lane < 48 ? O_ST + q * 65536 + i16 * 16 : 0x80000000u, rt * 1024 + c * 256);
      if (lane < 32) kv_compute(ws, 256 + lane, rt, c, in);
      if (b < 144) {
        const u32 ta = L_WOTILE + (18 * (q >> 1) + 8 * (q & 1) + (i16 >> 2)) * 128 + (i16 & 3) * 8;
        UNR for (int G = 0; G < 4; ++G) gst4(trd2(ta + G * 32, ta + G * 32 + 512), ws, O_WOT + lane * 16, (4 * cl + G) * 18432 + gg * 1024);
        u2 lf = trd(L_WOTILE + (16 + ((i16 >> 2) & 1) + 18 * (i16 >> 3)) * 128 + q * 32 + (i16 & 3) * 8);
        gst2(lf, ws, O_WOT + q * 18432 + i16 * 16, 4 * cl * 18432 + (16 + (gg >> 3)) * 1024 + ((gg >> 1) & 3) * 256 + (gg & 1) * 8);
      }
      FENCE();
      asm volatile("s_waitcnt lgkmcnt(0)" ::: "memory");
      st<u4>(L_WOTILE + lane * 16, p0);
      if (lane < 17) st<u4>(L_WOTILE + 1024 + lane * 16, p1);
      FENCE();
      auto wp16 = [&](int hd, int d, int f) -> u32 { u32 v = ld<u16>(L_WOTILE + (hd * 324 + d * 18 + (f < 18 ? f : 0)) * 2); return f < 18 ? v : 0u; };
      UNR for (int rd = 0; rd < 2; ++rd) {
        int t = rd * 64 + lane, tt = t < 80 ? t : 79, m = tt >> 4, cc = (tt >> 3) & 1, hd = (tt >> 2) & 1, f = 4 * m + (tt & 3);
        u4 v;
        UNR for (int d = 0; d < 4; ++d) v[d] = wp16(hd, 8 * cc + 2 * d, f) | (wp16(hd, 8 * cc + 2 * d + 1, f) << 16);
        st<u4>(L_TK + (tt >> 3) * 144 + (tt & 7) * 16, v);
        int hq = (tt >> 3) & 1, dh = (tt >> 2) & 1;
        UNR for (int d = 0; d < 4; ++d) v[d] = wp16(hq, 9 * dh + 2 * d, f) | (wp16(hq, 9 * dh + 2 * d + 1, f) << 16);
        st<u4>(L_TQ + m * 272 + (tt & 15) * 16, v);
        st<u32>(L_TQ2 + m * 68 + (tt & 15) * 4, wp16(hq, 9 * dh + 8, f));
      }
      {
        int tt = lane < 40 ? lane : 39, m = tt >> 3, hd = (tt >> 2) & 1, f = 4 * m + (tt & 3);
        st<u32>(L_TK2 + m * 36 + (tt & 7) * 4, wp16(hd, 16, f) | (wp16(hd, 17, f) << 16));
        u4 z = {0, 0, 0, 0};
        if (lane < 10) st<u4>(L_TK + lane * 144 + 128, z);
        if (lane < 5) { st<u4>(L_TQ + lane * 272 + 256, z); st<u32>(L_TK2 + lane * 36 + 32, 0u); st<u32>(L_TQ2 + lane * 68 + 64, 0u); }
      }
      FENCE();
      vmwait0();
      setflag(ws, O_FA + b * 128, tag, lane);
      if (ladd_rtn(CNT(C_KVC), lane) == 2) setflag(ws, O_FK + b * 128, tag, lane);
      STAMP(1);
      lwait(CNT(C_WOK), 1, dead);
      UNR for (int rd = 0; rd < 2; ++rd) {
        int blk = rd * 4 + q, qq = i16 >> 2, ee = i16 & 3;
        int col;
        if (rd == 0) col = 576 + 576 * (qq >> 1) + 36 * g + 18 * (qq & 1) + 4 * blk + ee;
        else col = blk == 4 ? 576 + 576 * (qq >> 1) + 36 * g + 18 * (qq & 1) + 16 + (ee & 1) : 36 * g + 9 * qq + (blk == 7 ? 8 : 4 * (blk - 5) + ee);
        float s = 0;
        UNR for (int k = 0; k < 9; ++k) s += __uint_as_float(gld1(ws, O_CS + col * 4, k * 6912, 1));
        st<float>(L_CSL + rd * 256 + lane * 4, s);
      }
      if (lane < 18) st<u4>(L_ZERO + lane * 16, u4{0, 0, 0, 0});
      FENCE();
      ladd(CNT(C_TBL), lane);
      STAMP(2);
      {
        poll(ws, O_FA + wg_of(lane < 16 ? lane : p, lane < 16 ? g : (lane & 15)) * 128, tag, dead);
        int ln = (144 * c + 48 * q + (q == 3 ? -1 : 0)) >> 6;
        poll(ws, O_FA + (i16 * 9 + ln) * 128, tag, dead);
      }
      lset(CNT(C_FAOK), 1);
      lwait(CNT(C_WPROG), 10, dead);
      UNR for (int i = 0; i < 9; ++i) {
        u32 o = i * 1024 + lane * 16;
        o += o >= 4608 ? 18432 - 4608 : 0;
        gst4(s4, ws, O_X2 + o, 2 * rt * 18432 + c * 4608);
      }
      UNR for (int i = 0; i < 3; ++i) gst4(s4, ws, (i < 2 || lane < 48) ? O_SP + i * 1024 + lane * 16 : 0x80000000u, SP_L + g * SP_G + p * SP_P);
      UNR for (int i = 0; i < 9; ++i) gst4(s4, ws, O_OB + lane * 16, OB_L + p * OB_P + i * 16384 + g * 1024);
      STAMP(3);
    }
    u4 wo[18], wo8[18];
    lwait(CNT(C_WPROG), 10, dead);
    lwait(CNT(C_FAOK), 1, dead);
    UNR for (int i = 0; i < 18; ++i) wo[i] = gld4(ws, O_WOT + lane * 16, (9 * c + w) * 18432 + i * 1024, 1);
    lwait(pr ? CNT(C_PU1) : CNT(C_PU0), 1, dead);
    STAMP(4);
    conv_pair(a, ws, lH, lU, 2 * b + pr, e, pr ? CNT(C_PA1) : CNT(C_PA0), pr ? CNT(C_PB1) : CNT(C_PB0), 2, lane, dead);
    STAMP(5);
    vmwait0();
    if (w == 7) setflag(ws, O_FB + b * 128, tag, lane);
    if (ladd_rtn(CNT(C_FYC), lane) == 3) setflag(ws, O_FY + b * 128, tag, lane);
    state_sum<0>(a, ws, b, w, lane, g, dead STPASS);
      {
        if (c == 0 && w < 6) {
          const int ch = 512 + rt;
          lwait(CNT(C_FYC), 4, dead);
          if (w == 5) {
            Hin hin;
            h_load(a, ch, lane, hin);
            poll(ws, O_FK + wg_of(lane >> 2, 4 * (lane & 3) + 3) * 128, tag, dead);
            u_stage(ws, L_U0, ch, lane);
            h_stage(L_H0, L_U0, lane, hin);
            vmwait0();
            ladd(CNT(C_PU0), lane);
          }
          lwait(CNT(C_PU0), 2, dead);
          conv_pair(a, ws, L_H0, L_U0, ch, e, CNT(C_PA0), CNT(C_PB0), 4, lane, dead);
          vmwait0();
          if (ladd_rtn(CNT(C_FZC), lane) == 1) setflag(ws, O_FZ + (ch - 512) * 128, tag, lane);
        }
        if (w < 6) UNR for (int i = 0; i < 18; ++i) wo8[i] = gld4(ws, O_WOT + lane * 16, (9 * c + 8) * 18432 + i * 1024, 1);
        if (w == 7) poll(ws, O_FB + wg_of(lane < 16 ? lane : p, lane < 16 ? g : (lane & 15)) * 128, tag, dead);
        STAMP(8);
      }
    tail<true, 0>(a, ws, b, w, lane, g, rt, c, rot, tag, wo, wo8, dead STPASS);
    state_sum<1>(a, ws, b, w, lane, g, dead STPASS);
    tail<true, 1>(a, ws, b, w, lane, g, rt, c, rot, tag, wo, wo8, dead STPASS);
    if (b == 0 && w == 7) {
      UNR for (int i = 0; i < 4; ++i) poll(ws, O_FA + (i * 64 + lane) * 128, tag, dead);
      setflag(ws, 0, tag, lane);
    }
  }
  STAMP(31); STAMPC(30);
}

extern "C" void kernel_launch(void* const* d_in, const int* in_sizes, int n_in, void* d_out, int out_size, void* d_ws, size_t ws_size, hipStream_t stream) {
  Args a;
  a.x = (const u16*)d_in[0]; a.wqkv = (const u16*)d_in[1]; a.wphi = (const u16*)d_in[2]; a.wo = (const u16*)d_in[3];
  a.bk = (const u16*)d_in[5]; a.bv = (const u16*)d_in[6]; a.sk = (const u16*)d_in[7]; a.ssm = (const u16*)d_in[8]; a.dv = (const u16*)d_in[9];
  a.out = (u16*)d_out; a.ws = (unsigned char*)d_ws;
  (void)hipFuncSetAttribute((const void*)fused_kernel, hipFuncAttributeMaxDynamicSharedMemorySize, LDS_BYTES);
  fused_kernel<<<256, 512, LDS_BYTES, stream>>>(a);
}
```

```cpp
#include <hip/hip_runtime.h>
#include <hip/hip_bf16.h>

typedef unsigned u32;
typedef unsigned short u16;
typedef float f2 __attribute__((ext_vector_type(2)));
typedef float f4 __attribute__((ext_vector_type(4)));
typedef u32 u2 __attribute__((ext_vector_type(2)));
typedef u32 u4 __attribute__((ext_vector_type(4)));
typedef u32 u2a __attribute__((ext_vector_type(2), aligned(4)));
typedef __bf16 bf2 __attribute__((ext_vector_type(2)));
typedef __bf16 bf8 __attribute__((ext_vector_type(8)));
typedef short s4v __attribute__((ext_vector_type(4)));
typedef __amdgpu_buffer_rsrc_t rsrc_t;
#define LAS __attribute__((address_space(3)))
#define DEV __device__ __forceinline__
#define UNR _Pragma("unroll")
#define NOUNR _Pragma("unroll 1")
#define FENCE() asm volatile("" ::: "memory")
#define SC1 16

constexpr u32 O_FW = 4096, O_FA = O_FW + 32768, O_FK = O_FA + 32768, O_FO = O_FK + 32768, O_FY = O_FO + 32768, O_FZ = O_FY + 32768, O_FB = O_FZ + 32768;
constexpr u32 O_WT = 262144, WT_G = 124416;
constexpr u32 O_CS = O_WT + 16 * WT_G;
constexpr u32 O_WOT = O_CS + 62464;
constexpr u32 O_KVT = O_WOT + 663552;
constexpr u32 O_YT = O_KVT + 2359296;
constexpr u32 O_SP = O_YT + 2359296, SP_L = 720896, SP_G = 45056, SP_P = 2816;
constexpr u32 O_OB = O_SP + 2 * SP_L, OB_L = 2359296, OB_P = 147456;
constexpr u32 O_ST = O_OB + 2 * OB_L;
constexpr u32 O_X2 = O_ST + 3 * 65536;
constexpr u32 O_STAMP = O_X2 + 2359296;
constexpr u32 WS_BYTES = O_STAMP + 256 * 8 * 32 * 8;
constexpr u32 L_WQ = 82944, L_STG = 46080;
constexpr u32 L_H0 = 124416, L_H1 = 132736, L_U0 = 141056, L_Y = 124416, L_XR = 133632;
constexpr u32 L_TILE = 147456, L_U1 = 147456, L_STB = 152064, L_ZERO = 154832, L_CSL = 155136;
constexpr u32 L_WOTILE = 155648, L_REDA = 155648, L_REDB = 157696;
constexpr u32 L_TK = 160256, L_TK2 = 161696, L_TQ = 161888, L_TQ2 = 163248, L_CNT = 163712, LDS_BYTES = 163840;
enum { C_PROG = 0, C_WPROG = 4, C_WOK, C_TBL, C_KVC, C_PHI, C_STB, C_PU0, C_PU1, C_PA0, C_PA1, C_PB0, C_PB1, C_REDA, C_REDB, C_FYC, C_FAOK, C_FZC, C_GEM, C_PUB };
#define CNT(i) (L_CNT + 4 * (i))
constexpr u32 SENT = 0xFFFFFFFFu;
constexpr int SPIN_G = 1 << 17, SPIN_L = 1 << 21;

#define MAP 1
__device__ __forceinline__ int wg_of(int p, int g) {
  return (p >> 2) * 2 + (g >> 3) + 8 * ((p & 3) * 8 + (g & 7));
}
struct Args {
  const u16 *x, *wqkv, *wphi, *wo, *bk, *bv, *sk, *ssm, *dv;
  u16* out;
  unsigned char* ws;
};

DEV int opq(int v) { asm volatile("" : "+v"(v)); return v; }
DEV u32 rfl(u32 v) { return __builtin_amdgcn_readfirstlane(v); }
DEV rsrc_t mkrs(const void* p, u32 n) { return __builtin_amdgcn_make_buffer_rsrc((void*)p, 0, n, 0x00020000); }
DEV u32 pk(float a, float b) { f2 v = {a, b}; return __builtin_bit_cast(u32, __builtin_convertvector(v, bf2)); }
DEV u32 pk(f2 v) { return __builtin_bit_cast(u32, __builtin_convertvector(v, bf2)); }
DEV f2 up(u32 p) { f2 r = {__uint_as_float(p << 16), __uint_as_float(p & 0xffff0000u)}; return r; }
DEV f2 rnd(f2 v) { return up(pk(v)); }
DEV float rnd1(float v) { return __uint_as_float(pk(v, v) & 0xffff0000u); }
DEV f4 mfma(u4 a, u4 b, f4 c) { return __builtin_amdgcn_mfma_f32_16x16x32_bf16(__builtin_bit_cast(bf8, a), __builtin_bit_cast(bf8, b), c, 0, 0, 0); }
DEV u32 umin(u32 a, u32 b) { return a < b ? a : b; }
DEV u32 umax(u32 a, u32 b) { return a > b ? a : b; }
DEV u32 umax3(u32 a, u32 b, u32 c) { return umax(umax(a, b), c); }
DEV u32 san(u32 v) { return umin(v, 0xFFFFFFFEu); }
DEV u4 san(u4 v) { u4 r = {san(v.x), san(v.y), san(v.z), san(v.w)}; return r; }
DEV u32 vm(u32 m, u4 v) { return umax3(umax3(m, v.x, v.y), v.z, v.w); }
DEV bool anyl(bool c) { return __builtin_amdgcn_ballot_w64(c) != 0; }
DEV float dot2(u32 a, u32 b, float c) { return __builtin_amdgcn_fdot2_f32_bf16(__builtin_bit_cast(bf2, a), __builtin_bit_cast(bf2, b), c, false); }
template <class T> DEV T ld(u32 o) { return *(LAS T*)(uintptr_t)o; }
template <class T> DEV void st(u32 o, T v) { *(LAS T*)(uintptr_t)o = v; }
DEV u2 trd(u32 o) { return __builtin_bit_cast(u2, __builtin_amdgcn_ds_read_tr16_b64_v4i16((LAS s4v*)(uintptr_t)o)); }
DEV u4 trd2(u32 o0, u32 o1) { u2 a = trd(o0), b = trd(o1); u4 r = {a.x, a.y, b.x, b.y}; return r; }
DEV u4 gld4(rsrc_t r, u32 v, u32 s, int aux) { return aux ? __builtin_amdgcn_raw_buffer_load_b128(r, v, s, SC1) : __builtin_amdgcn_raw_buffer_load_b128(r, v, s, 0); }
DEV u2 gld2(rsrc_t r, u32 v, u32 s, int aux) { return aux ? __builtin_amdgcn_raw_buffer_load_b64(r, v, s, SC1) : __builtin_amdgcn_raw_buffer_load_b64(r, v, s, 0); }
DEV u32 gld1(rsrc_t r, u32 v, u32 s, int aux) { return aux ? __builtin_amdgcn_raw_buffer_load_b32(r, v, s, SC1) : __builtin_amdgcn_raw_buffer_load_b32(r, v, s, 0); }
DEV void gst4(u4 d, rsrc_t r, u32 v, u32 s) { __builtin_amdgcn_raw_buffer_store_b128(d, r, v + s, 0, SC1); }
DEV void gst2(u2 d, rsrc_t r, u32 v, u32 s) { __builtin_amdgcn_raw_buffer_store_b64(d, r, v, s, SC1); }
DEV void gst1(u32 d, rsrc_t r, u32 v, u32 s) { __builtin_amdgcn_raw_buffer_store_b32(d, r, v, s, SC1); }
template <int AUX> DEV void dma(rsrc_t r, u32 l, u32 v, u32 s) {
#if defined(__HIP_DEVICE_COMPILE__)
  __builtin_amdgcn_raw_ptr_buffer_load_lds(r, (LAS void*)(uintptr_t)l, 16, v, s, 0, AUX);
#endif
}
DEV void vmwait0() { asm volatile("s_waitcnt vmcnt(0)" ::: "memory"); }
template <int N> DEV void vmwait() { asm volatile("s_waitcnt vmcnt(%0)" ::"n"(N) : "memory"); }
DEV void bar() { asm volatile("s_waitcnt lgkmcnt(0)\n\ts_barrier" ::: "memory"); }
DEV void lset(u32 a, u32 v) { asm volatile("ds_write_b32 %0, %1" ::"v"(a), "v"(v) : "memory"); }
DEV void ladd(u32 a, int lane) { if (lane == 0) { u32 one = 1; asm volatile("ds_add_u32 %0, %1" ::"v"(a), "v"(one) : "memory"); } }
DEV u32 ladd_rtn(u32 a, int lane) {
  u32 r = 0;
  if (lane == 0) { u32 one = 1; asm volatile("ds_add_rtn_u32 %0, %1, %2\n\ts_waitcnt lgkmcnt(0)" : "=v"(r) : "v"(a), "v"(one) : "memory"); }
  return rfl(r);
}
DEV u32 lget(u32 a) { u32 r; asm volatile("ds_read_b32 %0, %1\n\ts_waitcnt lgkmcnt(0)" : "=v"(r) : "v"(a) : "memory"); return rfl(r); }
DEV u32 lwait(u32 a, u32 t, int& dead) {
  u32 r = lget(a);
  if (__builtin_expect(r < t, 0)) {
    NOUNR for (int i = dead ? SPIN_L : 0; i < SPIN_L; ++i) { r = lget(a); if (r >= t) return r; __builtin_amdgcn_s_sleep(1); }
    dead = 1;
  }
  return r;
}
DEV void poll(rsrc_t ws, u32 off, u32 tag, int& dead) {
  NOUNR for (int i = dead ? SPIN_G : 0; i < SPIN_G; ++i) {
    FENCE();
    u32 v = gld1(ws, off, 0, 1);
    if (!anyl(v != tag)) { FENCE(); return; }
    __builtin_amdgcn_s_sleep(1);
  }
  dead = 1;
}
DEV void setflag(rsrc_t ws, u32 off, u32 tag, int lane) { if (lane == 0) gst1(tag, ws, off, 0); }
#define SW16(a, b) __builtin_amdgcn_permlane16_swap(a, b, false, false)
#define SW32(a, b) __builtin_amdgcn_permlane32_swap(a, b, false, false)
DEV float sw16add(float a, float b) { auto r = SW16(__float_as_uint(a), __float_as_uint(b)); return __uint_as_float(r[0]) + __uint_as_float(r[1]); }
DEV float sw32add(float a, float b) { auto r = SW32(__float_as_uint(a), __float_as_uint(b)); return __uint_as_float(r[0]) + __uint_as_float(r[1]); }

#define STAMP(i)
#define STAMPC(i)
#define STARG
#define STPASS

DEV f2 ln_stats(float s, float sq) {
  float mean = s * (1.0f / 576.0f);
  float var = fmaxf(sq * (1.0f / 576.0f) - mean * mean, 0.0f);
  float sd = rnd1(__builtin_amdgcn_sqrtf(rnd1(rnd1(var) + 1e-5f)));
  f2 r = {rnd1(mean), __builtin_amdgcn_rcpf(sd)};
  return r;
}
DEV void stats_from(u4 v, float (&m)[2], float (&rs)[2]) {
  float s = __uint_as_float(v.x) + __uint_as_float(v.z), sq = __uint_as_float(v.y) + __uint_as_float(v.w);
  s = sw16add(s, s); sq = sw16add(sq, sq);
  f2 t = ln_stats(s, sq);
  auto r0 = SW32(__float_as_uint(t.x), __float_as_uint(t.x));
  auto r1 = SW32(__float_as_uint(t.y), __float_as_uint(t.y));
  m[0] = __uint_as_float(r0[0]); m[1] = __uint_as_float(r0[1]); rs[0] = __uint_as_float(r1[0]); rs[1] = __uint_as_float(r1[1]);
}
DEV u4 ld_stats(rsrc_t ws, u32 off) { u2 a = gld2(ws, off, 0, 1), b = gld2(ws, off + 256, 0, 1); return u4{a.x, a.y, b.x, b.y}; }
DEV u4 poll_stats(rsrc_t ws, u32 off, u4 v, int& dead) {
  if (anyl(vm(0, v) == SENT)) {
    NOUNR for (int i = dead ? SPIN_G : 0; i < SPIN_G; ++i) {
      FENCE();
      v = ld_stats(ws, off);
      if (!anyl(vm(0, v) == SENT)) { FENCE(); return v; }
    }
    dead = 1;
  }
  FENCE();
  return v;
}

DEV void pub_stats(rsrc_t ws, float s0, float s1, float q0, float q1, u32 red, u32 cnt, u32 last, u32 dst, int w, int lane) {
  float t = sw16add(sw32add(s0, s1), sw32add(q0, q1));
  st<float>(red + w * 256 + (16 * (lane >> 5) + (lane & 15)) * 8 + ((lane >> 4) & 1) * 4, t);
  FENCE();
  if (ladd_rtn(cnt, lane) == last) {
    f4 r = {0, 0, 0, 0};
    UNR for (int i = 0; i < 8; ++i) r += ld<f4>(red + i * 256 + (lane & 15) * 16);
    gst4(san(__builtin_bit_cast(u4, r)), ws, lane < 16 ? dst + lane * 16 : 0x80000000u, 0);
  }
}

struct KVin { u32 k[11], v[8]; u4 w; };
DEV void kv_load(const Args& a, int T, int rt, int c, KVin& in) {
  rsrc_t rk = mkrs(a.bk, 2359296), rv = mkrs(a.bv, 2359296), rw = mkrs(a.sk, 4608);
  int cp = T >> 2, sub = T & 3, ch = 144 * c + 2 * cp, s0 = 32 * rt + 8 * sub;
  u32 base = s0 * 1152 + ch * 2;
  UNR for (int i = 0; i < 3; ++i) in.k[i] = gld1(rk, s0 == 0 ? 0x80000000u : base - (3 - i) * 1152, 0, 0);
  UNR for (int i = 0; i < 8; ++i) in.k[3 + i] = gld1(rk, base, i * 1152, 0);
  UNR for (int i = 0; i < 8; ++i) in.v[i] = gld1(rv, base, i * 1152, 0);
  in.w = gld4(rw, ch * 8, 0, 0);
}
DEV void kv_compute(rsrc_t ws, int T, int rt, int c, const KVin& in) {
  int cp = T >> 2, sub = T & 3, ch = 144 * c + 2 * cp, s0 = 32 * rt + 8 * sub;
  f2 w0 = {__uint_as_float(in.w.x << 16), __uint_as_float(in.w.z << 16)}, w1 = {__uint_as_float(in.w.x & 0xffff0000u), __uint_as_float(in.w.z & 0xffff0000u)};
  f2 w2 = {__uint_as_float(in.w.y << 16), __uint_as_float(in.w.w << 16)}, w3 = {__uint_as_float(in.w.y & 0xffff0000u), __uint_as_float(in.w.w & 0xffff0000u)};
  f2 kf[11];
  UNR for (int i = 0; i < 11; ++i) kf[i] = up(in.k[i]);
  u32 P[8];
  UNR for (int s = 0; s < 8; ++s) {
    f2 t = rnd(kf[s] * w3);
    t = rnd(t + rnd(kf[s + 1] * w2));
    t = rnd(t + rnd(kf[s + 2] * w1));
    t = rnd(t + rnd(kf[s + 3] * w0));
    P[s] = pk(t * up(in.v[s]));
  }
  u4 o0, o1;
  UNR for (int d = 0; d < 4; ++d) {
    o0[d] = __builtin_amdgcn_perm(P[6 - 2 * d], P[7 - 2 * d], 0x05040100u);
    o1[d] = __builtin_amdgcn_perm(P[6 - 2 * d], P[7 - 2 * d], 0x07060302u);
  }
  u32 off = O_KVT + ch * 4096 + (2040 - s0) * 2;
  gst4(o0, ws, off, 0);
  gst4(o1, ws, off, 4096);
}

struct Hin { u4 h[4]; u32 n[4]; };
DEV void h_load(const Args& a, int ch, int lane, Hin& in) {
  rsrc_t rh = mkrs(a.ssm, 2359296);
  UNR for (int m = 0; m < 4; ++m) { in.h[m] = gld4(rh, ch * 4096 + lane * 16, m * 1024, 0); in.n[m] = gld1(rh, ch * 4096 + lane * 16 + 16, m * 1024, 0); }
}
DEV void h_stage(u32 lH, u32 lU, int lane, const Hin& in) {
  UNR for (int m = 0; m < 4; ++m) {
    u4 h = in.h[m];
    u4 s = {__builtin_amdgcn_alignbit(h.y, h.x, 16), __builtin_amdgcn_alignbit(h.z, h.y, 16), __builtin_amdgcn_alignbit(h.w, h.z, 16), __builtin_amdgcn_alignbit(in.n[m], h.w, 16)};
    st<u4>(lH + 32 + m * 1024 + lane * 16, h);
    st<u4>(lH + 4160 + 32 + m * 1024 + lane * 16, s);
  }
  u32 h0 = rfl(in.h[0].x) << 16;
  u4 z = {0, 0, 0, lane == 3 ? h0 : 0u};
  if (lane < 4) st<u4>(lH + (lane >> 1) * 4160 + (lane & 1) * 16, z);
  u4 zz = {0, 0, 0, 0};
  if (lane < 30) st<u4>(lU + 4096 + lane * 16, zz);
}
DEV void u_stage(rsrc_t ws, u32 lU, int ch, int lane) {
  UNR for (int m = 0; m < 4; ++m) dma<SC1>(ws, lU + m * 1024, O_KVT + ch * 4096 + lane * 16, m * 1024);
}
DEV void conv_pair(const Args& a, rsrc_t ws, u32 lH, u32 lU, int ch, int e, u32 cA, u32 cB, u32 tgt, int lane, int& dead) {
  const int q = lane >> 4, i16 = lane & 15;
  f4 acc[8];
  UNR for (int i = 0; i < 8; ++i) acc[i] = f4{0, 0, 0, 0};
  const u32 aB = lU + 2 * (2032 - 16 * i16 + 8 * q) - 3584 + 256 * e;
  const u32 aA = lH + ((i16 & 1) ? 0 : 4160) + 4 * (((i16 + 1) >> 1) + 4 * q) + 256 * e;
  const u32 aA4[4] = {aA, aA + 1024, aA + 2048, aA + 3072};
  float dv = __uint_as_float((u32)a.dv[ch] << 16);
  u4 B[8];
  u2 Al[8], Ah[8];
#define CLD(t, bb)                                                                                                              \
  asm volatile("ds_read2_b32 %0, %3 offset0:%5 offset1:%6\n\tds_read2_b32 %1, %3 offset0:%7 offset1:%8\n\tds_read_b128 %2, %4 offset:%9" \
               : "=&v"(Al[t]), "=&v"(Ah[t]), "=&v"(B[7 - (t)])                                                                    \
               : "v"(aA4[(t) >> 1]), "v"(aB), "n"(16 * (bb) + 128 * ((t) & 1)), "n"(16 * (bb) + 128 * ((t) & 1) + 1),              \
                 "n"(16 * (bb) + 128 * ((t) & 1) + 2), "n"(16 * (bb) + 128 * ((t) & 1) + 3), "n"(512 * (t) + 64 * (bb))           \
               : "memory")
  UNR for (int t = 0; t < 8; ++t) CLD(t, 0);
  UNR for (int bb = 0; bb < 4; ++bb) {
    asm volatile("s_waitcnt lgkmcnt(6)" : "+v"(Al[0]), "+v"(Ah[0]), "+v"(Al[1]), "+v"(Ah[1]), "+v"(Al[2]), "+v"(Ah[2]), "+v"(Al[3]), "+v"(Ah[3]), "+v"(Al[4]),
                 "+v"(Ah[4]), "+v"(Al[5]), "+v"(Ah[5]), "+v"(B[7]), "+v"(B[6]), "+v"(B[5]), "+v"(B[4]), "+v"(B[3]), "+v"(B[2]));
    UNR for (int t = 0; t < 8; ++t) {
      u4 A = {Al[t].x, Al[t].y, Ah[t].x, Ah[t].y};
      UNR for (int v = 7 - t; v >= 0; --v) {
        if (t == 0 && v == 1) {
          __builtin_amdgcn_sched_barrier(0);
          asm volatile("s_waitcnt lgkmcnt(0)" : "+v"(Al[6]), "+v"(Ah[6]), "+v"(Al[7]), "+v"(Ah[7]), "+v"(B[1]), "+v"(B[0]));
        }
        acc[t + v] = mfma(A, B[v], acc[t + v]);
      }
      __builtin_amdgcn_sched_barrier(0);
      if (bb < 3) CLD(t, bb + 1);
    }
  }
  FENCE();
  ladd(cA, lane);
  lwait(cA, tgt, dead);
  f4 keep[4];
  UNR for (int i = 0; i < 4; ++i) {
    keep[i] = e ? acc[4 + i] : acc[i];
    f4 snd = e ? acc[i] : acc[4 + i];
    st<f4>(lH + e * 4096 + i * 1024 + lane * 16, snd);
  }
  FENCE();
  ladd(cB, lane);
  lwait(cB, tgt, dead);
  u2 Y[4];
  UNR for (int i = 0; i < 4; ++i) {
    f4 s = keep[i] + ld<f4>(lH + (1 - e) * 4096 + i * 1024 + lane * 16);
    u2 uu = ld<u2>(lU + 2 * (2044 - 256 * (4 * e + i) - 16 * i16 - 4 * q));
    f2 u32_ = up(uu.x), u10 = up(uu.y);
    f2 g01 = {u10.y * dv, u10.x * dv}, g23 = {u32_.y * dv, u32_.x * dv};
    f2 s01 = {s.x, s.y}, s23 = {s.z, s.w};
    Y[i].x = pk(rnd(s01) + rnd(g01));
    Y[i].y = pk(rnd(s23) + rnd(g23));
  }
  UNR for (int i = 0; i < 2; ++i) {
    auto r0 = SW16(Y[2 * i].x, Y[2 * i + 1].x);
    auto r1 = SW16(Y[2 * i].y, Y[2 * i + 1].y);
    u4 o = {r0[0], r1[0], r0[1], r1[1]};
    gst4(o, ws, O_YT + ch * 4096 + 2 * (256 * (4 * e + 2 * i + (q & 1)) + 16 * i16 + 8 * (q >> 1)), 0);
  }
}

template <bool HELP, int L>
DEV void tail(const Args& a, rsrc_t ws, int b, int w, int lane0, int g, int rt, int c, int rot, u32 tag, const u4 (&wo)[18], const u4 (&wo8)[18], int& dead STARG) {
  const int lane = opq(lane0), q = lane >> 4, i16 = lane & 15;
  const bool ex = HELP && w < 6;
  const int tb8 = w & 1;
  u2 xr[3];
  if (L == 0) {
    rsrc_t rx = mkrs(a.x, 2359296);
    u32 o = (32 * rt + i16) * 1152 + (144 * c + 4 * q) * 2;
    xr[0] = gld2(rx, o, w * 32, 0); xr[1] = gld2(rx, o, w * 32 + 16 * 1152, 0);
    xr[2] = gld2(rx, o, 256 + tb8 * 16 * 1152, 0);
  } else {
    xr[0] = ld<u2>(L_XR + w * 1024 + lane * 8); xr[1] = ld<u2>(L_XR + w * 1024 + 512 + lane * 8);
    xr[2] = ld<u2>(L_XR + 8192 + tb8 * 512 + lane * 8);
  }
  {
    const u32 pb = L * OB_L + (rt >> 2) * OB_P, tbp = 2 * (rt & 3);
    u4 v[5];
    if (HELP) lwait(CNT(C_PUB), 8 * L + 8, dead);
    NOUNR for (int it = dead ? SPIN_G - 1 : 0;; ++it) {
      FENCE();
      UNR for (int i = 0; i < 4; ++i) v[i] = gld4(ws, O_OB + lane * 16, pb + (tbp + (i >> 1)) * 16384 + (w + 8 * (i & 1)) * 1024, 1);
      if (!HELP) {
        u32 o = O_OB + 131072 + q * 2048 + i16 * 8, so = pb + (w & 1) * 8192 + (tbp + (w >> 1)) * 128;
        u2 l0 = gld2(ws, o, so, 1), l1 = gld2(ws, o + 1024, so, 1);
        v[4] = u4{l0.x, l0.y, l1.x, l1.y};
      }
      u32 m = 0;
      UNR for (int i = 0; i < (HELP ? 4 : 5); ++i) m = vm(m, v[i]);
      if (!anyl(m == SENT)) break;
      if (it >= SPIN_G) { dead = 1; break; }
    }
    FENCE();
    UNR for (int i = 0; i < 4; ++i) st<u4>(L_STG + (i >> 1) * 18432 + (w + 8 * (i & 1)) * 1024 + lane * 16, v[i]);
    if (!HELP) st<u4>(L_STG + (w >> 1) * 18432 + (16 + (w & 1)) * 1024 + lane * 16, v[4]);
  }
  STAMP(16 + L * 8);
  bar();
  STAMP(17 + L * 8);
  u32 fyoff = 0, fy = tag;
  const int chl = 144 * c + ((lane & 16) ? 128 : 16 * w) + i16;
  if (L == 0) { fyoff = chl < 512 ? O_FY + (chl >> 1) * 128 : O_FZ + (chl - 512) * 128; fy = gld1(ws, fyoff, 0, 1); }
  f4 acc[3] = {f4{0, 0, 0, 0}, f4{0, 0, 0, 0}, f4{0, 0, 0, 0}};
  if (ex) {
    UNR for (int ks = 0; ks < 18; ++ks) {
      u4 b0 = ld<u4>(L_STG + ks * 1024 + lane * 16), b1 = ld<u4>(L_STG + 18432 + ks * 1024 + lane * 16);
      u4 b2 = ld<u4>(L_STG + tb8 * 18432 + ks * 1024 + lane * 16);
      acc[0] = mfma(wo[ks], b0, acc[0]); acc[1] = mfma(wo[ks], b1, acc[1]); acc[2] = mfma(wo8[ks], b2, acc[2]);
    }
  } else {
    UNR for (int ks = 0; ks < 18; ++ks) {
      u4 b0 = ld<u4>(L_STG + ks * 1024 + lane * 16), b1 = ld<u4>(L_STG + 18432 + ks * 1024 + lane * 16);
      acc[0] = mfma(wo[ks], b0, acc[0]); acc[1] = mfma(wo[ks], b1, acc[1]);
    }
  }
  u4 yv, yv8;
  if (L == 0) {
    if (anyl(fy != tag)) poll(ws, fyoff, tag, dead);
    FENCE();
    yv = gld4(ws, O_YT + (lane >> 2) * 4096 + (lane & 3) * 16, (144 * c + 16 * w) * 4096 + rt * 64, 1);
    yv8 = gld4(ws, O_YT + (lane >> 2) * 4096 + (lane & 3) * 16, (144 * c + 128) * 4096 + rt * 64, 1);
  }
  constexpr int NU = HELP ? 3 : 2;
  f2 x1[NU][2];
  float S[NU], Q[NU];
  UNR for (int u = 0; u < NU; ++u) {
    f2 lo = {acc[u].x, acc[u].y}, hi = {acc[u].z, acc[u].w};
    x1[u][0] = rnd(rnd(lo) + up(xr[u].x)); x1[u][1] = rnd(rnd(hi) + up(xr[u].y));
    f2 s = x1[u][0] + x1[u][1], sq = x1[u][0] * x1[u][0] + x1[u][1] * x1[u][1];
    S[u] = s.x + s.y; Q[u] = sq.x + sq.y;
  }
  if (HELP) { if (w == 4) { S[0] += S[2]; Q[0] += Q[2]; } if (w == 5) { S[1] += S[2]; Q[1] += Q[2]; } }
  const u32 stoff = O_ST + L * 2 * 65536 + rt * 1024 + (q & 1) * 512 + (16 * (q >> 1) + i16) * 8;
  pub_stats(ws, S[0], S[1], Q[0], Q[1], L_REDA, CNT(C_REDA), 8 * L + 7, O_ST + L * 2 * 65536 + rt * 1024 + c * 256, w, lane);
  STAMP(18 + L * 8);
  if (HELP && L == 0 && w == 4) {
    lwait(CNT(C_REDA), 8, dead);
    UNR for (int j = 5; j < 9; ++j) {
      int kr = j + rot; kr -= kr >= 9 ? 9 : 0;
      UNR for (int i = 0; i < 9; ++i) dma<SC1>(ws, j * 9216 + i * 1024, O_WT + lane * 16, g * WT_G + kr * 9216 + i * 1024);
    }
  }
  u4 sv = ld_stats(ws, stoff);
  if (L == 0) {
    st<u4>(L_Y + w * 1024 + lane * 16, yv);
    if (ex) st<u4>(L_Y + 8192 + lane * 16, yv8);
    FENCE();
  }
  u2 y[NU];
  {
    u32 ya = L_Y + (4 * q + (i16 >> 2)) * 64 + (i16 & 3) * 8;
    y[0] = trd(ya + w * 1024); y[1] = trd(ya + w * 1024 + 32);
    if (HELP) y[2] = trd(ya + 8192 + tb8 * 32);
  }
  sv = poll_stats(ws, stoff, sv, dead);
  STAMP(19 + L * 8);
  if (HELP && L == 0 && w == 4) { vmwait0(); lset(CNT(C_WPROG), 11); }
  float m[2], rs[2];
  stats_from(sv, m, rs);
  u2 x2[NU];
  UNR for (int u = 0; u < NU; ++u) {
    float mu = u < 2 ? m[u] : (tb8 ? m[1] : m[0]), ru = u < 2 ? rs[u] : (tb8 ? rs[1] : rs[0]);
    UNR for (int h = 0; h < 2; ++h) {
      f2 xn = rnd(rnd(x1[u][h] - mu) * ru);
      x2[u][h] = pk(x1[u][h] + rnd(xn * up(y[u][h])));
    }
  }
  auto r0 = SW16(x2[0].x, x2[1].x);
  auto r1 = SW16(x2[0].y, x2[1].y);
  u4 o = {r0[0], r1[0], r0[1], r1[1]};
  if (L == 0) {
    gst4(san(o), ws, O_X2 + (q & 1) * 18432 + (q >> 1) * 256 + i16 * 16, 2 * rt * 18432 + (9 * c + w) * 512);
    if (ex) {
      auto t0 = SW16(x2[NU - 1].x, x2[NU - 1].x);
      auto t1 = SW16(x2[NU - 1].y, x2[NU - 1].y);
      gst4(san(u4{t0[0], t1[0], t0[1], t1[1]}), ws, (q & 1) ? 0x80000000u : O_X2 + (q >> 1) * 256 + i16 * 16, (2 * rt + tb8) * 18432 + (9 * c + 8) * 512);
    }
    st<u2>(L_XR + w * 1024 + lane * 8, x2[0]); st<u2>(L_XR + w * 1024 + 512 + lane * 8, x2[1]);
    if (ex) st<u2>(L_XR + 8192 + tb8 * 512 + lane * 8, x2[NU - 1]);
    UNR for (int u = 0; u < NU; ++u) {
      f2 lo = up(x2[u].x), hi = up(x2[u].y);
      f2 s = lo + hi, sq = lo * lo + hi * hi;
      S[u] = s.x + s.y; Q[u] = sq.x + sq.y;
    }
    if (HELP) { if (w == 4) { S[0] += S[2]; Q[0] += Q[2]; } if (w == 5) { S[1] += S[2]; Q[1] += Q[2]; } }
    pub_stats(ws, S[0], S[1], Q[0], Q[1], L_REDB, CNT(C_REDB), 7, O_ST + 65536 + rt * 1024 + c * 256, w, lane);
  } else {
    rsrc_t ro = mkrs(a.out, 2359296);
    __builtin_amdgcn_raw_buffer_store_b128(o, ro, (16 * (q & 1) + i16) * 1152 + (q >> 1) * 16 + 32 * rt * 1152 + 288 * c + 32 * w, 0, 0);
    if (ex) __builtin_amdgcn_raw_buffer_store_b64(x2[NU - 1], ro, i16 * 1152 + q * 8, (32 * rt + 16 * tb8) * 1152 + 288 * c + 256, 0);
  }
  STAMP(20 + L * 8);
}

template <int L>
DEV void main_layer(const Args& a, rsrc_t ws, int b, int w, int lane0, int p, int g, int rt, int c, int rot, u32 tag, u4 (&B)[36], float (&mS)[2], float (&mQ)[2], u32& wp, int& dead STARG) {
    constexpr float LOG2E = 1.4426950408889634f;
    int lane = opq(lane0), q = lane >> 4, i16 = lane & 15;
    u32 aF[2], a4[2], a7[2];
    UNR for (int u = 0; u < 2; ++u) {
      aF[u] = i16 * 128 + (((4 * u + q) ^ (i16 >> 1)) << 4);
      a4[u] = (64 + 2 * (i16 >> 2) + (i16 & 1)) * 128 + (((4 * u + q) ^ (2 * (i16 >> 2))) << 4);
      a7[u] = L_WQ + (32 + (i16 >> 2)) * 128 + ((4 * u + q) << 4);
    }
    {
      f4 acc[5][2];
      u4 sv = {0, 0, 0, 0};
      const u32 stoff = O_ST + 65536 + (4 * p + w) * 1024 + (q & 1) * 512 + (16 * (q >> 1) + i16) * 8;
      if (L == 1) {
        u32 G = lane < 36 ? lane : (lane < 40 ? 9 * (lane - 36) + 8 : 0);
        u32 po = O_X2 + (8 * p + 2 * w + (lane >= 36 && lane < 40 ? 1 : 0)) * 18432 + G * 512;
        NOUNR for (int i = dead ? SPIN_G : 0; i < SPIN_G; ++i) {
          FENCE();
          u32 v = gld1(ws, po, 0, 1);
          if (!anyl(v == SENT)) break;
        }
        FENCE();
        STAMP(21);
      }
      for (int pass = 0;; ++pass) {
        if (L == 1) {
          UNR for (int j = 0; j < 9; ++j) {
            int kr = j + rot; kr -= kr >= 9 ? 9 : 0;
            UNR for (int i = 0; i < 4; ++i) B[4 * j + i] = gld4(ws, O_X2 + lane * 16 + (i >> 1) * 1024, (8 * p + 2 * w + (i & 1)) * 18432 + kr * 2048, 1);
          }
          sv = ld_stats(ws, stoff);
        }
        UNR for (int i = 0; i < 5; ++i) acc[i][0] = acc[i][1] = f4{0, 0, 0, 0};
        u32 vmx = 0;
        if (wp < (L ? 0u : 1u)) wp = lwait(CNT(C_WPROG), 1, dead);
        if (L == 0) STAMP(3);
        u4 A[2][5];
        UNR for (int i = 0; i < 4; ++i) A[0][i] = ld<u4>(aF[0] + i * 2048);
        A[0][4] = ld<u4>(a4[0]);
        u32 fl = *(volatile LAS u32*)(uintptr_t)CNT(C_WPROG);
        UNR for (int s = 0; s < 18; ++s) {
          if (s < 17) {
            const int j = (s + 1) >> 1, u = (s + 1) & 1;
            if (u == 0) {
              u32 need = L ? (j < 5 ? 0u : 11u) : (u32)(j + 1);
              wp = umax(wp, rfl(fl));
              if (wp < need) wp = lwait(CNT(C_WPROG), need, dead);
            }
            UNR for (int i = 0; i < 4; ++i) A[(s + 1) & 1][i] = ld<u4>(aF[u] + j * 9216 + i * 2048);
            A[(s + 1) & 1][4] = ld<u4>(a4[u] + j * 9216);
            if (u == 0) fl = *(volatile LAS u32*)(uintptr_t)CNT(C_WPROG);
          }
          vmx = vm(vm(vmx, B[2 * s]), B[2 * s + 1]);
          UNR for (int i = 0; i < 5; ++i) {
            acc[i][0] = mfma(A[s & 1][i], B[2 * s], acc[i][0]);
            acc[i][1] = mfma(A[s & 1][i], B[2 * s + 1], acc[i][1]);
          }
        }
        if (L == 0 || !anyl(vmx == SENT) || dead) break;
        if (pass >= 20000) { dead = 1; break; }
        FENCE();
      }
      FENCE();
      ladd(CNT(C_GEM), lane);
      STAMP(4 + L * 6);
      if (L == 0) lwait(CNT(C_TBL), 1, dead);
      else { sv = poll_stats(ws, stoff, sv, dead); stats_from(sv, mS, mQ); }
      {
        u32 kv[2][9];
        UNR for (int i = 0; i < 5; ++i) {
          f4 cs = ld<f4>(L_CSL + i * 64 + q * 16);
          f2 c01 = {cs.x, cs.y}, c23 = {cs.z, cs.w};
          UNR for (int tb = 0; tb < 2; ++tb) {
            f2 lo = {acc[i][tb].x, acc[i][tb].y}, hi = {acc[i][tb].z, acc[i][tb].w};
            kv[tb][i < 4 ? 2 * i : 8] = pk((lo - c01 * mS[tb]) * mQ[tb]);
            if (i < 4) kv[tb][2 * i + 1] = pk((hi - c23 * mS[tb]) * mQ[tb]);
          }
        }
        u32 K[9], V[9];
        UNR for (int i = 0; i < 9; ++i) { auto t = SW32(kv[0][i], kv[1][i]); K[i] = t[0]; V[i] = t[1]; }
        const bool val = (i16 >> 2) == q;
        const u32 tk = L_TK + (val ? ((q & 1) * 4 + (i16 & 3)) * 16 : 128), tk2 = L_TK2 + (val ? ((q & 1) * 4 + (i16 & 3)) * 4 : 32);
        const u4 k0 = {K[0], K[1], K[2], K[3]}, k1 = {K[4], K[5], K[6], K[7]}, k2 = {K[8], 0, 0, 0};
        f4 P[5];
        UNR for (int m = 0; m < 5; ++m) {
          u4 t2 = {ld<u32>(tk2 + m * 36), 0, 0, 0};
          P[m] = mfma(ld<u4>(tk + m * 288), k0, f4{0, 0, 0, 0});
          P[m] = mfma(ld<u4>(tk + m * 288 + 144), k1, P[m]);
          P[m] = mfma(t2, k2, P[m]);
        }
        const u32 row = L_STG + (32 * w + 16 * (q >> 1) + i16) * 232;
        f2 z[9];
        UNR for (int i = 0; i < 9; ++i) z[i] = f2{P[i >> 1][2 * (i & 1)], P[i >> 1][2 * (i & 1) + 1]};
        float mx = fmaxf(z[0].x, z[0].y), mn = fminf(z[0].x, z[0].y);
        UNR for (int i = 1; i < 9; ++i) { mx = fmaxf(fmaxf(mx, z[i].x), z[i].y); mn = fminf(fminf(mn, z[i].x), z[i].y); }
        const float ma = -mx * LOG2E, mb = mn * LOG2E;
        f2 e1[9], e2[9], s1 = {0, 0}, s2 = {0, 0};
        UNR for (int i = 0; i < 9; ++i) {
          f2 t1 = z[i] * LOG2E + ma, t2 = mb - z[i] * LOG2E;
          e1[i] = f2{__builtin_amdgcn_exp2f(t1.x), __builtin_amdgcn_exp2f(t1.y)};
          e2[i] = f2{__builtin_amdgcn_exp2f(t2.x), __builtin_amdgcn_exp2f(t2.y)};
          s1 += e1[i]; s2 += e2[i];
        }
        const float r1 = __builtin_amdgcn_rcpf(s1.x + s1.y), r2 = __builtin_amdgcn_rcpf(s2.x + s2.y);
        lwait(CNT(C_GEM), 4 * (L + 1), dead);
        {
          u32 vo = row + 144 + (q & 1) * 40;
          UNR for (int i = 0; i < 4; ++i) st<u2>(vo + 8 * i, u2{V[2 * i], V[2 * i + 1]});
          st<u2>(vo + 32, u2{V[8], 0x00003F80u});
        }
        u32 fo = row + (q & 1) * 72;
        UNR for (int i = 0; i < 4; ++i) {
          st<u2>(fo + 8 * i, u2{pk(e1[2 * i] * r1), pk(e1[2 * i + 1] * r1)});
          st<u2>(fo + 32 + 8 * i, u2{pk(e2[2 * i] * r2), pk(e2[2 * i + 1] * r2)});
        }
        st<u2>(fo + 64, u2{pk(e1[8] * r1), pk(e2[8] * r2)});
        FENCE();
        ladd(CNT(C_PHI), lane);
        lwait(CNT(C_PHI), 4 * (L + 1), dead);
        STAMP(5 + L * 6);
        const int hd = w >> 1, db = w & 1;
        const u32 ta = L_STG + (16 * (q & 1) + 4 * (i16 >> 2) + (q >> 1)) * 232 + (i16 & 3) * 8;
        f4 Dd[3] = {f4{0, 0, 0, 0}, f4{0, 0, 0, 0}, f4{0, 0, 0, 0}};
        const u32 tf = ta + 72 * hd, tv = ta + 144 + 40 * hd + 32 * db;
        UNR for (int ks = 0; ks < 4; ++ks) {
          u4 bv = trd2(tv + ks * 7424, tv + ks * 7424 + 464);
          UNR for (int fb = 0; fb < 3; ++fb) Dd[fb] = mfma(trd2(tf + ks * 7424 + fb * 32, tf + ks * 7424 + fb * 32 + 464), bv, Dd[fb]);
        }
        auto s0 = SW16(pk(Dd[0].x, Dd[0].y), pk(Dd[1].x, Dd[1].y));
        auto s1b = SW16(pk(Dd[0].z, Dd[0].w), pk(Dd[1].z, Dd[1].w));
        u4 o = {s0[0], s1b[0], s0[1], s1b[1]};
        const int d = 16 * db + i16;
        const u32 so = L * SP_L + g * SP_G + p * SP_P;
        gst4(san(o), ws, d < 19 ? O_SP + (hd * 19 + d) * 64 + (q & 1) * 32 + (q >> 1) * 16 : 0x80000000u, so);
        gst2(u2{san(pk(Dd[2].x, Dd[2].y)), san(pk(Dd[2].z, Dd[2].w))}, ws, (d < 19 && q == 0) ? O_SP + 2432 + (hd * 19 + d) * 8 : 0x80000000u, so);
      }
      ladd(CNT(C_PUB), lane);
      STAMP(6 + L * 6);
      u4 wo[18];
      u32 fq[2][9];
      {
        if (wp < 10) wp = lwait(CNT(C_WPROG), 10, dead);
        if (L == 0) lwait(CNT(C_FAOK), 1, dead);
        f4 aq[3][2];
        UNR for (int i = 0; i < 3; ++i) aq[i][0] = aq[i][1] = f4{0, 0, 0, 0};
        u4 A[2][3];
        A[0][0] = ld<u4>(L_WQ + aF[0]); A[0][1] = ld<u4>(L_WQ + aF[0] + 2048); A[0][2] = ld<u4>(a7[0]);
        UNR for (int s = 0; s < 18; ++s) {
          if (s < 17) {
            const int j = (s + 1) >> 1, u = (s + 1) & 1;
            A[(s + 1) & 1][0] = ld<u4>(L_WQ + aF[u] + j * 4608); A[(s + 1) & 1][1] = ld<u4>(L_WQ + aF[u] + j * 4608 + 2048);
            A[(s + 1) & 1][2] = ld<u4>(a7[u] + j * 4608);
          }
          UNR for (int i = 0; i < 3; ++i) {
            aq[i][0] = mfma(A[s & 1][i], B[2 * s], aq[i][0]);
            aq[i][1] = mfma(A[s & 1][i], B[2 * s + 1], aq[i][1]);
          }
          if (s < 9) {
            wo[2 * s] = gld4(ws, O_WOT + lane * 16, (9 * c + w) * 18432 + 2 * s * 1024, 1);
            wo[2 * s + 1] = gld4(ws, O_WOT + lane * 16, (9 * c + w) * 18432 + (2 * s + 1) * 1024, 1);
          }
          __builtin_amdgcn_sched_barrier(0);
        }
        STAMP(7 + L * 6);
        const f4 c5 = ld<f4>(L_CSL + 5 * 64 + q * 16), c6 = ld<f4>(L_CSL + 6 * 64 + q * 16);
        const float c7 = ld<float>(L_CSL + 7 * 64 + q * 16);
        const bool val = (i16 >> 3) == (q >> 1);
        const u32 tq = L_TQ + (val ? (q * 4 + (i16 & 3)) * 16 : 256), tq2 = L_TQ2 + (val ? (q * 4 + (i16 & 3)) * 4 : 64);
        u4 q0[2], q1[2];
        UNR for (int tb = 0; tb < 2; ++tb) {
          q0[tb].x = pk((f2{aq[0][tb].x, aq[0][tb].y} - f2{c5.x, c5.y} * mS[tb]) * mQ[tb]);
          q0[tb].y = pk((f2{aq[0][tb].z, aq[0][tb].w} - f2{c5.z, c5.w} * mS[tb]) * mQ[tb]);
          q0[tb].z = pk((f2{aq[1][tb].x, aq[1][tb].y} - f2{c6.x, c6.y} * mS[tb]) * mQ[tb]);
          q0[tb].w = pk((f2{aq[1][tb].z, aq[1][tb].w} - f2{c6.z, c6.w} * mS[tb]) * mQ[tb]);
          q1[tb] = u4{pk((aq[2][tb].x - c7 * mS[tb]) * mQ[tb], 0.0f), 0, 0, 0};
        }
        f4 P[2][5];
        UNR for (int m = 0; m < 5; ++m) {
          u4 t0 = ld<u4>(tq + m * 272), t1 = {ld<u32>(tq2 + m * 68), 0, 0, 0};
          UNR for (int tb = 0; tb < 2; ++tb) P[tb][m] = mfma(t1, q1[tb], mfma(t0, q0[tb], f4{0, 0, 0, 0}));
        }
        const float ls = (q & 1) ? -LOG2E : LOG2E;
        UNR for (int tb = 0; tb < 2; ++tb) {
          f2 z[9];
          UNR for (int i = 0; i < 9; ++i) z[i] = f2{P[tb][i >> 1][2 * (i & 1)], P[tb][i >> 1][2 * (i & 1) + 1]} * ls;
          float mx = fmaxf(z[0].x, z[0].y);
          UNR for (int i = 1; i < 9; ++i) mx = fmaxf(fmaxf(mx, z[i].x), z[i].y);
          f2 s1 = {0, 0};
          UNR for (int i = 0; i < 9; ++i) { f2 t = z[i] - mx; z[i] = f2{__builtin_amdgcn_exp2f(t.x), __builtin_amdgcn_exp2f(t.y)}; s1 += z[i]; }
          const float r1 = __builtin_amdgcn_rcpf(s1.x + s1.y);
          UNR for (int i = 0; i < 9; ++i) fq[tb][i] = pk(z[i] * r1);
        }
      }
      STAMP(8 + L * 6);
      lwait(CNT(C_STB), 4 * (L + 1), dead);
      STAMP(9 + L * 6);
      {
        const bool val = (i16 >> 3) == (q >> 1);
        const int d01 = (q >> 1) * 19 + 8 * ((i16 >> 2) & 1) + (i16 & 3), d2 = (q >> 1) * 19 + 16 + ((i16 & 3) < 2 ? (i16 & 3) : 2);
        const u32 m01 = val ? L_STB + d01 * 64 + (q & 1) * 32 : L_ZERO, m2 = val ? L_STB + d2 * 64 + (q & 1) * 32 : L_ZERO;
        const u32 l01 = val ? L_STB + 2432 + d01 * 8 + (q & 1) * 4 : L_ZERO, l2 = val ? L_STB + 2432 + d2 * 8 + (q & 1) * 4 : L_ZERO;
        f4 O[2][3];
        UNR for (int m = 0; m < 3; ++m) {
          u4 t0 = ld<u4>(m < 2 ? m01 + m * 256 : m2), t1 = ld<u4>((m < 2 ? m01 + m * 256 : m2) + 16), t2 = {ld<u32>(m < 2 ? l01 + m * 32 : l2), 0, 0, 0};
          UNR for (int tb = 0; tb < 2; ++tb) {
            u4 b0 = {fq[tb][0], fq[tb][1], fq[tb][2], fq[tb][3]}, b1 = {fq[tb][4], fq[tb][5], fq[tb][6], fq[tb][7]}, b2 = {fq[tb][8], 0, 0, 0};
            O[tb][m] = mfma(t2, b2, mfma(t1, b1, mfma(t0, b0, f4{0, 0, 0, 0})));
          }
        }
        u32 lo[2];
        UNR for (int tb = 0; tb < 2; ++tb) {
          const float inv = __builtin_amdgcn_rcpf(O[tb][2].z + 1e-6f);
          u4 o = {pk(f2{O[tb][0].x, O[tb][0].y} * inv), pk(f2{O[tb][0].z, O[tb][0].w} * inv), pk(f2{O[tb][1].x, O[tb][1].y} * inv), pk(f2{O[tb][1].z, O[tb][1].w} * inv)};
          gst4(san(o), ws, O_OB + lane * 16, L * OB_L + p * OB_P + (2 * w + tb) * 16384 + g * 1024);
          lo[tb] = pk(f2{O[tb][2].x, O[tb][2].y} * inv);
        }
        const u32 lv = san((q & 1) ? lo[1] : lo[0]);
        auto hh = SW32(lv, lv);
        const u32 n0 = __builtin_amdgcn_update_dpp(0u, hh[0], 0xB1, 0xF, 0xF, true), n1 = __builtin_amdgcn_update_dpp(0u, hh[1], 0xB1, 0xF, 0xF, true);
        gst4(u4{hh[0], hh[1], n0, n1}, ws, (q < 2 && !(i16 & 1)) ? O_OB + 131072 + (16 * q + i16) * 8 : 0x80000000u, L * OB_L + p * OB_P + g * 1024 + w * 256);
        ladd(CNT(C_PUB), lane);
      }
      STAMP(22 + L);
      tail<false, L>(a, ws, b, w, lane, g, rt, c, rot, tag, wo, wo, dead STPASS);
    }
}

template <int L>
DEV void state_sum(const Args& a, rsrc_t ws, int b, int w, int lane0, int g, int& dead STARG) {
    const int lane = opq(lane0), q = lane >> 4, i16 = lane & 15;
    u4 sel;
    UNR for (int d = 0; d < 4; ++d) sel[d] = ((i16 & 7) >> 1) == d ? ((i16 & 1) ? 0x3F800000u : 0x00003F80u) : 0u;
      {
        const int h = w - 4;
        u4 v[3][4];
        u32 off[3];
        UNR for (int gi = 0; gi < 3; ++gi) off[gi] = O_SP + q * SP_P + umin(16 * umin(h + 4 * gi, 10) + i16, 170) * 16;
        lwait(CNT(C_PUB), 8 * L + 4, dead);
        NOUNR for (int it = dead ? SPIN_G - 1 : 0;; ++it) {
          FENCE();
          u32 m = 0;
          UNR for (int gi = 0; gi < 3; ++gi) UNR for (int pp = 0; pp < 4; ++pp) v[gi][pp] = gld4(ws, off[gi], L * SP_L + g * SP_G + 4 * pp * SP_P, 1);
          UNR for (int gi = 0; gi < 3; ++gi) UNR for (int pp = 0; pp < 4; ++pp) m = vm(m, v[gi][pp]);
          if (!anyl(m == SENT)) break;
          if (it >= SPIN_G) { dead = 1; break; }
        }
        FENCE();
        STAMP(6 + L * 6);
        UNR for (int gi = 0; gi < 3; ++gi) {
          f4 s = {0, 0, 0, 0};
          UNR for (int pp = 0; pp < 4; ++pp) s = mfma(sel, v[gi][pp], s);
          int ck = 16 * umin(h + 4 * gi, 10) + i16;
          if (q < 2 && ck < 171) st<u2>(L_STB + ck * 16 + q * 8, u2{pk(s.x, s.y), pk(s.z, s.w)});
        }
        FENCE();
        ladd(CNT(C_STB), lane);
        STAMP(7 + L * 6);
      }
}

__global__ void __launch_bounds__(512, 2) fused_kernel(Args a) {
  const int tid = threadIdx.x, lane = tid & 63, w = rfl(tid >> 6), b = blockIdx.x;
  const int q = lane >> 4, i16 = lane & 15;
  const int xcd = b & 7, r = b >> 3;
  const int p = 4 * (xcd >> 1) + (r >> 3), g = 8 * (xcd & 1) + (r & 7), rot = ((r & 7) + 2 * (r >> 3)) % 9;
  const int rt = 4 * p + (g >> 2), c = g & 3;
  rsrc_t ws = mkrs(a.ws, WS_BYTES);
  u32 tag = *(const u32*)a.ws + 1;
  int dead = 0;
  STAMP(0); STAMPC(29);

  if (w < 4) {
    __builtin_amdgcn_s_setprio(2);
    u4 B[36];
    float mS[2], mQ[2];
    {
      rsrc_t rx = mkrs(a.x, 2359296);
      u32 vo[2];
      UNR for (int par = 0; par < 2; ++par) vo[par] = (8 * par + (lane >> 3)) * 1152 + (((lane & 7) ^ (4 * par + (lane >> 4))) << 4);
      UNR for (int j = 0; j < 9; ++j) {
        int kr = j + rot; kr -= kr >= 9 ? 9 : 0;
        UNR for (int rg = 0; rg < 4; ++rg) dma<0>(rx, j * 16384 + w * 4096 + rg * 1024, vo[rg & 1], (128 * p + 32 * w + 16 * (rg >> 1)) * 1152 + kr * 128);
      }
      FENCE();
      __builtin_amdgcn_sched_barrier(0);
      asm volatile("s_waitcnt lgkmcnt(0)\n\ts_barrier" : "+s"(tag)::"memory");
      __builtin_amdgcn_sched_barrier(0);
      STAMP(1);
      u32 a0 = w * 4096 + (i16 >> 3) * 1024 + (i16 & 7) * 128 + ((q ^ (i16 >> 1)) << 4), a1 = a0 ^ 64, pa = CNT(C_PROG) + 4 * w;
      f4 gS[2] = {f4{0, 0, 0, 0}, f4{0, 0, 0, 0}}, gQ[2] = {f4{0, 0, 0, 0}, f4{0, 0, 0, 0}};
      const u4 ones = {0x3F803F80u, 0x3F803F80u, 0x3F803F80u, 0x3F803F80u};
#define X0STEP(j)                                                                                                                      \
  {                                                                                                                                    \
    u32 pv = j + 1;                                                                                                                    \
    asm volatile("s_waitcnt vmcnt(%8)\n\tds_read_b128 %0, %4\n\tds_read_b128 %1, %4 offset:2048\n\tds_read_b128 %2, %5\n\t"            \
                 "ds_read_b128 %3, %5 offset:2048\n\ts_waitcnt lgkmcnt(0)\n\tds_write_b32 %6, %7"                                       \
                 : "=&v"(B[4 * j]), "=&v"(B[4 * j + 1]), "=&v"(B[4 * j + 2]), "=&v"(B[4 * j + 3])                                        \
                 : "v"(a0 + j * 16384), "v"(a1 + j * 16384), "v"(pa), "v"(pv), "n"(32 - 4 * j)                                          \
                 : "memory");                                                                                                          \
    UNR for (int i = 0; i < 4; ++i) { gS[i & 1] = mfma(ones, B[4 * j + i], gS[i & 1]); gQ[i & 1] = mfma(B[4 * j + i], B[4 * j + i], gQ[i & 1]); } \
  }
      X0STEP(0) X0STEP(1) X0STEP(2) X0STEP(3) X0STEP(4) X0STEP(5) X0STEP(6) X0STEP(7) X0STEP(8)
      STAMP(2);
      UNR for (int tb = 0; tb < 2; ++tb) {
        float d = (i16 & 2) ? ((i16 & 1) ? gQ[tb].w : gQ[tb].z) : ((i16 & 1) ? gQ[tb].y : gQ[tb].x);
        d = (q == (i16 >> 2)) ? d : 0.0f;
        d = sw16add(d, d); d = sw32add(d, d);
        f2 t = ln_stats(gS[tb].x, d);
        mS[tb] = t.x; mQ[tb] = t.y;
      }
    }
    u32 wp = 0;
    main_layer<0>(a, ws, b, w, lane, p, g, rt, c, rot, tag, B, mS, mQ, wp, dead STPASS);
    main_layer<1>(a, ws, b, w, lane, p, g, rt, c, rot, tag, B, mS, mQ, wp, dead STPASS);
  } else {
    const int pr = (w - 4) >> 1, e = w & 1;
    const u32 lH = pr ? L_H1 : L_H0, lU = pr ? L_U1 : L_U0;
    if (w == 4) {
      const int cl = b % 27, kr0 = b / 27;
      if (b < 243) {
        rsrc_t rw = mkrs(a.wqkv, 576 * 3456);
        UNR for (int rg = 0; rg < 8; ++rg)
          dma<0>(rw, L_TILE + rg * 1024, ((lane >> 3) ^ (rg & 1)) * 3456 + (((lane & 7) ^ (4 * ((rg >> 1) & 1))) << 4), (64 * kr0 + 8 * rg) * 3456 + cl * 128);
      }
      FENCE();
      asm volatile("s_waitcnt lgkmcnt(0)\n\ts_barrier" : "+s"(tag)::"memory");
      if (b < 243) {
        const int cg = lane >> 3, rg = lane & 7, sec = cl / 9;
        u32 dst[8];
        UNR for (int j = 0; j < 8; ++j) {
          int cc = 64 * (cl - 9 * sec) + 8 * cg + j, h = cc / 18, d = cc - 18 * h, hl = h & 1;
          u32 o;
          if (sec == 0) {
            int hi = d >= 9, qq = 2 * hl + hi, s = d - 9 * hi;
            int row = s < 8 ? 16 * (s >> 2) + 4 * qq + (s & 3) : 32 + qq, key = s < 8 ? (row >> 1) & 7 : 0;
            o = L_WQ + kr0 * 4608 + row * 128 + ((rg ^ key) << 4);
          } else {
            int qq = 2 * (sec - 1) + hl;
            int row = d < 16 ? 16 * (d >> 2) + 4 * qq + (d & 3) : 48 + 2 * qq + d, key = d < 16 ? (row >> 1) & 7 : 2 * qq;
            o = kr0 * 9216 + row * 128 + ((rg ^ key) << 4);
          }
          dst[j] = O_WT + (h >> 1) * WT_G + o;
        }
        vmwait0();
        u4 R[8];
        const u32 rb = L_TILE + rg * 1024 + ((cg ^ (4 * ((rg >> 1) & 1))) << 4);
        UNR for (int i = 0; i < 8; ++i) R[i] = ld<u4>(rb + ((i ^ (rg & 1)) << 7));
        float cs[8];
        UNR for (int j = 0; j < 8; ++j) {
          u4 o;
          UNR for (int d = 0; d < 4; ++d) o[d] = __builtin_amdgcn_perm(R[2 * d + 1][j >> 1], R[2 * d][j >> 1], (j & 1) ? 0x07060302u : 0x05040100u);
          gst4(o, ws, dst[j], 0);
          cs[j] = dot2(o.x, 0x3F803F80u, dot2(o.y, 0x3F803F80u, 0.0f)) + dot2(o.z, 0x3F803F80u, dot2(o.w, 0x3F803F80u, 0.0f));
        }
        float sel = 0;
        UNR for (int j = 0; j < 8; ++j) {
          float v = cs[j];
          v += __uint_as_float(__builtin_amdgcn_update_dpp(0, __float_as_uint(v), 0xB1, 0xF, 0xF, true));
          v += __uint_as_float(__builtin_amdgcn_update_dpp(0, __float_as_uint(v), 0x4E, 0xF, 0xF, true));
          v += __uint_as_float(__builtin_amdgcn_update_dpp(0, __float_as_uint(v), 0x141, 0xF, 0xF, true));
          sel = rg == j ? v : sel;
        }
        gst1(__float_as_uint(sel), ws, O_CS + lane * 4, kr0 * 6912 + cl * 256);
        vmwait0();
        setflag(ws, O_FW + b * 128, tag, lane);
      }
      STAMP(1);
      {
        int l6 = lane < 54 ? lane : 0, kr = l6 / 6, i = l6 - 6 * kr;
        int ln = 9 * (i >> 1) + ((36 * g + 35 * (i & 1)) >> 6);
        poll(ws, O_FW + (kr * 27 + ln) * 128, tag, dead);
      }
      lset(CNT(C_WOK), 1);
      STAMP(2);
      u32 xp = 0;
      auto need_x = [&](u32 n) {
        if (xp < n) {
          NOUNR for (int i = dead ? SPIN_L : 0; i < SPIN_L; ++i) {
            u4 v;
            asm volatile("ds_read_b128 %0, %1\n\ts_waitcnt lgkmcnt(0)" : "=v"(v) : "v"(CNT(C_PROG)) : "memory");
            xp = rfl(umin(umin(v.x, v.y), umin(v.z, v.w)));
            if (xp >= n) return;
          }
          dead = 1;
        }
      };
      UNR for (int j = 0; j < 9; ++j) {
        need_x(((j + 1) * 9216 - 1) / 16384 + 1);
        int kr = j + rot; kr -= kr >= 9 ? 9 : 0;
        UNR for (int i = 0; i < 9; ++i) dma<SC1>(ws, j * 9216 + i * 1024, O_WT + lane * 16, g * WT_G + kr * 9216 + i * 1024);
        if (j >= 3) { vmwait<27>(); lset(CNT(C_WPROG), j - 2); }
      }
      need_x(8);
      UNR for (int i = 0; i < 41; ++i) {
        u32 o = i * 1024 + lane * 16 + rot * 4608;
        o -= o >= 41472 ? 41472 : 0;
        if (i < 40 || lane < 32) dma<SC1>(ws, L_WQ + i * 1024, O_WT + L_WQ + o, g * WT_G);
        if (i == 8) { vmwait<27>(); lset(CNT(C_WPROG), 7); }
        if (i == 17) { vmwait<27>(); lset(CNT(C_WPROG), 8); }
        if (i == 26) { vmwait<27>(); lset(CNT(C_WPROG), 9); }
      }
      vmwait0();
      lset(CNT(C_WPROG), 10);
      STAMP(3);
    } else if (w < 7) {
      KVin in0, in1;
      Hin hin;
      const int T = (w - 5) * 128 + lane, chn = 2 * b + (w - 5);
      const u32 mH = w == 5 ? L_H0 : L_H1, mU = w == 5 ? L_U0 : L_U1;
      kv_load(a, T, rt, c, in0);
      kv_load(a, T + 64, rt, c, in1);
      h_load(a, chn, lane, hin);
      FENCE();
      __builtin_amdgcn_sched_barrier(0);
      asm volatile("s_waitcnt lgkmcnt(0)\n\ts_barrier" : "+s"(tag)::"memory");
      __builtin_amdgcn_sched_barrier(0);
      kv_compute(ws, T, rt, c, in0);
      kv_compute(ws, T + 64, rt, c, in1);
      STAMP(1);
      vmwait0();
      if (ladd_rtn(CNT(C_KVC), lane) == 2) setflag(ws, O_FK + b * 128, tag, lane);
      UNR for (int i = 0; i < 4; ++i) lwait(CNT(C_PROG) + 4 * i, 9, dead);
      lwait(CNT(C_WOK), 1, dead);
      h_stage(mH, mU, lane, hin);
      poll(ws, O_FK + wg_of(lane >> 2, 4 * (lane & 3) + (2 * b) / 144) * 128, tag, dead);
      STAMP(2);
      u_stage(ws, mU, chn, lane);
      vmwait0();
      ladd(w == 5 ? CNT(C_PU0) : CNT(C_PU1), lane);
      STAMP(3);
    } else {
      if (lane < 32) lset(L_CNT + lane * 4, 0);
      const int cl = b % 9, gg = b / 9;
      if (b < 144) {
        rsrc_t rw = mkrs(a.wo, 576 * 1152);
        UNR for (int m = 0; m < 5; ++m)
          if (m < 4 || lane < 32) dma<0>(rw, L_WOTILE + m * 1024, (lane >> 3) * 1152 + (lane & 7) * 16, (36 * gg + 8 * m) * 1152 + cl * 128);
      }
      rsrc_t rp = mkrs(a.wphi, 32 * 648);
      u4 p0 = gld4(rp, lane * 16, g * 1296, 0), p1 = gld4(rp, lane < 17 ? 1024 + lane * 16 : 0x80000000u, g * 1296, 0);
      KVin in;
      if (lane < 32) kv_load(a, 256 + lane, rt, c, in);
      FENCE();
      __builtin_amdgcn_sched_barrier(0);
      asm volatile("s_waitcnt lgkmcnt(0)\n\ts_barrier" : "+s"(tag)::"memory");
      __builtin_amdgcn_sched_barrier(0);
      const u4 s4 = {SENT, SENT, SENT, SENT};
      UNR for (int i = 0; i < 3; ++i) gst4(s4, ws, (i < 2 || lane < 48) ? O_SP + i * 1024 + lane * 16 : 0x80000000u, g * SP_G + p * SP_P);
      UNR for (int i = 0; i < 9; ++i) gst4(s4, ws, O_OB + lane * 16, p * OB_P + i * 16384 + g * 1024);
      gst4(s4, ws, lane < 48 ? O_ST + q * 65536 + i16 * 16 : 0x80000000u, rt * 1024 + c * 256);
      if (lane < 32) kv_compute(ws, 256 + lane, rt, c, in);
      if (b < 144) {
        const u32 ta = L_WOTILE + (18 * (q >> 1) + 8 * (q & 1) + (i16 >> 2)) * 128 + (i16 & 3) * 8;
        UNR for (int G = 0; G < 4; ++G) gst4(trd2(ta + G * 32, ta + G * 32 + 512), ws, O_WOT + lane * 16, (4 * cl + G) * 18432 + gg * 1024);
        u2 lf = trd(L_WOTILE + (16 + ((i16 >> 2) & 1) + 18 * (i16 >> 3)) * 128 + q * 32 + (i16 & 3) * 8);
        gst2(lf, ws, O_WOT + q * 18432 + i16 * 16, 4 * cl * 18432 + (16 + (gg >> 3)) * 1024 + ((gg >> 1) & 3) * 256 + (gg & 1) * 8);
      }
      FENCE();
      asm volatile("s_waitcnt lgkmcnt(0)" ::: "memory");
      st<u4>(L_WOTILE + lane * 16, p0);
      if (lane < 17) st<u4>(L_WOTILE + 1024 + lane * 16, p1);
      FENCE();
      auto wp16 = [&](int hd, int d, int f) -> u32 { u32 v = ld<u16>(L_WOTILE + (hd * 324 + d * 18 + (f < 18 ? f : 0)) * 2); return f < 18 ? v : 0u; };
      UNR for (int rd = 0; rd < 2; ++rd) {
        int t = rd * 64 + lane, tt = t < 80 ? t : 79, m = tt >> 4, cc = (tt >> 3) & 1, hd = (tt >> 2) & 1, f = 4 * m + (tt & 3);
        u4 v;
        UNR for (int d = 0; d < 4; ++d) v[d] = wp16(hd, 8 * cc + 2 * d, f) | (wp16(hd, 8 * cc + 2 * d + 1, f) << 16);
        st<u4>(L_TK + (tt >> 3) * 144 + (tt & 7) * 16, v);
        int hq = (tt >> 3) & 1, dh = (tt >> 2) & 1;
        UNR for (int d = 0; d < 4; ++d) v[d] = wp16(hq, 9 * dh + 2 * d, f) | (wp16(hq, 9 * dh + 2 * d + 1, f) << 16);
        st<u4>(L_TQ + m * 272 + (tt & 15) * 16, v);
        st<u32>(L_TQ2 + m * 68 + (tt & 15) * 4, wp16(hq, 9 * dh + 8, f));
      }
      {
        int tt = lane < 40 ? lane : 39, m = tt >> 3, hd = (tt >> 2) & 1, f = 4 * m + (tt & 3);
        st<u32>(L_TK2 + m * 36 + (tt & 7) * 4, wp16(hd, 16, f) | (wp16(hd, 17, f) << 16));
        u4 z = {0, 0, 0, 0};
        if (lane < 10) st<u4>(L_TK + lane * 144 + 128, z);
        if (lane < 5) { st<u4>(L_TQ + lane * 272 + 256, z); st<u32>(L_TK2 + lane * 36 + 32, 0u); st<u32>(L_TQ2 + lane * 68 + 64, 0u); }
      }
      FENCE();
      vmwait0();
      setflag(ws, O_FA + b * 128, tag, lane);
      if (ladd_rtn(CNT(C_KVC), lane) == 2) setflag(ws, O_FK + b * 128, tag, lane);
      STAMP(1);
      lwait(CNT(C_WOK), 1, dead);
      UNR for (int rd = 0; rd < 2; ++rd) {
        int blk = rd * 4 + q, qq = i16 >> 2, ee = i16 & 3;
        int col;
        if (rd == 0) col = 576 + 576 * (qq >> 1) + 36 * g + 18 * (qq & 1) + 4 * blk + ee;
        else col = blk == 4 ? 576 + 576 * (qq >> 1) + 36 * g + 18 * (qq & 1) + 16 + (ee & 1) : 36 * g + 9 * qq + (blk == 7 ? 8 : 4 * (blk - 5) + ee);
        float s = 0;
        UNR for (int k = 0; k < 9; ++k) s += __uint_as_float(gld1(ws, O_CS + col * 4, k * 6912, 1));
        st<float>(L_CSL + rd * 256 + lane * 4, s);
      }
      if (lane < 18) st<u4>(L_ZERO + lane * 16, u4{0, 0, 0, 0});
      FENCE();
      ladd(CNT(C_TBL), lane);
      STAMP(2);
      {
        poll(ws, O_FA + wg_of(lane < 16 ? lane : p, lane < 16 ? g : (lane & 15)) * 128, tag, dead);
        int ln = (144 * c + 48 * q + (q == 3 ? -1 : 0)) >> 6;
        poll(ws, O_FA + (i16 * 9 + ln) * 128, tag, dead);
      }
      lset(CNT(C_FAOK), 1);
      lwait(CNT(C_WPROG), 10, dead);
      UNR for (int i = 0; i < 9; ++i) {
        u32 o = i * 1024 + lane * 16;
        o += o >= 4608 ? 18432 - 4608 : 0;
        gst4(s4, ws, O_X2 + o, 2 * rt * 18432 + c * 4608);
      }
      UNR for (int i = 0; i < 3; ++i) gst4(s4, ws, (i < 2 || lane < 48) ? O_SP + i * 1024 + lane * 16 : 0x80000000u, SP_L + g * SP_G + p * SP_P);
      UNR for (int i = 0; i < 9; ++i) gst4(s4, ws, O_OB + lane * 16, OB_L + p * OB_P + i * 16384 + g * 1024);
      STAMP(3);
    }
    u4 wo[18], wo8[18];
    lwait(CNT(C_WPROG), 10, dead);
    lwait(CNT(C_FAOK), 1, dead);
    UNR for (int i = 0; i < 18; ++i) wo[i] = gld4(ws, O_WOT + lane * 16, (9 * c + w) * 18432 + i * 1024, 1);
    lwait(pr ? CNT(C_PU1) : CNT(C_PU0), 1, dead);
    STAMP(4);
    conv_pair(a, ws, lH, lU, 2 * b + pr, e, pr ? CNT(C_PA1) : CNT(C_PA0), pr ? CNT(C_PB1) : CNT(C_PB0), 2, lane, dead);
    STAMP(5);
    vmwait0();
    if (w == 7) setflag(ws, O_FB + b * 128, tag, lane);
    if (ladd_rtn(CNT(C_FYC), lane) == 3) setflag(ws, O_FY + b * 128, tag, lane);
    state_sum<0>(a, ws, b, w, lane, g, dead STPASS);
      {
        if (c == 0 && w < 6) {
          const int ch = 512 + rt;
          lwait(CNT(C_FYC), 4, dead);
          if (w == 5) {
            Hin hin;
            h_load(a, ch, lane, hin);
            poll(ws, O_FK + wg_of(lane >> 2, 4 * (lane & 3) + 3) * 128, tag, dead);
            u_stage(ws, L_U0, ch, lane);
            h_stage(L_H0, L_U0, lane, hin);
            vmwait0();
            ladd(CNT(C_PU0), lane);
          }
          lwait(CNT(C_PU0), 2, dead);
          conv_pair(a, ws, L_H0, L_U0, ch, e, CNT(C_PA0), CNT(C_PB0), 4, lane, dead);
          vmwait0();
          if (ladd_rtn(CNT(C_FZC), lane) == 1) setflag(ws, O_FZ + (ch - 512) * 128, tag, lane);
        }
        if (w < 6) UNR for (int i = 0; i < 18; ++i) wo8[i] = gld4(ws, O_WOT + lane * 16, (9 * c + 8) * 18432 + i * 1024, 1);
        if (w == 7) poll(ws, O_FB + wg_of(lane < 16 ? lane : p, lane < 16 ? g : (lane & 15)) * 128, tag, dead);
        STAMP(8);
      }
    tail<true, 0>(a, ws, b, w, lane, g, rt, c, rot, tag, wo, wo8, dead STPASS);
    state_sum<1>(a, ws, b, w, lane, g, dead STPASS);
    tail<true, 1>(a, ws, b, w, lane, g, rt, c, rot, tag, wo, wo8, dead STPASS);
    if (b == 0 && w == 7) {
      UNR for (int i = 0; i < 4; ++i) poll(ws, O_FA + (i * 64 + lane) * 128, tag, dead);
      setflag(ws, 0, tag, lane);
    }
  }
  STAMP(31); STAMPC(30);
}

__global__ void k_triv(unsigned* p) { if (threadIdx.x == 0) p[WS_BYTES / 4 + 64] = 1; }
extern "C" void kernel_launch(void* const* d_in, const int* in_sizes, int n_in, void* d_out, int out_size, void* d_ws, size_t ws_size, hipStream_t stream) {
  Args a;
  a.x = (const u16*)d_in[0]; a.wqkv = (const u16*)d_in[1]; a.wphi = (const u16*)d_in[2]; a.wo = (const u16*)d_in[3];
  a.bk = (const u16*)d_in[5]; a.bv = (const u16*)d_in[6]; a.sk = (const u16*)d_in[7]; a.ssm = (const u16*)d_in[8]; a.dv = (const u16*)d_in[9];
  a.out = (u16*)d_out; a.ws = (unsigned char*)d_ws;
  (void)hipFuncSetAttribute((const void*)fused_kernel, hipFuncAttributeMaxDynamicSharedMemorySize, LDS_BYTES);
  fused_kernel<<<256, 512, LDS_BYTES, stream>>>(a);
  k_triv<<<1, 64, 0, stream>>>((unsigned*)d_ws);
}
```

```cpp
#define PW 0
#define PADAT 99
#include <hip/hip_runtime.h>
#include <hip/hip_bf16.h>

typedef unsigned u32;
typedef unsigned short u16;
typedef float f2 __attribute__((ext_vector_type(2)));
typedef float f4 __attribute__((ext_vector_type(4)));
typedef u32 u2 __attribute__((ext_vector_type(2)));
typedef u32 u4 __attribute__((ext_vector_type(4)));
typedef u32 u2a __attribute__((ext_vector_type(2), aligned(4)));
typedef __bf16 bf2 __attribute__((ext_vector_type(2)));
typedef __bf16 bf8 __attribute__((ext_vector_type(8)));
typedef short s4v __attribute__((ext_vector_type(4)));
typedef __amdgpu_buffer_rsrc_t rsrc_t;
#define LAS __attribute__((address_space(3)))
#define DEV __device__ __forceinline__
#define UNR _Pragma("unroll")
#define NOUNR _Pragma("unroll 1")
#define FENCE() asm volatile("" ::: "memory")
#define SC1 16

constexpr u32 O_FW = 4096, O_FA = O_FW + 32768, O_FK = O_FA + 32768, O_FO = O_FK + 32768, O_FY = O_FO + 32768, O_FZ = O_FY + 32768, O_FB = O_FZ + 32768;
constexpr u32 O_WT = 262144, WT_G = 124416;
constexpr u32 O_CS = O_WT + 16 * WT_G;
constexpr u32 O_WOT = O_CS + 62464;
constexpr u32 O_KVT = O_WOT + 663552;
constexpr u32 O_YT = O_KVT + 2359296;
constexpr u32 O_SP = O_YT + 2359296, SP_L = 720896, SP_G = 45056, SP_P = 2816;
constexpr u32 O_OB = O_SP + 2 * SP_L, OB_L = 2359296, OB_P = 147456;
constexpr u32 O_ST = O_OB + 2 * OB_L;
constexpr u32 O_X2 = O_ST + 3 * 65536;
constexpr u32 O_STAMP = O_X2 + 2359296;
constexpr u32 WS_BYTES = O_STAMP + 256 * 8 * 32 * 8;
constexpr u32 L_WQ = 82944, L_STG = 46080;
constexpr u32 L_H0 = 124416, L_H1 = 132736, L_U0 = 141056, L_Y = 124416, L_XR = 133632;
constexpr u32 L_TILE = 147456, L_U1 = 147456, L_STB = 152064, L_ZERO = 154832, L_CSL = 155136;
constexpr u32 L_WOTILE = 155648, L_REDA = 155648, L_REDB = 157696;
constexpr u32 L_TK = 160256, L_TK2 = 161696, L_TQ = 161888, L_TQ2 = 163248, L_CNT = 163712, LDS_BYTES = 163840;
enum { C_PROG = 0, C_WPROG = 4, C_WOK, C_TBL, C_KVC, C_PHI, C_STB, C_PU0, C_PU1, C_PA0, C_PA1, C_PB0, C_PB1, C_REDA, C_REDB, C_FYC, C_FAOK, C_FZC, C_GEM, C_PUB };
#define CNT(i) (L_CNT + 4 * (i))
constexpr u32 SENT = 0xFFFFFFFFu;
constexpr int SPIN_G = 1 << 17, SPIN_L = 1 << 21;

#define MAP 1
__device__ __forceinline__ int wg_of(int p, int g) {
  return (p >> 2) * 2 + (g >> 3) + 8 * ((p & 3) * 8 + (g & 7));
}
struct Args {
  const u16 *x, *wqkv, *wphi, *wo, *bk, *bv, *sk, *ssm, *dv;
  u16* out;
  unsigned char* ws;
};

DEV int opq(int v) { asm volatile("" : "+v"(v)); return v; }
DEV u32 rfl(u32 v) { return __builtin_amdgcn_readfirstlane(v); }
DEV rsrc_t mkrs(const void* p, u32 n) { return __builtin_amdgcn_make_buffer_rsrc((void*)p, 0, n, 0x00020000); }
DEV u32 pk(float a, float b) { f2 v = {a, b}; return __builtin_bit_cast(u32, __builtin_convertvector(v, bf2)); }
DEV u32 pk(f2 v) { return __builtin_bit_cast(u32, __builtin_convertvector(v, bf2)); }
DEV f2 up(u32 p) { f2 r = {__uint_as_float(p << 16), __uint_as_float(p & 0xffff0000u)}; return r; }
DEV f2 rnd(f2 v) { return up(pk(v)); }
DEV float rnd1(float v) { return __uint_as_float(pk(v, v) & 0xffff0000u); }
DEV f4 mfma(u4 a, u4 b, f4 c) { return __builtin_amdgcn_mfma_f32_16x16x32_bf16(__builtin_bit_cast(bf8, a), __builtin_bit_cast(bf8, b), c, 0, 0, 0); }
DEV u32 umin(u32 a, u32 b) { return a < b ? a : b; }
DEV u32 umax(u32 a, u32 b) { return a > b ? a : b; }
DEV u32 umax3(u32 a, u32 b, u32 c) { return umax(umax(a, b), c); }
DEV u32 san(u32 v) { return umin(v, 0xFFFFFFFEu); }
DEV u4 san(u4 v) { u4 r = {san(v.x), san(v.y), san(v.z), san(v.w)}; return r; }
DEV u32 vm(u32 m, u4 v) { return umax3(umax3(m, v.x, v.y), v.z, v.w); }
DEV bool anyl(bool c) { return __builtin_amdgcn_ballot_w64(c) != 0; }
DEV float dot2(u32 a, u32 b, float c) { return __builtin_amdgcn_fdot2_f32_bf16(__builtin_bit_cast(bf2, a), __builtin_bit_cast(bf2, b), c, false); }
template <class T> DEV T ld(u32 o) { return *(LAS T*)(uintptr_t)o; }
template <class T> DEV void st(u32 o, T v) { *(LAS T*)(uintptr_t)o = v; }
DEV u2 trd(u32 o) { return __builtin_bit_cast(u2, __builtin_amdgcn_ds_read_tr16_b64_v4i16((LAS s4v*)(uintptr_t)o)); }
DEV u4 trd2(u32 o0, u32 o1) { u2 a = trd(o0), b = trd(o1); u4 r = {a.x, a.y, b.x, b.y}; return r; }
DEV u4 gld4(rsrc_t r, u32 v, u32 s, int aux) { return aux ? __builtin_amdgcn_raw_buffer_load_b128(r, v, s, SC1) : __builtin_amdgcn_raw_buffer_load_b128(r, v, s, 0); }
DEV u2 gld2(rsrc_t r, u32 v, u32 s, int aux) { return aux ? __builtin_amdgcn_raw_buffer_load_b64(r, v, s, SC1) : __builtin_amdgcn_raw_buffer_load_b64(r, v, s, 0); }
DEV u32 gld1(rsrc_t r, u32 v, u32 s, int aux) { return aux ? __builtin_amdgcn_raw_buffer_load_b32(r, v, s, SC1) : __builtin_amdgcn_raw_buffer_load_b32(r, v, s, 0); }
DEV void gst4(u4 d, rsrc_t r, u32 v, u32 s) { __builtin_amdgcn_raw_buffer_store_b128(d, r, v + s, 0, SC1); }
DEV void gst2(u2 d, rsrc_t r, u32 v, u32 s) { __builtin_amdgcn_raw_buffer_store_b64(d, r, v, s, SC1); }
DEV void gst1(u32 d, rsrc_t r, u32 v, u32 s) { __builtin_amdgcn_raw_buffer_store_b32(d, r, v, s, SC1); }
template <int AUX> DEV void dma(rsrc_t r, u32 l, u32 v, u32 s) {
#if defined(__HIP_DEVICE_COMPILE__)
  __builtin_amdgcn_raw_ptr_buffer_load_lds(r, (LAS void*)(uintptr_t)l, 16, v, s, 0, AUX);
#endif
}
DEV void vmwait0() { asm volatile("s_waitcnt vmcnt(0)" ::: "memory"); }
template <int N> DEV void vmwait() { asm volatile("s_waitcnt vmcnt(%0)" ::"n"(N) : "memory"); }
DEV void bar() { asm volatile("s_waitcnt lgkmcnt(0)\n\ts_barrier" ::: "memory"); }
DEV void lset(u32 a, u32 v) { asm volatile("ds_write_b32 %0, %1" ::"v"(a), "v"(v) : "memory"); }
DEV void ladd(u32 a, int lane) { if (lane == 0) { u32 one = 1; asm volatile("ds_add_u32 %0, %1" ::"v"(a), "v"(one) : "memory"); } }
DEV u32 ladd_rtn(u32 a, int lane) {
  u32 r = 0;
  if (lane == 0) { u32 one = 1; asm volatile("ds_add_rtn_u32 %0, %1, %2\n\ts_waitcnt lgkmcnt(0)" : "=v"(r) : "v"(a), "v"(one) : "memory"); }
  return rfl(r);
}
DEV u32 lget(u32 a) { u32 r; asm volatile("ds_read_b32 %0, %1\n\ts_waitcnt lgkmcnt(0)" : "=v"(r) : "v"(a) : "memory"); return rfl(r); }
DEV u32 lwait(u32 a, u32 t, int& dead) {
  u32 r = lget(a);
  if (__builtin_expect(r < t, 0)) {
    NOUNR for (int i = dead ? SPIN_L : 0; i < SPIN_L; ++i) { r = lget(a); if (r >= t) return r; __builtin_amdgcn_s_sleep(1); }
    dead = 1;
  }
  return r;
}
DEV void poll(rsrc_t ws, u32 off, u32 tag, int& dead) {
  NOUNR for (int i = dead ? SPIN_G : 0; i < SPIN_G; ++i) {
    FENCE();
    u32 v = gld1(ws, off, 0, 1);
    if (!anyl(v != tag)) { FENCE(); return; }
    __builtin_amdgcn_s_sleep(1);
  }
  dead = 1;
}
DEV void setflag(rsrc_t ws, u32 off, u32 tag, int lane) { if (lane == 0) gst1(tag, ws, off, 0); }
#define SW16(a, b) __builtin_amdgcn_permlane16_swap(a, b, false, false)
#define SW32(a, b) __builtin_amdgcn_permlane32_swap(a, b, false, false)
DEV float sw16add(float a, float b) { auto r = SW16(__float_as_uint(a), __float_as_uint(b)); return __uint_as_float(r[0]) + __uint_as_float(r[1]); }
DEV float sw32add(float a, float b) { auto r = SW32(__float_as_uint(a), __float_as_uint(b)); return __uint_as_float(r[0]) + __uint_as_float(r[1]); }

#define STAMP(i) do { if ((i) == PADAT && w < 4) { __builtin_amdgcn_sched_barrier(0); stv = (u32)__builtin_amdgcn_s_memrealtime(); __builtin_amdgcn_sched_barrier(0);} if ((i) == 31 && w < 4) { for (int z_ = 0; z_ < 100000; ++z_) { if ((u32)__builtin_amdgcn_s_memrealtime() - stv >= 6000u) break; __builtin_amdgcn_s_sleep(2); } } } while (0)
#define STAMPC(i)
#define STARG , u32& stv
#define STPASS , stv

DEV f2 ln_stats(float s, float sq) {
  float mean = s * (1.0f / 576.0f);
  float var = fmaxf(sq * (1.0f / 576.0f) - mean * mean, 0.0f);
  float sd = rnd1(__builtin_amdgcn_sqrtf(rnd1(rnd1(var) + 1e-5f)));
  f2 r = {rnd1(mean), __builtin_amdgcn_rcpf(sd)};
  return r;
}
DEV void stats_from(u4 v, float (&m)[2], float (&rs)[2]) {
  float s = __uint_as_float(v.x) + __uint_as_float(v.z), sq = __uint_as_float(v.y) + __uint_as_float(v.w);
  s = sw16add(s, s); sq = sw16add(sq, sq);
  f2 t = ln_stats(s, sq);
  auto r0 = SW32(__float_as_uint(t.x), __float_as_uint(t.x));
  auto r1 = SW32(__float_as_uint(t.y), __float_as_uint(t.y));
  m[0] = __uint_as_float(r0[0]); m[1] = __uint_as_float(r0[1]); rs[0] = __uint_as_float(r1[0]); rs[1] = __uint_as_float(r1[1]);
}
DEV u4 ld_stats(rsrc_t ws, u32 off) { u2 a = gld2(ws, off, 0, 1), b = gld2(ws, off + 256, 0, 1); return u4{a.x, a.y, b.x, b.y}; }
DEV u4 poll_stats(rsrc_t ws, u32 off, u4 v, int& dead) {
  if (anyl(vm(0, v) == SENT)) {
    NOUNR for (int i = dead ? SPIN_G : 0; i < SPIN_G; ++i) {
      FENCE();
      v = ld_stats(ws, off);
      if (!anyl(vm(0, v) == SENT)) { FENCE(); return v; }
    }
    dead = 1;
  }
  FENCE();
  return v;
}

DEV void pub_stats(rsrc_t ws, float s0, float s1, float q0, float q1, u32 red, u32 cnt, u32 last, u32 dst, int w, int lane) {
  float t = sw16add(sw32add(s0, s1), sw32add(q0, q1));
  st<float>(red + w * 256 + (16 * (lane >> 5) + (lane & 15)) * 8 + ((lane >> 4) & 1) * 4, t);
  FENCE();
  if (ladd_rtn(cnt, lane) == last) {
    f4 r = {0, 0, 0, 0};
    UNR for (int i = 0; i < 8; ++i) r += ld<f4>(red + i * 256 + (lane & 15) * 16);
    gst4(san(__builtin_bit_cast(u4, r)), ws, lane < 16 ? dst + lane * 16 : 0x80000000u, 0);
  }
}

struct KVin { u32 k[11], v[8]; u4 w; };
DEV void kv_load(const Args& a, int T, int rt, int c, KVin& in) {
  rsrc_t rk = mkrs(a.bk, 2359296), rv = mkrs(a.bv, 2359296), rw = mkrs(a.sk, 4608);
  int cp = T >> 2, sub = T & 3, ch = 144 * c + 2 * cp, s0 = 32 * rt + 8 * sub;
  u32 base = s0 * 1152 + ch * 2;
  UNR for (int i = 0; i < 3; ++i) in.k[i] = gld1(rk, s0 == 0 ? 0x80000000u : base - (3 - i) * 1152, 0, 0);
  UNR for (int i = 0; i < 8; ++i) in.k[3 + i] = gld1(rk, base, i * 1152, 0);
  UNR for (int i = 0; i < 8; ++i) in.v[i] = gld1(rv, base, i * 1152, 0);
  in.w = gld4(rw, ch * 8, 0, 0);
}
DEV void kv_compute(rsrc_t ws, int T, int rt, int c, const KVin& in) {
  int cp = T >> 2, sub = T & 3, ch = 144 * c + 2 * cp, s0 = 32 * rt + 8 * sub;
  f2 w0 = {__uint_as_float(in.w.x << 16), __uint_as_float(in.w.z << 16)}, w1 = {__uint_as_float(in.w.x & 0xffff0000u), __uint_as_float(in.w.z & 0xffff0000u)};
  f2 w2 = {__uint_as_float(in.w.y << 16), __uint_as_float(in.w.w << 16)}, w3 = {__uint_as_float(in.w.y & 0xffff0000u), __uint_as_float(in.w.w & 0xffff0000u)};
  f2 kf[11];
  UNR for (int i = 0; i < 11; ++i) kf[i] = up(in.k[i]);
  u32 P[8];
  UNR for (int s = 0; s < 8; ++s) {
    f2 t = rnd(kf[s] * w3);
    t = rnd(t + rnd(kf[s + 1] * w2));
    t = rnd(t + rnd(kf[s + 2] * w1));
    t = rnd(t + rnd(kf[s + 3] * w0));
    P[s] = pk(t * up(in.v[s]));
  }
  u4 o0, o1;
  UNR for (int d = 0; d < 4; ++d) {
    o0[d] = __builtin_amdgcn_perm(P[6 - 2 * d], P[7 - 2 * d], 0x05040100u);
    o1[d] = __builtin_amdgcn_perm(P[6 - 2 * d], P[7 - 2 * d], 0x07060302u);
  }
  u32 off = O_KVT + ch * 4096 + (2040 - s0) * 2;
  gst4(o0, ws, off, 0);
  gst4(o1, ws, off, 4096);
}

struct Hin { u4 h[4]; u32 n[4]; };
DEV void h_load(const Args& a, int ch, int lane, Hin& in) {
  rsrc_t rh = mkrs(a.ssm, 2359296);
  UNR for (int m = 0; m < 4; ++m) { in.h[m] = gld4(rh, ch * 4096 + lane * 16, m * 1024, 0); in.n[m] = gld1(rh, ch * 4096 + lane * 16 + 16, m * 1024, 0); }
}
DEV void h_stage(u32 lH, u32 lU, int lane, const Hin& in) {
  UNR for (int m = 0; m < 4; ++m) {
    u4 h = in.h[m];
    u4 s = {__builtin_amdgcn_alignbit(h.y, h.x, 16), __builtin_amdgcn_alignbit(h.z, h.y, 16), __builtin_amdgcn_alignbit(h.w, h.z, 16), __builtin_amdgcn_alignbit(in.n[m], h.w, 16)};
    st<u4>(lH + 32 + m * 1024 + lane * 16, h);
    st<u4>(lH + 4160 + 32 + m * 1024 + lane * 16, s);
  }
  u32 h0 = rfl(in.h[0].x) << 16;
  u4 z = {0, 0, 0, lane == 3 ? h0 : 0u};
  if (lane < 4) st<u4>(lH + (lane >> 1) * 4160 + (lane & 1) * 16, z);
  u4 zz = {0, 0, 0, 0};
  if (lane < 30) st<u4>(lU + 4096 + lane * 16, zz);
}
DEV void u_stage(rsrc_t ws, u32 lU, int ch, int lane) {
  UNR for (int m = 0; m < 4; ++m) dma<SC1>(ws, lU + m * 1024, O_KVT + ch * 4096 + lane * 16, m * 1024);
}
DEV void conv_pair(const Args& a, rsrc_t ws, u32 lH, u32 lU, int ch, int e, u32 cA, u32 cB, u32 tgt, int lane, int& dead) {
  const int q = lane >> 4, i16 = lane & 15;
  f4 acc[8];
  UNR for (int i = 0; i < 8; ++i) acc[i] = f4{0, 0, 0, 0};
  const u32 aB = lU + 2 * (2032 - 16 * i16 + 8 * q) - 3584 + 256 * e;
  const u32 aA = lH + ((i16 & 1) ? 0 : 4160) + 4 * (((i16 + 1) >> 1) + 4 * q) + 256 * e;
  const u32 aA4[4] = {aA, aA + 1024, aA + 2048, aA + 3072};
  float dv = __uint_as_float((u32)a.dv[ch] << 16);
  u4 B[8];
  u2 Al[8], Ah[8];
#define CLD(t, bb)                                                                                                              \
  asm volatile("ds_read2_b32 %0, %3 offset0:%5 offset1:%6\n\tds_read2_b32 %1, %3 offset0:%7 offset1:%8\n\tds_read_b128 %2, %4 offset:%9" \
               : "=&v"(Al[t]), "=&v"(Ah[t]), "=&v"(B[7 - (t)])                                                                    \
               : "v"(aA4[(t) >> 1]), "v"(aB), "n"(16 * (bb) + 128 * ((t) & 1)), "n"(16 * (bb) + 128 * ((t) & 1) + 1),              \
                 "n"(16 * (bb) + 128 * ((t) & 1) + 2), "n"(16 * (bb) + 128 * ((t) & 1) + 3), "n"(512 * (t) + 64 * (bb))           \
               : "memory")
  UNR for (int t = 0; t < 8; ++t) CLD(t, 0);
  UNR for (int bb = 0; bb < 4; ++bb) {
    asm volatile("s_waitcnt lgkmcnt(6)" : "+v"(Al[0]), "+v"(Ah[0]), "+v"(Al[1]), "+v"(Ah[1]), "+v"(Al[2]), "+v"(Ah[2]), "+v"(Al[3]), "+v"(Ah[3]), "+v"(Al[4]),
                 "+v"(Ah[4]), "+v"(Al[5]), "+v"(Ah[5]), "+v"(B[7]), "+v"(B[6]), "+v"(B[5]), "+v"(B[4]), "+v"(B[3]), "+v"(B[2]));
    UNR for (int t = 0; t < 8; ++t) {
      u4 A = {Al[t].x, Al[t].y, Ah[t].x, Ah[t].y};
      UNR for (int v = 7 - t; v >= 0; --v) {
        if (t == 0 && v == 1) {
          __builtin_amdgcn_sched_barrier(0);
          asm volatile("s_waitcnt lgkmcnt(0)" : "+v"(Al[6]), "+v"(Ah[6]), "+v"(Al[7]), "+v"(Ah[7]), "+v"(B[1]), "+v"(B[0]));
        }
        acc[t + v] = mfma(A, B[v], acc[t + v]);
      }
      __builtin_amdgcn_sched_barrier(0);
      if (bb < 3) CLD(t, bb + 1);
    }
  }
  FENCE();
  ladd(cA, lane);
  lwait(cA, tgt, dead);
  f4 keep[4];
  UNR for (int i = 0; i < 4; ++i) {
    keep[i] = e ? acc[4 + i] : acc[i];
    f4 snd = e ? acc[i] : acc[4 + i];
    st<f4>(lH + e * 4096 + i * 1024 + lane * 16, snd);
  }
  FENCE();
  ladd(cB, lane);
  lwait(cB, tgt, dead);
  u2 Y[4];
  UNR for (int i = 0; i < 4; ++i) {
    f4 s = keep[i] + ld<f4>(lH + (1 - e) * 4096 + i * 1024 + lane * 16);
    u2 uu = ld<u2>(lU + 2 * (2044 - 256 * (4 * e + i) - 16 * i16 - 4 * q));
    f2 u32_ = up(uu.x), u10 = up(uu.y);
    f2 g01 = {u10.y * dv, u10.x * dv}, g23 = {u32_.y * dv, u32_.x * dv};
    f2 s01 = {s.x, s.y}, s23 = {s.z, s.w};
    Y[i].x = pk(rnd(s01) + rnd(g01));
    Y[i].y = pk(rnd(s23) + rnd(g23));
  }
  UNR for (int i = 0; i < 2; ++i) {
    auto r0 = SW16(Y[2 * i].x, Y[2 * i + 1].x);
    auto r1 = SW16(Y[2 * i].y, Y[2 * i + 1].y);
    u4 o = {r0[0], r1[0], r0[1], r1[1]};
    gst4(o, ws, O_YT + ch * 4096 + 2 * (256 * (4 * e + 2 * i + (q & 1)) + 16 * i16 + 8 * (q >> 1)), 0);
  }
}

template <bool HELP, int L>
DEV void tail(const Args& a, rsrc_t ws, int b, int w, int lane0, int g, int rt, int c, int rot, u32 tag, const u4 (&wo)[18], const u4 (&wo8)[18], int& dead STARG) {
  const int lane = opq(lane0), q = lane >> 4, i16 = lane & 15;
  const bool ex = HELP && w < 6;
  const int tb8 = w & 1;
  u2 xr[3];
  if (L == 0) {
    rsrc_t rx = mkrs(a.x, 2359296);
    u32 o = (32 * rt + i16) * 1152 + (144 * c + 4 * q) * 2;
    xr[0] = gld2(rx, o, w * 32, 0); xr[1] = gld2(rx, o, w * 32 + 16 * 1152, 0);
    xr[2] = gld2(rx, o, 256 + tb8 * 16 * 1152, 0);
  } else {
    xr[0] = ld<u2>(L_XR + w * 1024 + lane * 8); xr[1] = ld<u2>(L_XR + w * 1024 + 512 + lane * 8);
    xr[2] = ld<u2>(L_XR + 8192 + tb8 * 512 + lane * 8);
  }
  {
    const u32 pb = L * OB_L + (rt >> 2) * OB_P, tbp = 2 * (rt & 3);
    u4 v[5];
    if (HELP) lwait(CNT(C_PUB), 8 * L + 8, dead);
    NOUNR for (int it = dead ? SPIN_G - 1 : 0;; ++it) {
      FENCE();
      UNR for (int i = 0; i < 4; ++i) v[i] = gld4(ws, O_OB + lane * 16, pb + (tbp + (i >> 1)) * 16384 + (w + 8 * (i & 1)) * 1024, 1);
      if (!HELP) {
        u32 o = O_OB + 131072 + q * 2048 + i16 * 8, so = pb + (w & 1) * 8192 + (tbp + (w >> 1)) * 128;
        u2 l0 = gld2(ws, o, so, 1), l1 = gld2(ws, o + 1024, so, 1);
        v[4] = u4{l0.x, l0.y, l1.x, l1.y};
      }
      u32 m = 0;
      UNR for (int i = 0; i < (HELP ? 4 : 5); ++i) m = vm(m, v[i]);
      if (!anyl(m == SENT)) break;
      if (it >= SPIN_G) { dead = 1; break; }
    }
    FENCE();
    UNR for (int i = 0; i < 4; ++i) st<u4>(L_STG + (i >> 1) * 18432 + (w + 8 * (i & 1)) * 1024 + lane * 16, v[i]);
    if (!HELP) st<u4>(L_STG + (w >> 1) * 18432 + (16 + (w & 1)) * 1024 + lane * 16, v[4]);
  }
  STAMP(16 + L * 8);
  bar();
  STAMP(17 + L * 8);
  u32 fyoff = 0, fy = tag;
  const int chl = 144 * c + ((lane & 16) ? 128 : 16 * w) + i16;
  if (L == 0) { fyoff = chl < 512 ? O_FY + (chl >> 1) * 128 : O_FZ + (chl - 512) * 128; fy = gld1(ws, fyoff, 0, 1); }
  f4 acc[3] = {f4{0, 0, 0, 0}, f4{0, 0, 0, 0}, f4{0, 0, 0, 0}};
  if (ex) {
    UNR for (int ks = 0; ks < 18; ++ks) {
      u4 b0 = ld<u4>(L_STG + ks * 1024 + lane * 16), b1 = ld<u4>(L_STG + 18432 + ks * 1024 + lane * 16);
      u4 b2 = ld<u4>(L_STG + tb8 * 18432 + ks * 1024 + lane * 16);
      acc[0] = mfma(wo[ks], b0, acc[0]); acc[1] = mfma(wo[ks], b1, acc[1]); acc[2] = mfma(wo8[ks], b2, acc[2]);
    }
  } else {
    UNR for (int ks = 0; ks < 18; ++ks) {
      u4 b0 = ld<u4>(L_STG + ks * 1024 + lane * 16), b1 = ld<u4>(L_STG + 18432 + ks * 1024 + lane * 16);
      acc[0] = mfma(wo[ks], b0, acc[0]); acc[1] = mfma(wo[ks], b1, acc[1]);
    }
  }
  u4 yv, yv8;
  if (L == 0) {
    if (anyl(fy != tag)) poll(ws, fyoff, tag, dead);
    FENCE();
    yv = gld4(ws, O_YT + (lane >> 2) * 4096 + (lane & 3) * 16, (144 * c + 16 * w) * 4096 + rt * 64, 1);
    yv8 = gld4(ws, O_YT + (lane >> 2) * 4096 + (lane & 3) * 16, (144 * c + 128) * 4096 + rt * 64, 1);
  }
  constexpr int NU = HELP ? 3 : 2;
  f2 x1[NU][2];
  float S[NU], Q[NU];
  UNR for (int u = 0; u < NU; ++u) {
    f2 lo = {acc[u].x, acc[u].y}, hi = {acc[u].z, acc[u].w};
    x1[u][0] = rnd(rnd(lo) + up(xr[u].x)); x1[u][1] = rnd(rnd(hi) + up(xr[u].y));
    f2 s = x1[u][0] + x1[u][1], sq = x1[u][0] * x1[u][0] + x1[u][1] * x1[u][1];
    S[u] = s.x + s.y; Q[u] = sq.x + sq.y;
  }
  if (HELP) { if (w == 4) { S[0] += S[2]; Q[0] += Q[2]; } if (w == 5) { S[1] += S[2]; Q[1] += Q[2]; } }
  const u32 stoff = O_ST + L * 2 * 65536 + rt * 1024 + (q & 1) * 512 + (16 * (q >> 1) + i16) * 8;
  pub_stats(ws, S[0], S[1], Q[0], Q[1], L_REDA, CNT(C_REDA), 8 * L + 7, O_ST + L * 2 * 65536 + rt * 1024 + c * 256, w, lane);
  STAMP(18 + L * 8);
  if (HELP && L == 0 && w == 4) {
    lwait(CNT(C_REDA), 8, dead);
    UNR for (int j = 5; j < 9; ++j) {
      int kr = j + rot; kr -= kr >= 9 ? 9 : 0;
      UNR for (int i = 0; i < 9; ++i) dma<SC1>(ws, j * 9216 + i * 1024, O_WT + lane * 16, g * WT_G + kr * 9216 + i * 1024);
    }
  }
  u4 sv = ld_stats(ws, stoff);
  if (L == 0) {
    st<u4>(L_Y + w * 1024 + lane * 16, yv);
    if (ex) st<u4>(L_Y + 8192 + lane * 16, yv8);
    FENCE();
  }
  u2 y[NU];
  {
    u32 ya = L_Y + (4 * q + (i16 >> 2)) * 64 + (i16 & 3) * 8;
    y[0] = trd(ya + w * 1024); y[1] = trd(ya + w * 1024 + 32);
    if (HELP) y[2] = trd(ya + 8192 + tb8 * 32);
  }
  sv = poll_stats(ws, stoff, sv, dead);
  STAMP(19 + L * 8);
  if (HELP && L == 0 && w == 4) { vmwait0(); lset(CNT(C_WPROG), 11); }
  float m[2], rs[2];
  stats_from(sv, m, rs);
  u2 x2[NU];
  UNR for (int u = 0; u < NU; ++u) {
    float mu = u < 2 ? m[u] : (tb8 ? m[1] : m[0]), ru = u < 2 ? rs[u] : (tb8 ? rs[1] : rs[0]);
    UNR for (int h = 0; h < 2; ++h) {
      f2 xn = rnd(rnd(x1[u][h] - mu) * ru);
      x2[u][h] = pk(x1[u][h] + rnd(xn * up(y[u][h])));
    }
  }
  auto r0 = SW16(x2[0].x, x2[1].x);
  auto r1 = SW16(x2[0].y, x2[1].y);
  u4 o = {r0[0], r1[0], r0[1], r1[1]};
  if (L == 0) {
    gst4(san(o), ws, O_X2 + (q & 1) * 18432 + (q >> 1) * 256 + i16 * 16, 2 * rt * 18432 + (9 * c + w) * 512);
    if (ex) {
      auto t0 = SW16(x2[NU - 1].x, x2[NU - 1].x);
      auto t1 = SW16(x2[NU - 1].y, x2[NU - 1].y);
      gst4(san(u4{t0[0], t1[0], t0[1], t1[1]}), ws, (q & 1) ? 0x80000000u : O_X2 + (q >> 1) * 256 + i16 * 16, (2 * rt + tb8) * 18432 + (9 * c + 8) * 512);
    }
    st<u2>(L_XR + w * 1024 + lane * 8, x2[0]); st<u2>(L_XR + w * 1024 + 512 + lane * 8, x2[1]);
    if (ex) st<u2>(L_XR + 8192 + tb8 * 512 + lane * 8, x2[NU - 1]);
    UNR for (int u = 0; u < NU; ++u) {
      f2 lo = up(x2[u].x), hi = up(x2[u].y);
      f2 s = lo + hi, sq = lo * lo + hi * hi;
      S[u] = s.x + s.y; Q[u] = sq.x + sq.y;
    }
    if (HELP) { if (w == 4) { S[0] += S[2]; Q[0] += Q[2]; } if (w == 5) { S[1] += S[2]; Q[1] += Q[2]; } }
    pub_stats(ws, S[0], S[1], Q[0], Q[1], L_REDB, CNT(C_REDB), 7, O_ST + 65536 + rt * 1024 + c * 256, w, lane);
  } else {
    rsrc_t ro = mkrs(a.out, 2359296);
    __builtin_amdgcn_raw_buffer_store_b128(o, ro, (16 * (q & 1) + i16) * 1152 + (q >> 1) * 16 + 32 * rt * 1152 + 288 * c + 32 * w, 0, 0);
    if (ex) __builtin_amdgcn_raw_buffer_store_b64(x2[NU - 1], ro, i16 * 1152 + q * 8, (32 * rt + 16 * tb8) * 1152 + 288 * c + 256, 0);
  }
  STAMP(20 + L * 8);
}

template <int L>
DEV void main_layer(const Args& a, rsrc_t ws, int b, int w, int lane0, int p, int g, int rt, int c, int rot, u32 tag, u4 (&B)[36], float (&mS)[2], float (&mQ)[2], u32& wp, int& dead STARG) {
    constexpr float LOG2E = 1.4426950408889634f;
    int lane = opq(lane0), q = lane >> 4, i16 = lane & 15;
    u32 aF[2], a4[2], a7[2];
    UNR for (int u = 0; u < 2; ++u) {
      aF[u] = i16 * 128 + (((4 * u + q) ^ (i16 >> 1)) << 4);
      a4[u] = (64 + 2 * (i16 >> 2) + (i16 & 1)) * 128 + (((4 * u + q) ^ (2 * (i16 >> 2))) << 4);
      a7[u] = L_WQ + (32 + (i16 >> 2)) * 128 + ((4 * u + q) << 4);
    }
    {
      f4 acc[5][2];
      u4 sv = {0, 0, 0, 0};
      const u32 stoff = O_ST + 65536 + (4 * p + w) * 1024 + (q & 1) * 512 + (16 * (q >> 1) + i16) * 8;
      if (L == 1) {
        u32 G = lane < 36 ? lane : (lane < 40 ? 9 * (lane - 36) + 8 : 0);
        u32 po = O_X2 + (8 * p + 2 * w + (lane >= 36 && lane < 40 ? 1 : 0)) * 18432 + G * 512;
        NOUNR for (int i = dead ? SPIN_G : 0; i < SPIN_G; ++i) {
          FENCE();
          u32 v = gld1(ws, po, 0, 1);
          if (!anyl(v == SENT)) break;
        }
        FENCE();
        STAMP(21);
      }
      for (int pass = 0;; ++pass) {
        if (L == 1) {
          UNR for (int j = 0; j < 9; ++j) {
            int kr = j + rot; kr -= kr >= 9 ? 9 : 0;
            UNR for (int i = 0; i < 4; ++i) B[4 * j + i] = gld4(ws, O_X2 + lane * 16 + (i >> 1) * 1024, (8 * p + 2 * w + (i & 1)) * 18432 + kr * 2048, 1);
          }
          sv = ld_stats(ws, stoff);
        }
        UNR for (int i = 0; i < 5; ++i) acc[i][0] = acc[i][1] = f4{0, 0, 0, 0};
        u32 vmx = 0;
        if (wp < (L ? 0u : 1u)) wp = lwait(CNT(C_WPROG), 1, dead);
        if (L == 0) STAMP(3);
        u4 A[2][5];
        UNR for (int i = 0; i < 4; ++i) A[0][i] = ld<u4>(aF[0] + i * 2048);
        A[0][4] = ld<u4>(a4[0]);
        u32 fl = *(volatile LAS u32*)(uintptr_t)CNT(C_WPROG);
        UNR for (int s = 0; s < 18; ++s) {
          if (s < 17) {
            const int j = (s + 1) >> 1, u = (s + 1) & 1;
            if (u == 0) {
              u32 need = L ? (j < 5 ? 0u : 11u) : (u32)(j + 1);
              wp = umax(wp, rfl(fl));
              if (wp < need) wp = lwait(CNT(C_WPROG), need, dead);
            }
            UNR for (int i = 0; i < 4; ++i) A[(s + 1) & 1][i] = ld<u4>(aF[u] + j * 9216 + i * 2048);
            A[(s + 1) & 1][4] = ld<u4>(a4[u] + j * 9216);
            if (u == 0) fl = *(volatile LAS u32*)(uintptr_t)CNT(C_WPROG);
          }
          vmx = vm(vm(vmx, B[2 * s]), B[2 * s + 1]);
          UNR for (int i = 0; i < 5; ++i) {
            acc[i][0] = mfma(A[s & 1][i], B[2 * s], acc[i][0]);
            acc[i][1] = mfma(A[s & 1][i], B[2 * s + 1], acc[i][1]);
          }
        }
        if (L == 0 || !anyl(vmx == SENT) || dead) break;
        if (pass >= 20000) { dead = 1; break; }
        FENCE();
      }
      FENCE();
      ladd(CNT(C_GEM), lane);
      STAMP(4 + L * 6);
      if (L == 0) lwait(CNT(C_TBL), 1, dead);
      else { sv = poll_stats(ws, stoff, sv, dead); stats_from(sv, mS, mQ); }
      {
        u32 kv[2][9];
        UNR for (int i = 0; i < 5; ++i) {
          f4 cs = ld<f4>(L_CSL + i * 64 + q * 16);
          f2 c01 = {cs.x, cs.y}, c23 = {cs.z, cs.w};
          UNR for (int tb = 0; tb < 2; ++tb) {
            f2 lo = {acc[i][tb].x, acc[i][tb].y}, hi = {acc[i][tb].z, acc[i][tb].w};
            kv[tb][i < 4 ? 2 * i : 8] = pk((lo - c01 * mS[tb]) * mQ[tb]);
            if (i < 4) kv[tb][2 * i + 1] = pk((hi - c23 * mS[tb]) * mQ[tb]);
          }
        }
        u32 K[9], V[9];
        UNR for (int i = 0; i < 9; ++i) { auto t = SW32(kv[0][i], kv[1][i]); K[i] = t[0]; V[i] = t[1]; }
        const bool val = (i16 >> 2) == q;
        const u32 tk = L_TK + (val ? ((q & 1) * 4 + (i16 & 3)) * 16 : 128), tk2 = L_TK2 + (val ? ((q & 1) * 4 + (i16 & 3)) * 4 : 32);
        const u4 k0 = {K[0], K[1], K[2], K[3]}, k1 = {K[4], K[5], K[6], K[7]}, k2 = {K[8], 0, 0, 0};
        f4 P[5];
        UNR for (int m = 0; m < 5; ++m) {
          u4 t2 = {ld<u32>(tk2 + m * 36), 0, 0, 0};
          P[m] = mfma(ld<u4>(tk + m * 288), k0, f4{0, 0, 0, 0});
          P[m] = mfma(ld<u4>(tk + m * 288 + 144), k1, P[m]);
          P[m] = mfma(t2, k2, P[m]);
        }
        const u32 row = L_STG + (32 * w + 16 * (q >> 1) + i16) * 232;
        f2 z[9];
        UNR for (int i = 0; i < 9; ++i) z[i] = f2{P[i >> 1][2 * (i & 1)], P[i >> 1][2 * (i & 1) + 1]};
        float mx = fmaxf(z[0].x, z[0].y), mn = fminf(z[0].x, z[0].y);
        UNR for (int i = 1; i < 9; ++i) { mx = fmaxf(fmaxf(mx, z[i].x), z[i].y); mn = fminf(fminf(mn, z[i].x), z[i].y); }
        const float ma = -mx * LOG2E, mb = mn * LOG2E;
        f2 e1[9], e2[9], s1 = {0, 0}, s2 = {0, 0};
        UNR for (int i = 0; i < 9; ++i) {
          f2 t1 = z[i] * LOG2E + ma, t2 = mb - z[i] * LOG2E;
          e1[i] = f2{__builtin_amdgcn_exp2f(t1.x), __builtin_amdgcn_exp2f(t1.y)};
          e2[i] = f2{__builtin_amdgcn_exp2f(t2.x), __builtin_amdgcn_exp2f(t2.y)};
          s1 += e1[i]; s2 += e2[i];
        }
        const float r1 = __builtin_amdgcn_rcpf(s1.x + s1.y), r2 = __builtin_amdgcn_rcpf(s2.x + s2.y);
        lwait(CNT(C_GEM), 4 * (L + 1), dead);
        {
          u32 vo = row + 144 + (q & 1) * 40;
          UNR for (int i = 0; i < 4; ++i) st<u2>(vo + 8 * i, u2{V[2 * i], V[2 * i + 1]});
          st<u2>(vo + 32, u2{V[8], 0x00003F80u});
        }
        u32 fo = row + (q & 1) * 72;
        UNR for (int i = 0; i < 4; ++i) {
          st<u2>(fo + 8 * i, u2{pk(e1[2 * i] * r1), pk(e1[2 * i + 1] * r1)});
          st<u2>(fo + 32 + 8 * i, u2{pk(e2[2 * i] * r2), pk(e2[2 * i + 1] * r2)});
        }
        st<u2>(fo + 64, u2{pk(e1[8] * r1), pk(e2[8] * r2)});
        FENCE();
        ladd(CNT(C_PHI), lane);
        lwait(CNT(C_PHI), 4 * (L + 1), dead);
        STAMP(5 + L * 6);
        const int hd = w >> 1, db = w & 1;
        const u32 ta = L_STG + (16 * (q & 1) + 4 * (i16 >> 2) + (q >> 1)) * 232 + (i16 & 3) * 8;
        f4 Dd[3] = {f4{0, 0, 0, 0}, f4{0, 0, 0, 0}, f4{0, 0, 0, 0}};
        const u32 tf = ta + 72 * hd, tv = ta + 144 + 40 * hd + 32 * db;
        UNR for (int ks = 0; ks < 4; ++ks) {
          u4 bv = trd2(tv + ks * 7424, tv + ks * 7424 + 464);
          UNR for (int fb = 0; fb < 3; ++fb) Dd[fb] = mfma(trd2(tf + ks * 7424 + fb * 32, tf + ks * 7424 + fb * 32 + 464), bv, Dd[fb]);
        }
        auto s0 = SW16(pk(Dd[0].x, Dd[0].y), pk(Dd[1].x, Dd[1].y));
        auto s1b = SW16(pk(Dd[0].z, Dd[0].w), pk(Dd[1].z, Dd[1].w));
        u4 o = {s0[0], s1b[0], s0[1], s1b[1]};
        const int d = 16 * db + i16;
        const u32 so = L * SP_L + g * SP_G + p * SP_P;
        gst4(san(o), ws, d < 19 ? O_SP + (hd * 19 + d) * 64 + (q & 1) * 32 + (q >> 1) * 16 : 0x80000000u, so);
        gst2(u2{san(pk(Dd[2].x, Dd[2].y)), san(pk(Dd[2].z, Dd[2].w))}, ws, (d < 19 && q == 0) ? O_SP + 2432 + (hd * 19 + d) * 8 : 0x80000000u, so);
      }
      ladd(CNT(C_PUB), lane);
      STAMP(6 + L * 6);
      u4 wo[18];
      u32 fq[2][9];
      {
        if (wp < 10) wp = lwait(CNT(C_WPROG), 10, dead);
        if (L == 0) lwait(CNT(C_FAOK), 1, dead);
        f4 aq[3][2];
        UNR for (int i = 0; i < 3; ++i) aq[i][0] = aq[i][1] = f4{0, 0, 0, 0};
        u4 A[2][3];
        A[0][0] = ld<u4>(L_WQ + aF[0]); A[0][1] = ld<u4>(L_WQ + aF[0] + 2048); A[0][2] = ld<u4>(a7[0]);
        UNR for (int s = 0; s < 18; ++s) {
          if (s < 17) {
            const int j = (s + 1) >> 1, u = (s + 1) & 1;
            A[(s + 1) & 1][0] = ld<u4>(L_WQ + aF[u] + j * 4608); A[(s + 1) & 1][1] = ld<u4>(L_WQ + aF[u] + j * 4608 + 2048);
            A[(s + 1) & 1][2] = ld<u4>(a7[u] + j * 4608);
          }
          UNR for (int i = 0; i < 3; ++i) {
            aq[i][0] = mfma(A[s & 1][i], B[2 * s], aq[i][0]);
            aq[i][1] = mfma(A[s & 1][i], B[2 * s + 1], aq[i][1]);
          }
          if (s < 9) {
            wo[2 * s] = gld4(ws, O_WOT + lane * 16, (9 * c + w) * 18432 + 2 * s * 1024, 1);
            wo[2 * s + 1] = gld4(ws, O_WOT + lane * 16, (9 * c + w) * 18432 + (2 * s + 1) * 1024, 1);
          }
          __builtin_amdgcn_sched_barrier(0);
        }
        STAMP(7 + L * 6);
        const f4 c5 = ld<f4>(L_CSL + 5 * 64 + q * 16), c6 = ld<f4>(L_CSL + 6 * 64 + q * 16);
        const float c7 = ld<float>(L_CSL + 7 * 64 + q * 16);
        const bool val = (i16 >> 3) == (q >> 1);
        const u32 tq = L_TQ + (val ? (q * 4 + (i16 & 3)) * 16 : 256), tq2 = L_TQ2 + (val ? (q * 4 + (i16 & 3)) * 4 : 64);
        u4 q0[2], q1[2];
        UNR for (int tb = 0; tb < 2; ++tb) {
          q0[tb].x = pk((f2{aq[0][tb].x, aq[0][tb].y} - f2{c5.x, c5.y} * mS[tb]) * mQ[tb]);
          q0[tb].y = pk((f2{aq[0][tb].z, aq[0][tb].w} - f2{c5.z, c5.w} * mS[tb]) * mQ[tb]);
          q0[tb].z = pk((f2{aq[1][tb].x, aq[1][tb].y} - f2{c6.x, c6.y} * mS[tb]) * mQ[tb]);
          q0[tb].w = pk((f2{aq[1][tb].z, aq[1][tb].w} - f2{c6.z, c6.w} * mS[tb]) * mQ[tb]);
          q1[tb] = u4{pk((aq[2][tb].x - c7 * mS[tb]) * mQ[tb], 0.0f), 0, 0, 0};
        }
        f4 P[2][5];
        UNR for (int m = 0; m < 5; ++m) {
          u4 t0 = ld<u4>(tq + m * 272), t1 = {ld<u32>(tq2 + m * 68), 0, 0, 0};
          UNR for (int tb = 0; tb < 2; ++tb) P[tb][m] = mfma(t1, q1[tb], mfma(t0, q0[tb], f4{0, 0, 0, 0}));
        }
        const float ls = (q & 1) ? -LOG2E : LOG2E;
        UNR for (int tb = 0; tb < 2; ++tb) {
          f2 z[9];
          UNR for (int i = 0; i < 9; ++i) z[i] = f2{P[tb][i >> 1][2 * (i & 1)], P[tb][i >> 1][2 * (i & 1) + 1]} * ls;
          float mx = fmaxf(z[0].x, z[0].y);
          UNR for (int i = 1; i < 9; ++i) mx = fmaxf(fmaxf(mx, z[i].x), z[i].y);
          f2 s1 = {0, 0};
          UNR for (int i = 0; i < 9; ++i) { f2 t = z[i] - mx; z[i] = f2{__builtin_amdgcn_exp2f(t.x), __builtin_amdgcn_exp2f(t.y)}; s1 += z[i]; }
          const float r1 = __builtin_amdgcn_rcpf(s1.x + s1.y);
          UNR for (int i = 0; i < 9; ++i) fq[tb][i] = pk(z[i] * r1);
        }
      }
      STAMP(8 + L * 6);
      lwait(CNT(C_STB), 4 * (L + 1), dead);
      STAMP(9 + L * 6);
      {
        const bool val = (i16 >> 3) == (q >> 1);
        const int d01 = (q >> 1) * 19 + 8 * ((i16 >> 2) & 1) + (i16 & 3), d2 = (q >> 1) * 19 + 16 + ((i16 & 3) < 2 ? (i16 & 3) : 2);
        const u32 m01 = val ? L_STB + d01 * 64 + (q & 1) * 32 : L_ZERO, m2 = val ? L_STB + d2 * 64 + (q & 1) * 32 : L_ZERO;
        const u32 l01 = val ? L_STB + 2432 + d01 * 8 + (q & 1) * 4 : L_ZERO, l2 = val ? L_STB + 2432 + d2 * 8 + (q & 1) * 4 : L_ZERO;
        f4 O[2][3];
        UNR for (int m = 0; m < 3; ++m) {
          u4 t0 = ld<u4>(m < 2 ? m01 + m * 256 : m2), t1 = ld<u4>((m < 2 ? m01 + m * 256 : m2) + 16), t2 = {ld<u32>(m < 2 ? l01 + m * 32 : l2), 0, 0, 0};
          UNR for (int tb = 0; tb < 2; ++tb) {
            u4 b0 = {fq[tb][0], fq[tb][1], fq[tb][2], fq[tb][3]}, b1 = {fq[tb][4], fq[tb][5], fq[tb][6], fq[tb][7]}, b2 = {fq[tb][8], 0, 0, 0};
            O[tb][m] = mfma(t2, b2, mfma(t1, b1, mfma(t0, b0, f4{0, 0, 0, 0})));
          }
        }
        u32 lo[2];
        UNR for (int tb = 0; tb < 2; ++tb) {
          const float inv = __builtin_amdgcn_rcpf(O[tb][2].z + 1e-6f);
          u4 o = {pk(f2{O[tb][0].x, O[tb][0].y} * inv), pk(f2{O[tb][0].z, O[tb][0].w} * inv), pk(f2{O[tb][1].x, O[tb][1].y} * inv), pk(f2{O[tb][1].z, O[tb][1].w} * inv)};
          gst4(san(o), ws, O_OB + lane * 16, L * OB_L + p * OB_P + (2 * w + tb) * 16384 + g * 1024);
          lo[tb] = pk(f2{O[tb][2].x, O[tb][2].y} * inv);
        }
        const u32 lv = san((q & 1) ? lo[1] : lo[0]);
        auto hh = SW32(lv, lv);
        const u32 n0 = __builtin_amdgcn_update_dpp(0u, hh[0], 0xB1, 0xF, 0xF, true), n1 = __builtin_amdgcn_update_dpp(0u, hh[1], 0xB1, 0xF, 0xF, true);
        gst4(u4{hh[0], hh[1], n0, n1}, ws, (q < 2 && !(i16 & 1)) ? O_OB + 131072 + (16 * q + i16) * 8 : 0x80000000u, L * OB_L + p * OB_P + g * 1024 + w * 256);
        ladd(CNT(C_PUB), lane);
      }
      STAMP(22 + L);
      tail<false, L>(a, ws, b, w, lane, g, rt, c, rot, tag, wo, wo, dead STPASS);
    }
}

template <int L>
DEV void state_sum(const Args& a, rsrc_t ws, int b, int w, int lane0, int g, int& dead STARG) {
    const int lane = opq(lane0), q = lane >> 4, i16 = lane & 15;
    u4 sel;
    UNR for (int d = 0; d < 4; ++d) sel[d] = ((i16 & 7) >> 1) == d ? ((i16 & 1) ? 0x3F800000u : 0x00003F80u) : 0u;
      {
        const int h = w - 4;
        u4 v[3][4];
        u32 off[3];
        UNR for (int gi = 0; gi < 3; ++gi) off[gi] = O_SP + q * SP_P + umin(16 * umin(h + 4 * gi, 10) + i16, 170) * 16;
        lwait(CNT(C_PUB), 8 * L + 4, dead);
        NOUNR for (int it = dead ? SPIN_G - 1 : 0;; ++it) {
          FENCE();
          u32 m = 0;
          UNR for (int gi = 0; gi < 3; ++gi) UNR for (int pp = 0; pp < 4; ++pp) v[gi][pp] = gld4(ws, off[gi], L * SP_L + g * SP_G + 4 * pp * SP_P, 1);
          UNR for (int gi = 0; gi < 3; ++gi) UNR for (int pp = 0; pp < 4; ++pp) m = vm(m, v[gi][pp]);
          if (!anyl(m == SENT)) break;
          if (it >= SPIN_G) { dead = 1; break; }
        }
        FENCE();
        STAMP(6 + L * 6);
        UNR for (int gi = 0; gi < 3; ++gi) {
          f4 s = {0, 0, 0, 0};
          UNR for (int pp = 0; pp < 4; ++pp) s = mfma(sel, v[gi][pp], s);
          int ck = 16 * umin(h + 4 * gi, 10) + i16;
          if (q < 2 && ck < 171) st<u2>(L_STB + ck * 16 + q * 8, u2{pk(s.x, s.y), pk(s.z, s.w)});
        }
        FENCE();
        ladd(CNT(C_STB), lane);
        STAMP(7 + L * 6);
      }
}

__global__ void __launch_bounds__(512, 2) fused_kernel(Args a) {
  unsigned long long t64_ = __builtin_amdgcn_s_memrealtime();
  __builtin_amdgcn_sched_barrier(0);
  const int tid = threadIdx.x, lane = tid & 63, w = rfl(tid >> 6), b = blockIdx.x;
  const int q = lane >> 4, i16 = lane & 15;
  const int xcd = b & 7, r = b >> 3;
  const int p = 4 * (xcd >> 1) + (r >> 3), g = 8 * (xcd & 1) + (r & 7), rot = ((r & 7) + 2 * (r >> 3)) % 9;
  const int rt = 4 * p + (g >> 2), c = g & 3;
  rsrc_t ws = mkrs(a.ws, WS_BYTES);
  u32 tag = rfl(*(const u32*)a.ws + 1);
  int dead = 0;
  u32 stv = (u32)t64_;
  if (w == PW) { gst1(stv, ws, lane == 0 ? O_STAMP + b * 4 : 0x80000000u, 0); }
  STAMP(0); STAMPC(29);

  if (w < 4) {
    __builtin_amdgcn_s_setprio(2);
    u4 B[36];
    float mS[2], mQ[2];
    {
      rsrc_t rx = mkrs(a.x, 2359296);
      u32 vo[2];
      UNR for (int par = 0; par < 2; ++par) vo[par] = (8 * par + (lane >> 3)) * 1152 + (((lane & 7) ^ (4 * par + (lane >> 4))) << 4);
      UNR for (int j = 0; j < 9; ++j) {
        int kr = j + rot; kr -= kr >= 9 ? 9 : 0;
        UNR for (int rg = 0; rg < 4; ++rg) dma<0>(rx, j * 16384 + w * 4096 + rg * 1024, vo[rg & 1], (128 * p + 32 * w + 16 * (rg >> 1)) * 1152 + kr * 128);
      }
      FENCE();
      __builtin_amdgcn_sched_barrier(0);
      asm volatile("s_waitcnt lgkmcnt(0)\n\ts_barrier" : "+s"(tag)::"memory");
      __builtin_amdgcn_sched_barrier(0);
      STAMP(1);
      u32 a0 = w * 4096 + (i16 >> 3) * 1024 + (i16 & 7) * 128 + ((q ^ (i16 >> 1)) << 4), a1 = a0 ^ 64, pa = CNT(C_PROG) + 4 * w;
      f4 gS[2] = {f4{0, 0, 0, 0}, f4{0, 0, 0, 0}}, gQ[2] = {f4{0, 0, 0, 0}, f4{0, 0, 0, 0}};
      const u4 ones = {0x3F803F80u, 0x3F803F80u, 0x3F803F80u, 0x3F803F80u};
#define X0STEP(j)                                                                                                                      \
  {                                                                                                                                    \
    u32 pv = j + 1;                                                                                                                    \
    asm volatile("s_waitcnt vmcnt(%8)\n\tds_read_b128 %0, %4\n\tds_read_b128 %1, %4 offset:2048\n\tds_read_b128 %2, %5\n\t"            \
                 "ds_read_b128 %3, %5 offset:2048\n\ts_waitcnt lgkmcnt(0)\n\tds_write_b32 %6, %7"                                       \
                 : "=&v"(B[4 * j]), "=&v"(B[4 * j + 1]), "=&v"(B[4 * j + 2]), "=&v"(B[4 * j + 3])                                        \
                 : "v"(a0 + j * 16384), "v"(a1 + j * 16384), "v"(pa), "v"(pv), "n"(32 - 4 * j)                                          \
                 : "memory");                                                                                                          \
    UNR for (int i = 0; i < 4; ++i) { gS[i & 1] = mfma(ones, B[4 * j + i], gS[i & 1]); gQ[i & 1] = mfma(B[4 * j + i], B[4 * j + i], gQ[i & 1]); } \
  }
      X0STEP(0) X0STEP(1) X0STEP(2) X0STEP(3) X0STEP(4) X0STEP(5) X0STEP(6) X0STEP(7) X0STEP(8)
      STAMP(2);
      UNR for (int tb = 0; tb < 2; ++tb) {
        float d = (i16 & 2) ? ((i16 & 1) ? gQ[tb].w : gQ[tb].z) : ((i16 & 1) ? gQ[tb].y : gQ[tb].x);
        d = (q == (i16 >> 2)) ? d : 0.0f;
        d = sw16add(d, d); d = sw32add(d, d);
        f2 t = ln_stats(gS[tb].x, d);
        mS[tb] = t.x; mQ[tb] = t.y;
      }
    }
    u32 wp = 0;
    main_layer<0>(a, ws, b, w, lane, p, g, rt, c, rot, tag, B, mS, mQ, wp, dead STPASS);
    main_layer<1>(a, ws, b, w, lane, p, g, rt, c, rot, tag, B, mS, mQ, wp, dead STPASS);
  } else {
    const int pr = (w - 4) >> 1, e = w & 1;
    const u32 lH = pr ? L_H1 : L_H0, lU = pr ? L_U1 : L_U0;
    if (w == 4) {
      const int cl = b % 27, kr0 = b / 27;
      if (b < 243) {
        rsrc_t rw = mkrs(a.wqkv, 576 * 3456);
        UNR for (int rg = 0; rg < 8; ++rg)
          dma<0>(rw, L_TILE + rg * 1024, ((lane >> 3) ^ (rg & 1)) * 3456 + (((lane & 7) ^ (4 * ((rg >> 1) & 1))) << 4), (64 * kr0 + 8 * rg) * 3456 + cl * 128);
      }
      FENCE();
      asm volatile("s_waitcnt lgkmcnt(0)\n\ts_barrier" : "+s"(tag)::"memory");
      if (b < 243) {
        const int cg = lane >> 3, rg = lane & 7, sec = cl / 9;
        u32 dst[8];
        UNR for (int j = 0; j < 8; ++j) {
          int cc = 64 * (cl - 9 * sec) + 8 * cg + j, h = cc / 18, d = cc - 18 * h, hl = h & 1;
          u32 o;
          if (sec == 0) {
            int hi = d >= 9, qq = 2 * hl + hi, s = d - 9 * hi;
            int row = s < 8 ? 16 * (s >> 2) + 4 * qq + (s & 3) : 32 + qq, key = s < 8 ? (row >> 1) & 7 : 0;
            o = L_WQ + kr0 * 4608 + row * 128 + ((rg ^ key) << 4);
          } else {
            int qq = 2 * (sec - 1) + hl;
            int row = d < 16 ? 16 * (d >> 2) + 4 * qq + (d & 3) : 48 + 2 * qq + d, key = d < 16 ? (row >> 1) & 7 : 2 * qq;
            o = kr0 * 9216 + row * 128 + ((rg ^ key) << 4);
          }
          dst[j] = O_WT + (h >> 1) * WT_G + o;
        }
        vmwait0();
        u4 R[8];
        const u32 rb = L_TILE + rg * 1024 + ((cg ^ (4 * ((rg >> 1) & 1))) << 4);
        UNR for (int i = 0; i < 8; ++i) R[i] = ld<u4>(rb + ((i ^ (rg & 1)) << 7));
        float cs[8];
        UNR for (int j = 0; j < 8; ++j) {
          u4 o;
          UNR for (int d = 0; d < 4; ++d) o[d] = __builtin_amdgcn_perm(R[2 * d + 1][j >> 1], R[2 * d][j >> 1], (j & 1) ? 0x07060302u : 0x05040100u);
          gst4(o, ws, dst[j], 0);
          cs[j] = dot2(o.x, 0x3F803F80u, dot2(o.y, 0x3F803F80u, 0.0f)) + dot2(o.z, 0x3F803F80u, dot2(o.w, 0x3F803F80u, 0.0f));
        }
        float sel = 0;
        UNR for (int j = 0; j < 8; ++j) {
          float v = cs[j];
          v += __uint_as_float(__builtin_amdgcn_update_dpp(0, __float_as_uint(v), 0xB1, 0xF, 0xF, true));
          v += __uint_as_float(__builtin_amdgcn_update_dpp(0, __float_as_uint(v), 0x4E, 0xF, 0xF, true));
          v += __uint_as_float(__builtin_amdgcn_update_dpp(0, __float_as_uint(v), 0x141, 0xF, 0xF, true));
          sel = rg == j ? v : sel;
        }
        gst1(__float_as_uint(sel), ws, O_CS + lane * 4, kr0 * 6912 + cl * 256);
        vmwait0();
        setflag(ws, O_FW + b * 128, tag, lane);
      }
      STAMP(1);
      {
        int l6 = lane < 54 ? lane : 0, kr = l6 / 6, i = l6 - 6 * kr;
        int ln = 9 * (i >> 1) + ((36 * g + 35 * (i & 1)) >> 6);
        poll(ws, O_FW + (kr * 27 + ln) * 128, tag, dead);
      }
      lset(CNT(C_WOK), 1);
      STAMP(2);
      u32 xp = 0;
      auto need_x = [&](u32 n) {
        if (xp < n) {
          NOUNR for (int i = dead ? SPIN_L : 0; i < SPIN_L; ++i) {
            u4 v;
            asm volatile("ds_read_b128 %0, %1\n\ts_waitcnt lgkmcnt(0)" : "=v"(v) : "v"(CNT(C_PROG)) : "memory");
            xp = rfl(umin(umin(v.x, v.y), umin(v.z, v.w)));
            if (xp >= n) return;
          }
          dead = 1;
        }
      };
      UNR for (int j = 0; j < 9; ++j) {
        need_x(((j + 1) * 9216 - 1) / 16384 + 1);
        int kr = j + rot; kr -= kr >= 9 ? 9 : 0;
        UNR for (int i = 0; i < 9; ++i) dma<SC1>(ws, j * 9216 + i * 1024, O_WT + lane * 16, g * WT_G + kr * 9216 + i * 1024);
        if (j >= 3) { vmwait<27>(); lset(CNT(C_WPROG), j - 2); }
      }
      need_x(8);
      UNR for (int i = 0; i < 41; ++i) {
        u32 o = i * 1024 + lane * 16 + rot * 4608;
        o -= o >= 41472 ? 41472 : 0;
        if (i < 40 || lane < 32) dma<SC1>(ws, L_WQ + i * 1024, O_WT + L_WQ + o, g * WT_G);
        if (i == 8) { vmwait<27>(); lset(CNT(C_WPROG), 7); }
        if (i == 17) { vmwait<27>(); lset(CNT(C_WPROG), 8); }
        if (i == 26) { vmwait<27>(); lset(CNT(C_WPROG), 9); }
      }
      vmwait0();
      lset(CNT(C_WPROG), 10);
      STAMP(3);
    } else if (w < 7) {
      KVin in0, in1;
      Hin hin;
      const int T = (w - 5) * 128 + lane, chn = 2 * b + (w - 5);
      const u32 mH = w == 5 ? L_H0 : L_H1, mU = w == 5 ? L_U0 : L_U1;
      kv_load(a, T, rt, c, in0);
      kv_load(a, T + 64, rt, c, in1);
      h_load(a, chn, lane, hin);
      FENCE();
      __builtin_amdgcn_sched_barrier(0);
      asm volatile("s_waitcnt lgkmcnt(0)\n\ts_barrier" : "+s"(tag)::"memory");
      __builtin_amdgcn_sched_barrier(0);
      kv_compute(ws, T, rt, c, in0);
      kv_compute(ws, T + 64, rt, c, in1);
      STAMP(1);
      vmwait0();
      if (ladd_rtn(CNT(C_KVC), lane) == 2) setflag(ws, O_FK + b * 128, tag, lane);
      UNR for (int i = 0; i < 4; ++i) lwait(CNT(C_PROG) + 4 * i, 9, dead);
      lwait(CNT(C_WOK), 1, dead);
      h_stage(mH, mU, lane, hin);
      poll(ws, O_FK + wg_of(lane >> 2, 4 * (lane & 3) + (2 * b) / 144) * 128, tag, dead);
      STAMP(2);
      u_stage(ws, mU, chn, lane);
      vmwait0();
      ladd(w == 5 ? CNT(C_PU0) : CNT(C_PU1), lane);
      STAMP(3);
    } else {
      if (lane < 32) lset(L_CNT + lane * 4, 0);
      const int cl = b % 9, gg = b / 9;
      if (b < 144) {
        rsrc_t rw = mkrs(a.wo, 576 * 1152);
        UNR for (int m = 0; m < 5; ++m)
          if (m < 4 || lane < 32) dma<0>(rw, L_WOTILE + m * 1024, (lane >> 3) * 1152 + (lane & 7) * 16, (36 * gg + 8 * m) * 1152 + cl * 128);
      }
      rsrc_t rp = mkrs(a.wphi, 32 * 648);
      u4 p0 = gld4(rp, lane * 16, g * 1296, 0), p1 = gld4(rp, lane < 17 ? 1024 + lane * 16 : 0x80000000u, g * 1296, 0);
      KVin in;
      if (lane < 32) kv_load(a, 256 + lane, rt, c, in);
      FENCE();
      __builtin_amdgcn_sched_barrier(0);
      asm volatile("s_waitcnt lgkmcnt(0)\n\ts_barrier" : "+s"(tag)::"memory");
      __builtin_amdgcn_sched_barrier(0);
      const u4 s4 = {SENT, SENT, SENT, SENT};
      UNR for (int i = 0; i < 3; ++i) gst4(s4, ws, (i < 2 || lane < 48) ? O_SP + i * 1024 + lane * 16 : 0x80000000u, g * SP_G + p * SP_P);
      UNR for (int i = 0; i < 9; ++i) gst4(s4, ws, O_OB + lane * 16, p * OB_P + i * 16384 + g * 1024);
      gst4(s4, ws, lane < 48 ? O_ST + q * 65536 + i16 * 16 : 0x80000000u, rt * 1024 + c * 256);
      if (lane < 32) kv_compute(ws, 256 + lane, rt, c, in);
      if (b < 144) {
        const u32 ta = L_WOTILE + (18 * (q >> 1) + 8 * (q & 1) + (i16 >> 2)) * 128 + (i16 & 3) * 8;
        UNR for (int G = 0; G < 4; ++G) gst4(trd2(ta + G * 32, ta + G * 32 + 512), ws, O_WOT + lane * 16, (4 * cl + G) * 18432 + gg * 1024);
        u2 lf = trd(L_WOTILE + (16 + ((i16 >> 2) & 1) + 18 * (i16 >> 3)) * 128 + q * 32 + (i16 & 3) * 8);
        gst2(lf, ws, O_WOT + q * 18432 + i16 * 16, 4 * cl * 18432 + (16 + (gg >> 3)) * 1024 + ((gg >> 1) & 3) * 256 + (gg & 1) * 8);
      }
      FENCE();
      asm volatile("s_waitcnt lgkmcnt(0)" ::: "memory");
      st<u4>(L_WOTILE + lane * 16, p0);
      if (lane < 17) st<u4>(L_WOTILE + 1024 + lane * 16, p1);
      FENCE();
      auto wp16 = [&](int hd, int d, int f) -> u32 { u32 v = ld<u16>(L_WOTILE + (hd * 324 + d * 18 + (f < 18 ? f : 0)) * 2); return f < 18 ? v : 0u; };
      UNR for (int rd = 0; rd < 2; ++rd) {
        int t = rd * 64 + lane, tt = t < 80 ? t : 79, m = tt >> 4, cc = (tt >> 3) & 1, hd = (tt >> 2) & 1, f = 4 * m + (tt & 3);
        u4 v;
        UNR for (int d = 0; d < 4; ++d) v[d] = wp16(hd, 8 * cc + 2 * d, f) | (wp16(hd, 8 * cc + 2 * d + 1, f) << 16);
        st<u4>(L_TK + (tt >> 3) * 144 + (tt & 7) * 16, v);
        int hq = (tt >> 3) & 1, dh = (tt >> 2) & 1;
        UNR for (int d = 0; d < 4; ++d) v[d] = wp16(hq, 9 * dh + 2 * d, f) | (wp16(hq, 9 * dh + 2 * d + 1, f) << 16);
        st<u4>(L_TQ + m * 272 + (tt & 15) * 16, v);
        st<u32>(L_TQ2 + m * 68 + (tt & 15) * 4, wp16(hq, 9 * dh + 8, f));
      }
      {
        int tt = lane < 40 ? lane : 39, m = tt >> 3, hd = (tt >> 2) & 1, f = 4 * m + (tt & 3);
        st<u32>(L_TK2 + m * 36 + (tt & 7) * 4, wp16(hd, 16, f) | (wp16(hd, 17, f) << 16));
        u4 z = {0, 0, 0, 0};
        if (lane < 10) st<u4>(L_TK + lane * 144 + 128, z);
        if (lane < 5) { st<u4>(L_TQ + lane * 272 + 256, z); st<u32>(L_TK2 + lane * 36 + 32, 0u); st<u32>(L_TQ2 + lane * 68 + 64, 0u); }
      }
      FENCE();
      vmwait0();
      setflag(ws, O_FA + b * 128, tag, lane);
      if (ladd_rtn(CNT(C_KVC), lane) == 2) setflag(ws, O_FK + b * 128, tag, lane);
      STAMP(1);
      lwait(CNT(C_WOK), 1, dead);
      UNR for (int rd = 0; rd < 2; ++rd) {
        int blk = rd * 4 + q, qq = i16 >> 2, ee = i16 & 3;
        int col;
        if (rd == 0) col = 576 + 576 * (qq >> 1) + 36 * g + 18 * (qq & 1) + 4 * blk + ee;
        else col = blk == 4 ? 576 + 576 * (qq >> 1) + 36 * g + 18 * (qq & 1) + 16 + (ee & 1) : 36 * g + 9 * qq + (blk == 7 ? 8 : 4 * (blk - 5) + ee);
        float s = 0;
        UNR for (int k = 0; k < 9; ++k) s += __uint_as_float(gld1(ws, O_CS + col * 4, k * 6912, 1));
        st<float>(L_CSL + rd * 256 + lane * 4, s);
      }
      if (lane < 18) st<u4>(L_ZERO + lane * 16, u4{0, 0, 0, 0});
      FENCE();
      ladd(CNT(C_TBL), lane);
      STAMP(2);
      {
        poll(ws, O_FA + wg_of(lane < 16 ? lane : p, lane < 16 ? g : (lane & 15)) * 128, tag, dead);
        int ln = (144 * c + 48 * q + (q == 3 ? -1 : 0)) >> 6;
        poll(ws, O_FA + (i16 * 9 + ln) * 128, tag, dead);
      }
      lset(CNT(C_FAOK), 1);
      lwait(CNT(C_WPROG), 10, dead);
      UNR for (int i = 0; i < 9; ++i) {
        u32 o = i * 1024 + lane * 16;
        o += o >= 4608 ? 18432 - 4608 : 0;
        gst4(s4, ws, O_X2 + o, 2 * rt * 18432 + c * 4608);
      }
      UNR for (int i = 0; i < 3; ++i) gst4(s4, ws, (i < 2 || lane < 48) ? O_SP + i * 1024 + lane * 16 : 0x80000000u, SP_L + g * SP_G + p * SP_P);
      UNR for (int i = 0; i < 9; ++i) gst4(s4, ws, O_OB + lane * 16, OB_L + p * OB_P + i * 16384 + g * 1024);
      STAMP(3);
    }
    u4 wo[18], wo8[18];
    lwait(CNT(C_WPROG), 10, dead);
    lwait(CNT(C_FAOK), 1, dead);
    UNR for (int i = 0; i < 18; ++i) wo[i] = gld4(ws, O_WOT + lane * 16, (9 * c + w) * 18432 + i * 1024, 1);
    lwait(pr ? CNT(C_PU1) : CNT(C_PU0), 1, dead);
    STAMP(4);
    conv_pair(a, ws, lH, lU, 2 * b + pr, e, pr ? CNT(C_PA1) : CNT(C_PA0), pr ? CNT(C_PB1) : CNT(C_PB0), 2, lane, dead);
    STAMP(5);
    vmwait0();
    if (w == 7) setflag(ws, O_FB + b * 128, tag, lane);
    if (ladd_rtn(CNT(C_FYC), lane) == 3) setflag(ws, O_FY + b * 128, tag, lane);
    state_sum<0>(a, ws, b, w, lane, g, dead STPASS);
      {
        if (c == 0 && w < 6) {
          const int ch = 512 + rt;
          lwait(CNT(C_FYC), 4, dead);
          if (w == 5) {
            Hin hin;
            h_load(a, ch, lane, hin);
            poll(ws, O_FK + wg_of(lane >> 2, 4 * (lane & 3) + 3) * 128, tag, dead);
            u_stage(ws, L_U0, ch, lane);
            h_stage(L_H0, L_U0, lane, hin);
            vmwait0();
            ladd(CNT(C_PU0), lane);
          }
          lwait(CNT(C_PU0), 2, dead);
          conv_pair(a, ws, L_H0, L_U0, ch, e, CNT(C_PA0), CNT(C_PB0), 4, lane, dead);
          vmwait0();
          if (ladd_rtn(CNT(C_FZC), lane) == 1) setflag(ws, O_FZ + (ch - 512) * 128, tag, lane);
        }
        if (w < 6) UNR for (int i = 0; i < 18; ++i) wo8[i] = gld4(ws, O_WOT + lane * 16, (9 * c + 8) * 18432 + i * 1024, 1);
        if (w == 7) poll(ws, O_FB + wg_of(lane < 16 ? lane : p, lane < 16 ? g : (lane & 15)) * 128, tag, dead);
        STAMP(8);
      }
    tail<true, 0>(a, ws, b, w, lane, g, rt, c, rot, tag, wo, wo8, dead STPASS);
    state_sum<1>(a, ws, b, w, lane, g, dead STPASS);
    tail<true, 1>(a, ws, b, w, lane, g, rt, c, rot, tag, wo, wo8, dead STPASS);
    if (b == 0 && w == 7) {
      UNR for (int i = 0; i < 4; ++i) poll(ws, O_FA + (i * 64 + lane) * 128, tag, dead);
      setflag(ws, 0, tag, lane);
    }
  }
  if (w < 4) {
    u32 m = 0x7fffffffu, base = stv - 100000u;
    for (int i = 0; i < 4; ++i) { u32 v = gld1(ws, O_STAMP + (i * 64 + lane) * 4, 0, 1) - base; m = umin(m, v); }
    for (int o = 1; o < 64; o <<= 1) m = umin(m, (u32)__shfl_xor((int)m, o));
    stv = rfl(m) + base;
  }
  STAMP(31); STAMPC(30);
}

extern "C" void kernel_launch(void* const* d_in, const int* in_sizes, int n_in, void* d_out, int out_size, void* d_ws, size_t ws_size, hipStream_t stream) {
  Args a;
  a.x = (const u16*)d_in[0]; a.wqkv = (const u16*)d_in[1]; a.wphi = (const u16*)d_in[2]; a.wo = (const u16*)d_in[3];
  a.bk = (const u16*)d_in[5]; a.bv = (const u16*)d_in[6]; a.sk = (const u16*)d_in[7]; a.ssm = (const u16*)d_in[8]; a.dv = (const u16*)d_in[9];
  a.out = (u16*)d_out; a.ws = (unsigned char*)d_ws;
  (void)hipFuncSetAttribute((const void*)fused_kernel, hipFuncAttributeMaxDynamicSharedMemorySize, LDS_BYTES);
  fused_kernel<<<256, 512, LDS_BYTES, stream>>>(a);
}
```

```cpp
#include <hip/hip_runtime.h>
#include <hip/hip_bf16.h>

typedef unsigned u32;
typedef unsigned short u16;
typedef float f2 __attribute__((ext_vector_type(2)));
typedef float f4 __attribute__((ext_vector_type(4)));
typedef u32 u2 __attribute__((ext_vector_type(2)));
typedef u32 u4 __attribute__((ext_vector_type(4)));
typedef u32 u2a __attribute__((ext_vector_type(2), aligned(4)));
typedef __bf16 bf2 __attribute__((ext_vector_type(2)));
typedef __bf16 bf8 __attribute__((ext_vector_type(8)));
typedef short s4v __attribute__((ext_vector_type(4)));
typedef __amdgpu_buffer_rsrc_t rsrc_t;
#define LAS __attribute__((address_space(3)))
#define DEV __device__ __forceinline__
#define UNR _Pragma("unroll")
#define NOUNR _Pragma("unroll 1")
#define FENCE() asm volatile("" ::: "memory")
#define SC1 16

constexpr u32 O_FW = 4096, O_FA = O_FW + 32768, O_FK = O_FA + 32768, O_FO = O_FK + 32768, O_FY = O_FO + 32768, O_FZ = O_FY + 32768, O_FB = O_FZ + 32768;
constexpr u32 O_WT = 262144, WT_G = 124416;
constexpr u32 O_CS = O_WT + 16 * WT_G;
constexpr u32 O_WOT = O_CS + 62464;
constexpr u32 O_KVT = O_WOT + 663552;
constexpr u32 O_YT = O_KVT + 2359296;
constexpr u32 O_SP = O_YT + 2359296, SP_L = 720896, SP_G = 45056, SP_P = 2816;
constexpr u32 O_OB = O_SP + 2 * SP_L, OB_L = 2359296, OB_P = 147456;
constexpr u32 O_ST = O_OB + 2 * OB_L;
constexpr u32 O_X2 = O_ST + 3 * 65536;
constexpr u32 O_STAMP = O_X2 + 2359296;
constexpr u32 WS_BYTES = O_STAMP + 256 * 8 * 32 * 8;
constexpr u32 L_WQ = 82944, L_STG = 46080;
constexpr u32 L_H0 = 124416, L_H1 = 132736, L_U0 = 141056, L_Y = 124416, L_XR = 133632;
constexpr u32 L_TILE = 147456, L_U1 = 147456, L_STB = 152064, L_ZERO = 154832, L_CSL = 155136;
constexpr u32 L_WOTILE = 155648, L_REDA = 155648, L_REDB = 157696;
constexpr u32 L_TK = 160256, L_TK2 = 161696, L_TQ = 161888, L_TQ2 = 163248, L_CNT = 163712, LDS_BYTES = 163840;
enum { C_PROG = 0, C_WPROG = 4, C_WOK, C_TBL, C_KVC, C_PHI, C_STB, C_PU0, C_PU1, C_PA0, C_PA1, C_PB0, C_PB1, C_REDA, C_REDB, C_FYC, C_FAOK, C_FZC, C_GEM, C_PUB };
#define CNT(i) (L_CNT + 4 * (i))
constexpr u32 SENT = 0xFFFFFFFFu;
constexpr int SPIN_G = 1 << 17, SPIN_L = 1 << 21;

#define MAP 1
__device__ __forceinline__ int wg_of(int p, int g) {
  return (p >> 2) * 2 + (g >> 3) + 8 * ((p & 3) * 8 + (g & 7));
}
struct Args {
  const u16 *x, *wqkv, *wphi, *wo, *bk, *bv, *sk, *ssm, *dv;
  u16* out;
  unsigned char* ws;
};

DEV int opq(int v) { asm volatile("" : "+v"(v)); return v; }
DEV u32 rfl(u32 v) { return __builtin_amdgcn_readfirstlane(v); }
DEV rsrc_t mkrs(const void* p, u32 n) { return __builtin_amdgcn_make_buffer_rsrc((void*)p, 0, n, 0x00020000); }
DEV u32 pk(float a, float b) { f2 v = {a, b}; return __builtin_bit_cast(u32, __builtin_convertvector(v, bf2)); }
DEV u32 pk(f2 v) { return __builtin_bit_cast(u32, __builtin_convertvector(v, bf2)); }
DEV f2 up(u32 p) { f2 r = {__uint_as_float(p << 16), __uint_as_float(p & 0xffff0000u)}; return r; }
DEV f2 rnd(f2 v) { return up(pk(v)); }
DEV float rnd1(float v) { return __uint_as_float(pk(v, v) & 0xffff0000u); }
DEV f4 mfma(u4 a, u4 b, f4 c) { return __builtin_amdgcn_mfma_f32_16x16x32_bf16(__builtin_bit_cast(bf8, a), __builtin_bit_cast(bf8, b), c, 0, 0, 0); }
DEV u32 umin(u32 a, u32 b) { return a < b ? a : b; }
DEV u32 umax(u32 a, u32 b) { return a > b ? a : b; }
DEV u32 umax3(u32 a, u32 b, u32 c) { return umax(umax(a, b), c); }
DEV u32 san(u32 v) { return umin(v, 0xFFFFFFFEu); }
DEV u4 san(u4 v) { u4 r = {san(v.x), san(v.y), san(v.z), san(v.w)}; return r; }
DEV u32 vm(u32 m, u4 v) { return umax3(umax3(m, v.x, v.y), v.z, v.w); }
DEV bool anyl(bool c) { return __builtin_amdgcn_ballot_w64(c) != 0; }
DEV float dot2(u32 a, u32 b, float c) { return __builtin_amdgcn_fdot2_f32_bf16(__builtin_bit_cast(bf2, a), __builtin_bit_cast(bf2, b), c, false); }
template <class T> DEV T ld(u32 o) { return *(LAS T*)(uintptr_t)o; }
template <class T> DEV void st(u32 o, T v) { *(LAS T*)(uintptr_t)o = v; }
DEV u2 trd(u32 o) { return __builtin_bit_cast(u2, __builtin_amdgcn_ds_read_tr16_b64_v4i16((LAS s4v*)(uintptr_t)o)); }
DEV u4 trd2(u32 o0, u32 o1) { u2 a = trd(o0), b = trd(o1); u4 r = {a.x, a.y, b.x, b.y}; return r; }
DEV u4 gld4(rsrc_t r, u32 v, u32 s, int aux) { return aux ? __builtin_amdgcn_raw_buffer_load_b128(r, v, s, SC1) : __builtin_amdgcn_raw_buffer_load_b128(r, v, s, 0); }
DEV u2 gld2(rsrc_t r, u32 v, u32 s, int aux) { return aux ? __builtin_amdgcn_raw_buffer_load_b64(r, v, s, SC1) : __builtin_amdgcn_raw_buffer_load_b64(r, v, s, 0); }
DEV u32 gld1(rsrc_t r, u32 v, u32 s, int aux) { return aux ? __builtin_amdgcn_raw_buffer_load_b32(r, v, s, SC1) : __builtin_amdgcn_raw_buffer_load_b32(r, v, s, 0); }
DEV void gst4(u4 d, rsrc_t r, u32 v, u32 s) { __builtin_amdgcn_raw_buffer_store_b128(d, r, v + s, 0, SC1); }
DEV void gst2(u2 d, rsrc_t r, u32 v, u32 s) { __builtin_amdgcn_raw_buffer_store_b64(d, r, v, s, SC1); }
DEV void gst1(u32 d, rsrc_t r, u32 v, u32 s) { __builtin_amdgcn_raw_buffer_store_b32(d, r, v, s, SC1); }
template <int AUX> DEV void dma(rsrc_t r, u32 l, u32 v, u32 s) {
#if defined(__HIP_DEVICE_COMPILE__)
  __builtin_amdgcn_raw_ptr_buffer_load_lds(r, (LAS void*)(uintptr_t)l, 16, v, s, 0, AUX);
#endif
}
DEV void vmwait0() { asm volatile("s_waitcnt vmcnt(0)" ::: "memory"); }
template <int N> DEV void vmwait() { asm volatile("s_waitcnt vmcnt(%0)" ::"n"(N) : "memory"); }
DEV void bar() { asm volatile("s_waitcnt lgkmcnt(0)\n\ts_barrier" ::: "memory"); }
DEV void lset(u32 a, u32 v) { asm volatile("ds_write_b32 %0, %1" ::"v"(a), "v"(v) : "memory"); }
DEV void ladd(u32 a, int lane) { if (lane == 0) { u32 one = 1; asm volatile("ds_add_u32 %0, %1" ::"v"(a), "v"(one) : "memory"); } }
DEV u32 ladd_rtn(u32 a, int lane) {
  u32 r = 0;
  if (lane == 0) { u32 one = 1; asm volatile("ds_add_rtn_u32 %0, %1, %2\n\ts_waitcnt lgkmcnt(0)" : "=v"(r) : "v"(a), "v"(one) : "memory"); }
  return rfl(r);
}
DEV u32 lget(u32 a) { u32 r; asm volatile("ds_read_b32 %0, %1\n\ts_waitcnt lgkmcnt(0)" : "=v"(r) : "v"(a) : "memory"); return rfl(r); }
DEV u32 lwait(u32 a, u32 t, int& dead) {
  u32 r = lget(a);
  if (__builtin_expect(r < t, 0)) {
    NOUNR for (int i = dead ? SPIN_L : 0; i < SPIN_L; ++i) { r = lget(a); if (r >= t) return r; __builtin_amdgcn_s_sleep(1); }
    dead = 1;
  }
  return r;
}
DEV void poll(rsrc_t ws, u32 off, u32 tag, int& dead) {
  NOUNR for (int i = dead ? SPIN_G : 0; i < SPIN_G; ++i) {
    FENCE();
    u32 v = gld1(ws, off, 0, 1);
    if (!anyl(v != tag)) { FENCE(); return; }
    __builtin_amdgcn_s_sleep(1);
  }
  dead = 1;
}
DEV void setflag(rsrc_t ws, u32 off, u32 tag, int lane) { if (lane == 0) gst1(tag, ws, off, 0); }
#define SW16(a, b) __builtin_amdgcn_permlane16_swap(a, b, false, false)
#define SW32(a, b) __builtin_amdgcn_permlane32_swap(a, b, false, false)
DEV float sw16add(float a, float b) { auto r = SW16(__float_as_uint(a), __float_as_uint(b)); return __uint_as_float(r[0]) + __uint_as_float(r[1]); }
DEV float sw32add(float a, float b) { auto r = SW32(__float_as_uint(a), __float_as_uint(b)); return __uint_as_float(r[0]) + __uint_as_float(r[1]); }

#define STAMP(i)
#define STAMPC(i)
#define STARG
#define STPASS

DEV f2 ln_stats(float s, float sq) {
  float mean = s * (1.0f / 576.0f);
  float var = fmaxf(sq * (1.0f / 576.0f) - mean * mean, 0.0f);
  float sd = rnd1(__builtin_amdgcn_sqrtf(rnd1(rnd1(var) + 1e-5f)));
  f2 r = {rnd1(mean), __builtin_amdgcn_rcpf(sd)};
  return r;
}
DEV void stats_from(u4 v, float (&m)[2], float (&rs)[2]) {
  float s = __uint_as_float(v.x) + __uint_as_float(v.z), sq = __uint_as_float(v.y) + __uint_as_float(v.w);
  s = sw16add(s, s); sq = sw16add(sq, sq);
  f2 t = ln_stats(s, sq);
  auto r0 = SW32(__float_as_uint(t.x), __float_as_uint(t.x));
  auto r1 = SW32(__float_as_uint(t.y), __float_as_uint(t.y));
  m[0] = __uint_as_float(r0[0]); m[1] = __uint_as_float(r0[1]); rs[0] = __uint_as_float(r1[0]); rs[1] = __uint_as_float(r1[1]);
}
DEV u4 ld_stats(rsrc_t ws, u32 off) { u2 a = gld2(ws, off, 0, 1), b = gld2(ws, off + 256, 0, 1); return u4{a.x, a.y, b.x, b.y}; }
DEV u4 poll_stats(rsrc_t ws, u32 off, u4 v, int& dead) {
  if (anyl(vm(0, v) == SENT)) {
    NOUNR for (int i = dead ? SPIN_G : 0; i < SPIN_G; ++i) {
      FENCE();
      v = ld_stats(ws, off);
      if (!anyl(vm(0, v) == SENT)) { FENCE(); return v; }
    }
    dead = 1;
  }
  FENCE();
  return v;
}

DEV void pub_stats(rsrc_t ws, float s0, float s1, float q0, float q1, u32 red, u32 cnt, u32 last, u32 dst, int w, int lane) {
  float t = sw16add(sw32add(s0, s1), sw32add(q0, q1));
  st<float>(red + w * 256 + (16 * (lane >> 5) + (lane & 15)) * 8 + ((lane >> 4) & 1) * 4, t);
  FENCE();
  if (ladd_rtn(cnt, lane) == last) {
    f4 r = {0, 0, 0, 0};
    UNR for (int i = 0; i < 8; ++i) r += ld<f4>(red + i * 256 + (lane & 15) * 16);
    gst4(san(__builtin_bit_cast(u4, r)), ws, lane < 16 ? dst + lane * 16 : 0x80000000u, 0);
  }
}

struct KVin { u32 k[11], v[8]; u4 w; };
DEV void kv_load(const Args& a, int T, int rt, int c, KVin& in) {
  rsrc_t rk = mkrs(a.bk, 2359296), rv = mkrs(a.bv, 2359296), rw = mkrs(a.sk, 4608);
  int cp = T >> 2, sub = T & 3, ch = 144 * c + 2 * cp, s0 = 32 * rt + 8 * sub;
  u32 base = s0 * 1152 + ch * 2;
  UNR for (int i = 0; i < 3; ++i) in.k[i] = gld1(rk, s0 == 0 ? 0x80000000u : base - (3 - i) * 1152, 0, 0);
  UNR for (int i = 0; i < 8; ++i) in.k[3 + i] = gld1(rk, base, i * 1152, 0);
  UNR for (int i = 0; i < 8; ++i) in.v[i] = gld1(rv, base, i * 1152, 0);
  in.w = gld4(rw, ch * 8, 0, 0);
}
DEV void kv_compute(rsrc_t ws, int T, int rt, int c, const KVin& in) {
  int cp = T >> 2, sub = T & 3, ch = 144 * c + 2 * cp, s0 = 32 * rt + 8 * sub;
  f2 w0 = {__uint_as_float(in.w.x << 16), __uint_as_float(in.w.z << 16)}, w1 = {__uint_as_float(in.w.x & 0xffff0000u), __uint_as_float(in.w.z & 0xffff0000u)};
  f2 w2 = {__uint_as_float(in.w.y << 16), __uint_as_float(in.w.w << 16)}, w3 = {__uint_as_float(in.w.y & 0xffff0000u), __uint_as_float(in.w.w & 0xffff0000u)};
  f2 kf[11];
  UNR for (int i = 0; i < 11; ++i) kf[i] = up(in.k[i]);
  u32 P[8];
  UNR for (int s = 0; s < 8; ++s) {
    f2 t = rnd(kf[s] * w3);
    t = rnd(t + rnd(kf[s + 1] * w2));
    t = rnd(t + rnd(kf[s + 2] * w1));
    t = rnd(t + rnd(kf[s + 3] * w0));
    P[s] = pk(t * up(in.v[s]));
  }
  u4 o0, o1;
  UNR for (int d = 0; d < 4; ++d) {
    o0[d] = __builtin_amdgcn_perm(P[6 - 2 * d], P[7 - 2 * d], 0x05040100u);
    o1[d] = __builtin_amdgcn_perm(P[6 - 2 * d], P[7 - 2 * d], 0x07060302u);
  }
  u32 off = O_KVT + ch * 4096 + (2040 - s0) * 2;
  gst4(o0, ws, off, 0);
  gst4(o1, ws, off, 4096);
}

struct Hin { u4 h[4]; u32 n[4]; };
DEV void h_load(const Args& a, int ch, int lane, Hin& in) {
  rsrc_t rh = mkrs(a.ssm, 2359296);
  UNR for (int m = 0; m < 4; ++m) { in.h[m] = gld4(rh, ch * 4096 + lane * 16, m * 1024, 0); in.n[m] = gld1(rh, ch * 4096 + lane * 16 + 16, m * 1024, 0); }
}
DEV void h_stage(u32 lH, u32 lU, int lane, const Hin& in) {
  UNR for (int m = 0; m < 4; ++m) {
    u4 h = in.h[m];
    u4 s = {__builtin_amdgcn_alignbit(h.y, h.x, 16), __builtin_amdgcn_alignbit(h.z, h.y, 16), __builtin_amdgcn_alignbit(h.w, h.z, 16), __builtin_amdgcn_alignbit(in.n[m], h.w, 16)};
    st<u4>(lH + 32 + m * 1024 + lane * 16, h);
    st<u4>(lH + 4160 + 32 + m * 1024 + lane * 16, s);
  }
  u32 h0 = rfl(in.h[0].x) << 16;
  u4 z = {0, 0, 0, lane == 3 ? h0 : 0u};
  if (lane < 4) st<u4>(lH + (lane >> 1) * 4160 + (lane & 1) * 16, z);
  u4 zz = {0, 0, 0, 0};
  if (lane < 30) st<u4>(lU + 4096 + lane * 16, zz);
}
DEV void u_stage(rsrc_t ws, u32 lU, int ch, int lane) {
  UNR for (int m = 0; m < 4; ++m) dma<SC1>(ws, lU + m * 1024, O_KVT + ch * 4096 + lane * 16, m * 1024);
}
DEV void conv_pair(const Args& a, rsrc_t ws, u32 lH, u32 lU, int ch, int e, u32 cA, u32 cB, u32 tgt, int lane, int& dead) {
  const int q = lane >> 4, i16 = lane & 15;
  f4 acc[8];
  UNR for (int i = 0; i < 8; ++i) acc[i] = f4{0, 0, 0, 0};
  const u32 aB = lU + 2 * (2032 - 16 * i16 + 8 * q) - 3584 + 256 * e;
  const u32 aA = lH + ((i16 & 1) ? 0 : 4160) + 4 * (((i16 + 1) >> 1) + 4 * q) + 256 * e;
  const u32 aA4[4] = {aA, aA + 1024, aA + 2048, aA + 3072};
  float dv = __uint_as_float((u32)a.dv[ch] << 16);
  u4 B[8];
  u2 Al[8], Ah[8];
#define CLD(t, bb)                                                                                                              \
  asm volatile("ds_read2_b32 %0, %3 offset0:%5 offset1:%6\n\tds_read2_b32 %1, %3 offset0:%7 offset1:%8\n\tds_read_b128 %2, %4 offset:%9" \
               : "=&v"(Al[t]), "=&v"(Ah[t]), "=&v"(B[7 - (t)])                                                                    \
               : "v"(aA4[(t) >> 1]), "v"(aB), "n"(16 * (bb) + 128 * ((t) & 1)), "n"(16 * (bb) + 128 * ((t) & 1) + 1),              \
                 "n"(16 * (bb) + 128 * ((t) & 1) + 2), "n"(16 * (bb) + 128 * ((t) & 1) + 3), "n"(512 * (t) + 64 * (bb))           \
               : "memory")
  UNR for (int t = 0; t < 8; ++t) CLD(t, 0);
  UNR for (int bb = 0; bb < 4; ++bb) {
    asm volatile("s_waitcnt lgkmcnt(6)" : "+v"(Al[0]), "+v"(Ah[0]), "+v"(Al[1]), "+v"(Ah[1]), "+v"(Al[2]), "+v"(Ah[2]), "+v"(Al[3]), "+v"(Ah[3]), "+v"(Al[4]),
                 "+v"(Ah[4]), "+v"(Al[5]), "+v"(Ah[5]), "+v"(B[7]), "+v"(B[6]), "+v"(B[5]), "+v"(B[4]), "+v"(B[3]), "+v"(B[2]));
    UNR for (int t = 0; t < 8; ++t) {
      u4 A = {Al[t].x, Al[t].y, Ah[t].x, Ah[t].y};
      UNR for (int v = 7 - t; v >= 0; --v) {
        if (t == 0 && v == 1) {
          __builtin_amdgcn_sched_barrier(0);
          asm volatile("s_waitcnt lgkmcnt(0)" : "+v"(Al[6]), "+v"(Ah[6]), "+v"(Al[7]), "+v"(Ah[7]), "+v"(B[1]), "+v"(B[0]));
        }
        acc[t + v] = mfma(A, B[v], acc[t + v]);
      }
      __builtin_amdgcn_sched_barrier(0);
      if (bb < 3) CLD(t, bb + 1);
    }
  }
  FENCE();
  ladd(cA, lane);
  lwait(cA, tgt, dead);
  f4 keep[4];
  UNR for (int i = 0; i < 4; ++i) {
    keep[i] = e ? acc[4 + i] : acc[i];
    f4 snd = e ? acc[i] : acc[4 + i];
    st<f4>(lH + e * 4096 + i * 1024 + lane * 16, snd);
  }
  FENCE();
  ladd(cB, lane);
  lwait(cB, tgt, dead);
  u2 Y[4];
  UNR for (int i = 0; i < 4; ++i) {
    f4 s = keep[i] + ld<f4>(lH + (1 - e) * 4096 + i * 1024 + lane * 16);
    u2 uu = ld<u2>(lU + 2 * (2044 - 256 * (4 * e + i) - 16 * i16 - 4 * q));
    f2 u32_ = up(uu.x), u10 = up(uu.y);
    f2 g01 = {u10.y * dv, u10.x * dv}, g23 = {u32_.y * dv, u32_.x * dv};
    f2 s01 = {s.x, s.y}, s23 = {s.z, s.w};
    Y[i].x = pk(rnd(s01) + rnd(g01));
    Y[i].y = pk(rnd(s23) + rnd(g23));
  }
  UNR for (int i = 0; i < 2; ++i) {
    auto r0 = SW16(Y[2 * i].x, Y[2 * i + 1].x);
    auto r1 = SW16(Y[2 * i].y, Y[2 * i + 1].y);
    u4 o = {r0[0], r1[0], r0[1], r1[1]};
    gst4(o, ws, O_YT + ch * 4096 + 2 * (256 * (4 * e + 2 * i + (q & 1)) + 16 * i16 + 8 * (q >> 1)), 0);
  }
}

template <bool HELP, int L>
DEV void tail(const Args& a, rsrc_t ws, int b, int w, int lane0, int g, int rt, int c, int rot, u32 tag, const u4 (&wo)[18], const u4 (&wo8)[18], int& dead STARG) {
  const int lane = opq(lane0), q = lane >> 4, i16 = lane & 15;
  const bool ex = HELP && w < 6;
  const int tb8 = w & 1;
  u2 xr[3];
  if (L == 0) {
    rsrc_t rx = mkrs(a.x, 2359296);
    u32 o = (32 * rt + i16) * 1152 + (144 * c + 4 * q) * 2;
    xr[0] = gld2(rx, o, w * 32, 0); xr[1] = gld2(rx, o, w * 32 + 16 * 1152, 0);
    xr[2] = gld2(rx, o, 256 + tb8 * 16 * 1152, 0);
  } else {
    xr[0] = ld<u2>(L_XR + w * 1024 + lane * 8); xr[1] = ld<u2>(L_XR + w * 1024 + 512 + lane * 8);
    xr[2] = ld<u2>(L_XR + 8192 + tb8 * 512 + lane * 8);
  }
  {
    const u32 pb = L * OB_L + (rt >> 2) * OB_P, tbp = 2 * (rt & 3);
    u4 v[5];
    if (HELP) lwait(CNT(C_PUB), 8 * L + 8, dead);
    NOUNR for (int it = dead ? SPIN_G - 1 : 0;; ++it) {
      FENCE();
      UNR for (int i = 0; i < 4; ++i) v[i] = gld4(ws, O_OB + lane * 16, pb + (tbp + (i >> 1)) * 16384 + (w + 8 * (i & 1)) * 1024, 1);
      if (!HELP) {
        u32 o = O_OB + 131072 + q * 2048 + i16 * 8, so = pb + (w & 1) * 8192 + (tbp + (w >> 1)) * 128;
        u2 l0 = gld2(ws, o, so, 1), l1 = gld2(ws, o + 1024, so, 1);
        v[4] = u4{l0.x, l0.y, l1.x, l1.y};
      }
      u32 m = 0;
      UNR for (int i = 0; i < (HELP ? 4 : 5); ++i) m = vm(m, v[i]);
      if (!anyl(m == SENT)) break;
      if (it >= SPIN_G) { dead = 1; break; }
    }
    FENCE();
    UNR for (int i = 0; i < 4; ++i) st<u4>(L_STG + (i >> 1) * 18432 + (w + 8 * (i & 1)) * 1024 + lane * 16, v[i]);
    if (!HELP) st<u4>(L_STG + (w >> 1) * 18432 + (16 + (w & 1)) * 1024 + lane * 16, v[4]);
  }
  STAMP(16 + L * 8);
  bar();
  STAMP(17 + L * 8);
  u32 fyoff = 0, fy = tag;
  const int chl = 144 * c + ((lane & 16) ? 128 : 16 * w) + i16;
  if (L == 0) { fyoff = chl < 512 ? O_FY + (chl >> 1) * 128 : O_FZ + (chl - 512) * 128; fy = gld1(ws, fyoff, 0, 1); }
  f4 acc[3] = {f4{0, 0, 0, 0}, f4{0, 0, 0, 0}, f4{0, 0, 0, 0}};
  if (ex) {
    UNR for (int ks = 0; ks < 18; ++ks) {
      u4 b0 = ld<u4>(L_STG + ks * 1024 + lane * 16), b1 = ld<u4>(L_STG + 18432 + ks * 1024 + lane * 16);
      u4 b2 = ld<u4>(L_STG + tb8 * 18432 + ks * 1024 + lane * 16);
      acc[0] = mfma(wo[ks], b0, acc[0]); acc[1] = mfma(wo[ks], b1, acc[1]); acc[2] = mfma(wo8[ks], b2, acc[2]);
    }
  } else {
    UNR for (int ks = 0; ks < 18; ++ks) {
      u4 b0 = ld<u4>(L_STG + ks * 1024 + lane * 16), b1 = ld<u4>(L_STG + 18432 + ks * 1024 + lane * 16);
      acc[0] = mfma(wo[ks], b0, acc[0]); acc[1] = mfma(wo[ks], b1, acc[1]);
    }
  }
  u4 yv, yv8;
  if (L == 0) {
    if (anyl(fy != tag)) poll(ws, fyoff, tag, dead);
    FENCE();
    yv = gld4(ws, O_YT + (lane >> 2) * 4096 + (lane & 3) * 16, (144 * c + 16 * w) * 4096 + rt * 64, 1);
    yv8 = gld4(ws, O_YT + (lane >> 2) * 4096 + (lane & 3) * 16, (144 * c + 128) * 4096 + rt * 64, 1);
  }
  constexpr int NU = HELP ? 3 : 2;
  f2 x1[NU][2];
  float S[NU], Q[NU];
  UNR for (int u = 0; u < NU; ++u) {
    f2 lo = {acc[u].x, acc[u].y}, hi = {acc[u].z, acc[u].w};
    x1[u][0] = rnd(rnd(lo) + up(xr[u].x)); x1[u][1] = rnd(rnd(hi) + up(xr[u].y));
    f2 s = x1[u][0] + x1[u][1], sq = x1[u][0] * x1[u][0] + x1[u][1] * x1[u][1];
    S[u] = s.x + s.y; Q[u] = sq.x + sq.y;
  }
  if (HELP) { if (w == 4) { S[0] += S[2]; Q[0] += Q[2]; } if (w == 5) { S[1] += S[2]; Q[1] += Q[2]; } }
  const u32 stoff = O_ST + L * 2 * 65536 + rt * 1024 + (q & 1) * 512 + (16 * (q >> 1) + i16) * 8;
  pub_stats(ws, S[0], S[1], Q[0], Q[1], L_REDA, CNT(C_REDA), 8 * L + 7, O_ST + L * 2 * 65536 + rt * 1024 + c * 256, w, lane);
  STAMP(18 + L * 8);
  if (HELP && L == 0 && w == 4) {
    lwait(CNT(C_REDA), 8, dead);
    UNR for (int j = 5; j < 9; ++j) {
      int kr = j + rot; kr -= kr >= 9 ? 9 : 0;
      UNR for (int i = 0; i < 9; ++i) dma<SC1>(ws, j * 9216 + i * 1024, O_WT + lane * 16, g * WT_G + kr * 9216 + i * 1024);
    }
  }
  u4 sv = ld_stats(ws, stoff);
  if (L == 0) {
    st<u4>(L_Y + w * 1024 + lane * 16, yv);
    if (ex) st<u4>(L_Y + 8192 + lane * 16, yv8);
    FENCE();
  }
  u2 y[NU];
  {
    u32 ya = L_Y + (4 * q + (i16 >> 2)) * 64 + (i16 & 3) * 8;
    y[0] = trd(ya + w * 1024); y[1] = trd(ya + w * 1024 + 32);
    if (HELP) y[2] = trd(ya + 8192 + tb8 * 32);
  }
  sv = poll_stats(ws, stoff, sv, dead);
  STAMP(19 + L * 8);
  if (HELP && L == 0 && w == 4) { vmwait0(); lset(CNT(C_WPROG), 11); }
  float m[2], rs[2];
  stats_from(sv, m, rs);
  u2 x2[NU];
  UNR for (int u = 0; u < NU; ++u) {
    float mu = u < 2 ? m[u] : (tb8 ? m[1] : m[0]), ru = u < 2 ? rs[u] : (tb8 ? rs[1] : rs[0]);
    UNR for (int h = 0; h < 2; ++h) {
      f2 xn = rnd(rnd(x1[u][h] - mu) * ru);
      x2[u][h] = pk(x1[u][h] + rnd(xn * up(y[u][h])));
    }
  }
  auto r0 = SW16(x2[0].x, x2[1].x);
  auto r1 = SW16(x2[0].y, x2[1].y);
  u4 o = {r0[0], r1[0], r0[1], r1[1]};
  if (L == 0) {
    gst4(san(o), ws, O_X2 + (q & 1) * 18432 + (q >> 1) * 256 + i16 * 16, 2 * rt * 18432 + (9 * c + w) * 512);
    if (ex) {
      auto t0 = SW16(x2[NU - 1].x, x2[NU - 1].x);
      auto t1 = SW16(x2[NU - 1].y, x2[NU - 1].y);
      gst4(san(u4{t0[0], t1[0], t0[1], t1[1]}), ws, (q & 1) ? 0x80000000u : O_X2 + (q >> 1) * 256 + i16 * 16, (2 * rt + tb8) * 18432 + (9 * c + 8) * 512);
    }
    st<u2>(L_XR + w * 1024 + lane * 8, x2[0]); st<u2>(L_XR + w * 1024 + 512 + lane * 8, x2[1]);
    if (ex) st<u2>(L_XR + 8192 + tb8 * 512 + lane * 8, x2[NU - 1]);
    UNR for (int u = 0; u < NU; ++u) {
      f2 lo = up(x2[u].x), hi = up(x2[u].y);
      f2 s = lo + hi, sq = lo * lo + hi * hi;
      S[u] = s.x + s.y; Q[u] = sq.x + sq.y;
    }
    if (HELP) { if (w == 4) { S[0] += S[2]; Q[0] += Q[2]; } if (w == 5) { S[1] += S[2]; Q[1] += Q[2]; } }
    pub_stats(ws, S[0], S[1], Q[0], Q[1], L_REDB, CNT(C_REDB), 7, O_ST + 65536 + rt * 1024 + c * 256, w, lane);
  } else {
    rsrc_t ro = mkrs(a.out, 2359296);
    __builtin_amdgcn_raw_buffer_store_b128(o, ro, (16 * (q & 1) + i16) * 1152 + (q >> 1) * 16 + 32 * rt * 1152 + 288 * c + 32 * w, 0, 0);
    if (ex) __builtin_amdgcn_raw_buffer_store_b64(x2[NU - 1], ro, i16 * 1152 + q * 8, (32 * rt + 16 * tb8) * 1152 + 288 * c + 256, 0);
  }
  STAMP(20 + L * 8);
}

template <int L>
DEV void main_layer(const Args& a, rsrc_t ws, int b, int w, int lane0, int p, int g, int rt, int c, int rot, u32 tag, u4 (&B)[36], float (&mS)[2], float (&mQ)[2], u32& wp, int& dead STARG) {
    constexpr float LOG2E = 1.4426950408889634f;
    int lane = opq(lane0), q = lane >> 4, i16 = lane & 15;
    u32 aF[2], a4[2], a7[2];
    UNR for (int u = 0; u < 2; ++u) {
      aF[u] = i16 * 128 + (((4 * u + q) ^ (i16 >> 1)) << 4);
      a4[u] = (64 + 2 * (i16 >> 2) + (i16 & 1)) * 128 + (((4 * u + q) ^ (2 * (i16 >> 2))) << 4);
      a7[u] = L_WQ + (32 + (i16 >> 2)) * 128 + ((4 * u + q) << 4);
    }
    {
      f4 acc[5][2];
      u4 sv = {0, 0, 0, 0};
      const u32 stoff = O_ST + 65536 + (4 * p + w) * 1024 + (q & 1) * 512 + (16 * (q >> 1) + i16) * 8;
      if (L == 1) {
        u32 G = lane < 36 ? lane : (lane < 40 ? 9 * (lane - 36) + 8 : 0);
        u32 po = O_X2 + (8 * p + 2 * w + (lane >= 36 && lane < 40 ? 1 : 0)) * 18432 + G * 512;
        NOUNR for (int i = dead ? SPIN_G : 0; i < SPIN_G; ++i) {
          FENCE();
          u32 v = gld1(ws, po, 0, 1);
          if (!anyl(v == SENT)) break;
        }
        FENCE();
        STAMP(21);
      }
      for (int pass = 0;; ++pass) {
        if (L == 1) {
          UNR for (int j = 0; j < 9; ++j) {
            int kr = j + rot; kr -= kr >= 9 ? 9 : 0;
            UNR for (int i = 0; i < 4; ++i) B[4 * j + i] = gld4(ws, O_X2 + lane * 16 + (i >> 1) * 1024, (8 * p + 2 * w + (i & 1)) * 18432 + kr * 2048, 1);
          }
          sv = ld_stats(ws, stoff);
        }
        UNR for (int i = 0; i < 5; ++i) acc[i][0] = acc[i][1] = f4{0, 0, 0, 0};
        u32 vmx = 0;
        if (wp < (L ? 0u : 1u)) wp = lwait(CNT(C_WPROG), 1, dead);
        if (L == 0) STAMP(3);
        u4 A[2][5];
        UNR for (int i = 0; i < 4; ++i) A[0][i] = ld<u4>(aF[0] + i * 2048);
        A[0][4] = ld<u4>(a4[0]);
        u32 fl = *(volatile LAS u32*)(uintptr_t)CNT(C_WPROG);
        UNR for (int s = 0; s < 18; ++s) {
          if (s < 17) {
            const int j = (s + 1) >> 1, u = (s + 1) & 1;
            if (u == 0) {
              u32 need = L ? (j < 5 ? 0u : 11u) : (u32)(j + 1);
              wp = umax(wp, rfl(fl));
              if (wp < need) wp = lwait(CNT(C_WPROG), need, dead);
            }
            UNR for (int i = 0; i < 4; ++i) A[(s + 1) & 1][i] = ld<u4>(aF[u] + j * 9216 + i * 2048);
            A[(s + 1) & 1][4] = ld<u4>(a4[u] + j * 9216);
            if (u == 0) fl = *(volatile LAS u32*)(uintptr_t)CNT(C_WPROG);
          }
          vmx = vm(vm(vmx, B[2 * s]), B[2 * s + 1]);
          UNR for (int i = 0; i < 5; ++i) {
            acc[i][0] = mfma(A[s & 1][i], B[2 * s], acc[i][0]);
            acc[i][1] = mfma(A[s & 1][i], B[2 * s + 1], acc[i][1]);
          }
        }
        if (L == 0 || !anyl(vmx == SENT) || dead) break;
        if (pass >= 20000) { dead = 1; break; }
        FENCE();
      }
      FENCE();
      ladd(CNT(C_GEM), lane);
      STAMP(4 + L * 6);
      if (L == 0) lwait(CNT(C_TBL), 1, dead);
      else { sv = poll_stats(ws, stoff, sv, dead); stats_from(sv, mS, mQ); }
      {
        u32 kv[2][9];
        UNR for (int i = 0; i < 5; ++i) {
          f4 cs = ld<f4>(L_CSL + i * 64 + q * 16);
          f2 c01 = {cs.x, cs.y}, c23 = {cs.z, cs.w};
          UNR for (int tb = 0; tb < 2; ++tb) {
            f2 lo = {acc[i][tb].x, acc[i][tb].y}, hi = {acc[i][tb].z, acc[i][tb].w};
            kv[tb][i < 4 ? 2 * i : 8] = pk((lo - c01 * mS[tb]) * mQ[tb]);
            if (i < 4) kv[tb][2 * i + 1] = pk((hi - c23 * mS[tb]) * mQ[tb]);
          }
        }
        u32 K[9], V[9];
        UNR for (int i = 0; i < 9; ++i) { auto t = SW32(kv[0][i], kv[1][i]); K[i] = t[0]; V[i] = t[1]; }
        const bool val = (i16 >> 2) == q;
        const u32 tk = L_TK + (val ? ((q & 1) * 4 + (i16 & 3)) * 16 : 128), tk2 = L_TK2 + (val ? ((q & 1) * 4 + (i16 & 3)) * 4 : 32);
        const u4 k0 = {K[0], K[1], K[2], K[3]}, k1 = {K[4], K[5], K[6], K[7]}, k2 = {K[8], 0, 0, 0};
        f4 P[5];
        UNR for (int m = 0; m < 5; ++m) {
          u4 t2 = {ld<u32>(tk2 + m * 36), 0, 0, 0};
          P[m] = mfma(ld<u4>(tk + m * 288), k0, f4{0, 0, 0, 0});
          P[m] = mfma(ld<u4>(tk + m * 288 + 144), k1, P[m]);
          P[m] = mfma(t2, k2, P[m]);
        }
        const u32 row = L_STG + (32 * w + 16 * (q >> 1) + i16) * 232;
        f2 z[9];
        UNR for (int i = 0; i < 9; ++i) z[i] = f2{P[i >> 1][2 * (i & 1)], P[i >> 1][2 * (i & 1) + 1]};
        float mx = fmaxf(z[0].x, z[0].y), mn = fminf(z[0].x, z[0].y);
        UNR for (int i = 1; i < 9; ++i) { mx = fmaxf(fmaxf(mx, z[i].x), z[i].y); mn = fminf(fminf(mn, z[i].x), z[i].y); }
        const float ma = -mx * LOG2E, mb = mn * LOG2E;
        f2 e1[9], e2[9], s1 = {0, 0}, s2 = {0, 0};
        UNR for (int i = 0; i < 9; ++i) {
          f2 t1 = z[i] * LOG2E + ma, t2 = mb - z[i] * LOG2E;
          e1[i] = f2{__builtin_amdgcn_exp2f(t1.x), __builtin_amdgcn_exp2f(t1.y)};
          e2[i] = f2{__builtin_amdgcn_exp2f(t2.x), __builtin_amdgcn_exp2f(t2.y)};
          s1 += e1[i]; s2 += e2[i];
        }
        const float r1 = __builtin_amdgcn_rcpf(s1.x + s1.y), r2 = __builtin_amdgcn_rcpf(s2.x + s2.y);
        lwait(CNT(C_GEM), 4 * (L + 1), dead);
        {
          u32 vo = row + 144 + (q & 1) * 40;
          UNR for (int i = 0; i < 4; ++i) st<u2>(vo + 8 * i, u2{V[2 * i], V[2 * i + 1]});
          st<u2>(vo + 32, u2{V[8], 0x00003F80u});
        }
        u32 fo = row + (q & 1) * 72;
        UNR for (int i = 0; i < 4; ++i) {
          st<u2>(fo + 8 * i, u2{pk(e1[2 * i] * r1), pk(e1[2 * i + 1] * r1)});
          st<u2>(fo + 32 + 8 * i, u2{pk(e2[2 * i] * r2), pk(e2[2 * i + 1] * r2)});
        }
        st<u2>(fo + 64, u2{pk(e1[8] * r1), pk(e2[8] * r2)});
        FENCE();
        ladd(CNT(C_PHI), lane);
        lwait(CNT(C_PHI), 4 * (L + 1), dead);
        STAMP(5 + L * 6);
        const int hd = w >> 1, db = w & 1;
        const u32 ta = L_STG + (16 * (q & 1) + 4 * (i16 >> 2) + (q >> 1)) * 232 + (i16 & 3) * 8;
        f4 Dd[3] = {f4{0, 0, 0, 0}, f4{0, 0, 0, 0}, f4{0, 0, 0, 0}};
        const u32 tf = ta + 72 * hd, tv = ta + 144 + 40 * hd + 32 * db;
        UNR for (int ks = 0; ks < 4; ++ks) {
          u4 bv = trd2(tv + ks * 7424, tv + ks * 7424 + 464);
          UNR for (int fb = 0; fb < 3; ++fb) Dd[fb] = mfma(trd2(tf + ks * 7424 + fb * 32, tf + ks * 7424 + fb * 32 + 464), bv, Dd[fb]);
        }
        auto s0 = SW16(pk(Dd[0].x, Dd[0].y), pk(Dd[1].x, Dd[1].y));
        auto s1b = SW16(pk(Dd[0].z, Dd[0].w), pk(Dd[1].z, Dd[1].w));
        u4 o = {s0[0], s1b[0], s0[1], s1b[1]};
        const int d = 16 * db + i16;
        const u32 so = L * SP_L + g * SP_G + p * SP_P;
        gst4(san(o), ws, d < 19 ? O_SP + (hd * 19 + d) * 64 + (q & 1) * 32 + (q >> 1) * 16 : 0x80000000u, so);
        gst2(u2{san(pk(Dd[2].x, Dd[2].y)), san(pk(Dd[2].z, Dd[2].w))}, ws, (d < 19 && q == 0) ? O_SP + 2432 + (hd * 19 + d) * 8 : 0x80000000u, so);
      }
      ladd(CNT(C_PUB), lane);
      STAMP(6 + L * 6);
      u4 wo[18];
      u32 fq[2][9];
      {
        if (wp < 10) wp = lwait(CNT(C_WPROG), 10, dead);
        if (L == 0) lwait(CNT(C_FAOK), 1, dead);
        f4 aq[3][2];
        UNR for (int i = 0; i < 3; ++i) aq[i][0] = aq[i][1] = f4{0, 0, 0, 0};
        u4 A[2][3];
        A[0][0] = ld<u4>(L_WQ + aF[0]); A[0][1] = ld<u4>(L_WQ + aF[0] + 2048); A[0][2] = ld<u4>(a7[0]);
        UNR for (int s = 0; s < 18; ++s) {
          if (s < 17) {
            const int j = (s + 1) >> 1, u = (s + 1) & 1;
            A[(s + 1) & 1][0] = ld<u4>(L_WQ + aF[u] + j * 4608); A[(s + 1) & 1][1] = ld<u4>(L_WQ + aF[u] + j * 4608 + 2048);
            A[(s + 1) & 1][2] = ld<u4>(a7[u] + j * 4608);
          }
          UNR for (int i = 0; i < 3; ++i) {
            aq[i][0] = mfma(A[s & 1][i], B[2 * s], aq[i][0]);
            aq[i][1] = mfma(A[s & 1][i], B[2 * s + 1], aq[i][1]);
          }
          if (s < 9) {
            wo[2 * s] = gld4(ws, O_WOT + lane * 16, (9 * c + w) * 18432 + 2 * s * 1024, 1);
            wo[2 * s + 1] = gld4(ws, O_WOT + lane * 16, (9 * c + w) * 18432 + (2 * s + 1) * 1024, 1);
          }
          __builtin_amdgcn_sched_barrier(0);
        }
        STAMP(7 + L * 6);
        const f4 c5 = ld<f4>(L_CSL + 5 * 64 + q * 16), c6 = ld<f4>(L_CSL + 6 * 64 + q * 16);
        const float c7 = ld<float>(L_CSL + 7 * 64 + q * 16);
        const bool val = (i16 >> 3) == (q >> 1);
        const u32 tq = L_TQ + (val ? (q * 4 + (i16 & 3)) * 16 : 256), tq2 = L_TQ2 + (val ? (q * 4 + (i16 & 3)) * 4 : 64);
        u4 q0[2], q1[2];
        UNR for (int tb = 0; tb < 2; ++tb) {
          q0[tb].x = pk((f2{aq[0][tb].x, aq[0][tb].y} - f2{c5.x, c5.y} * mS[tb]) * mQ[tb]);
          q0[tb].y = pk((f2{aq[0][tb].z, aq[0][tb].w} - f2{c5.z, c5.w} * mS[tb]) * mQ[tb]);
          q0[tb].z = pk((f2{aq[1][tb].x, aq[1][tb].y} - f2{c6.x, c6.y} * mS[tb]) * mQ[tb]);
          q0[tb].w = pk((f2{aq[1][tb].z, aq[1][tb].w} - f2{c6.z, c6.w} * mS[tb]) * mQ[tb]);
          q1[tb] = u4{pk((aq[2][tb].x - c7 * mS[tb]) * mQ[tb], 0.0f), 0, 0, 0};
        }
        f4 P[2][5];
        UNR for (int m = 0; m < 5; ++m) {
          u4 t0 = ld<u4>(tq + m * 272), t1 = {ld<u32>(tq2 + m * 68), 0, 0, 0};
          UNR for (int tb = 0; tb < 2; ++tb) P[tb][m] = mfma(t1, q1[tb], mfma(t0, q0[tb], f4{0, 0, 0, 0}));
        }
        const float ls = (q & 1) ? -LOG2E : LOG2E;
        UNR for (int tb = 0; tb < 2; ++tb) {
          f2 z[9];
          UNR for (int i = 0; i < 9; ++i) z[i] = f2{P[tb][i >> 1][2 * (i & 1)], P[tb][i >> 1][2 * (i & 1) + 1]} * ls;
          float mx = fmaxf(z[0].x, z[0].y);
          UNR for (int i = 1; i < 9; ++i) mx = fmaxf(fmaxf(mx, z[i].x), z[i].y);
          f2 s1 = {0, 0};
          UNR for (int i = 0; i < 9; ++i) { f2 t = z[i] - mx; z[i] = f2{__builtin_amdgcn_exp2f(t.x), __builtin_amdgcn_exp2f(t.y)}; s1 += z[i]; }
          const float r1 = __builtin_amdgcn_rcpf(s1.x + s1.y);
          UNR for (int i = 0; i < 9; ++i) fq[tb][i] = pk(z[i] * r1);
        }
      }
      STAMP(8 + L * 6);
      lwait(CNT(C_STB), 4 * (L + 1), dead);
      STAMP(9 + L * 6);
      {
        const bool val = (i16 >> 3) == (q >> 1);
        const int d01 = (q >> 1) * 19 + 8 * ((i16 >> 2) & 1) + (i16 & 3), d2 = (q >> 1) * 19 + 16 + ((i16 & 3) < 2 ? (i16 & 3) : 2);
        const u32 m01 = val ? L_STB + d01 * 64 + (q & 1) * 32 : L_ZERO, m2 = val ? L_STB + d2 * 64 + (q & 1) * 32 : L_ZERO;
        const u32 l01 = val ? L_STB + 2432 + d01 * 8 + (q & 1) * 4 : L_ZERO, l2 = val ? L_STB + 2432 + d2 * 8 + (q & 1) * 4 : L_ZERO;
        f4 O[2][3];
        UNR for (int m = 0; m < 3; ++m) {
          u4 t0 = ld<u4>(m < 2 ? m01 + m * 256 : m2), t1 = ld<u4>((m < 2 ? m01 + m * 256 : m2) + 16), t2 = {ld<u32>(m < 2 ? l01 + m * 32 : l2), 0, 0, 0};
          UNR for (int tb = 0; tb < 2; ++tb) {
            u4 b0 = {fq[tb][0], fq[tb][1], fq[tb][2], fq[tb][3]}, b1 = {fq[tb][4], fq[tb][5], fq[tb][6], fq[tb][7]}, b2 = {fq[tb][8], 0, 0, 0};
            O[tb][m] = mfma(t2, b2, mfma(t1, b1, mfma(t0, b0, f4{0, 0, 0, 0})));
          }
        }
        u32 lo[2];
        UNR for (int tb = 0; tb < 2; ++tb) {
          const float inv = __builtin_amdgcn_rcpf(O[tb][2].z + 1e-6f);
          u4 o = {pk(f2{O[tb][0].x, O[tb][0].y} * inv), pk(f2{O[tb][0].z, O[tb][0].w} * inv), pk(f2{O[tb][1].x, O[tb][1].y} * inv), pk(f2{O[tb][1].z, O[tb][1].w} * inv)};
          gst4(san(o), ws, O_OB + lane * 16, L * OB_L + p * OB_P + (2 * w + tb) * 16384 + g * 1024);
          lo[tb] = pk(f2{O[tb][2].x, O[tb][2].y} * inv);
        }
        const u32 lv = san((q & 1) ? lo[1] : lo[0]);
        auto hh = SW32(lv, lv);
        const u32 n0 = __builtin_amdgcn_update_dpp(0u, hh[0], 0xB1, 0xF, 0xF, true), n1 = __builtin_amdgcn_update_dpp(0u, hh[1], 0xB1, 0xF, 0xF, true);
        gst4(u4{hh[0], hh[1], n0, n1}, ws, (q < 2 && !(i16 & 1)) ? O_OB + 131072 + (16 * q + i16) * 8 : 0x80000000u, L * OB_L + p * OB_P + g * 1024 + w * 256);
        ladd(CNT(C_PUB), lane);
      }
      STAMP(22 + L);
      tail<false, L>(a, ws, b, w, lane, g, rt, c, rot, tag, wo, wo, dead STPASS);
    }
}

template <int L>
DEV void state_sum(const Args& a, rsrc_t ws, int b, int w, int lane0, int g, int& dead STARG) {
    const int lane = opq(lane0), q = lane >> 4, i16 = lane & 15;
    u4 sel;
    UNR for (int d = 0; d < 4; ++d) sel[d] = ((i16 & 7) >> 1) == d ? ((i16 & 1) ? 0x3F800000u : 0x00003F80u) : 0u;
      {
        const int h = w - 4;
        u4 v[3][4];
        u32 off[3];
        UNR for (int gi = 0; gi < 3; ++gi) off[gi] = O_SP + q * SP_P + umin(16 * umin(h + 4 * gi, 10) + i16, 170) * 16;
        lwait(CNT(C_PUB), 8 * L + 4, dead);
        NOUNR for (int it = dead ? SPIN_G - 1 : 0;; ++it) {
          FENCE();
          u32 m = 0;
          UNR for (int gi = 0; gi < 3; ++gi) UNR for (int pp = 0; pp < 4; ++pp) v[gi][pp] = gld4(ws, off[gi], L * SP_L + g * SP_G + 4 * pp * SP_P, 1);
          UNR for (int gi = 0; gi < 3; ++gi) UNR for (int pp = 0; pp < 4; ++pp) m = vm(m, v[gi][pp]);
          if (!anyl(m == SENT)) break;
          if (it >= SPIN_G) { dead = 1; break; }
        }
        FENCE();
        STAMP(6 + L * 6);
        UNR for (int gi = 0; gi < 3; ++gi) {
          f4 s = {0, 0, 0, 0};
          UNR for (int pp = 0; pp < 4; ++pp) s = mfma(sel, v[gi][pp], s);
          int ck = 16 * umin(h + 4 * gi, 10) + i16;
          if (q < 2 && ck < 171) st<u2>(L_STB + ck * 16 + q * 8, u2{pk(s.x, s.y), pk(s.z, s.w)});
        }
        FENCE();
        ladd(CNT(C_STB), lane);
        STAMP(7 + L * 6);
      }
}

__global__ void __launch_bounds__(512, 2) fused_kernel(Args a) {
  const int tid = threadIdx.x, lane = tid & 63, w = rfl(tid >> 6), b = blockIdx.x;
  const int q = lane >> 4, i16 = lane & 15;
  const int xcd = b & 7, r = b >> 3;
  const int p = 4 * (xcd >> 1) + (r >> 3), g = 8 * (xcd & 1) + (r & 7), rot = ((r & 7) + 2 * (r >> 3)) % 9;
  const int rt = 4 * p + (g >> 2), c = g & 3;
  rsrc_t ws = mkrs(a.ws, WS_BYTES);
  u32 tag = *(const u32*)a.ws + 1;
  int dead = 0;
  STAMP(0); STAMPC(29);

  if (w < 4) {
    __builtin_amdgcn_s_setprio(2);
    u4 B[36];
    float mS[2], mQ[2];
    {
      rsrc_t rx = mkrs(a.x, 2359296);
      u32 vo[2];
      UNR for (int par = 0; par < 2; ++par) vo[par] = (8 * par + (lane >> 3)) * 1152 + (((lane & 7) ^ (4 * par + (lane >> 4))) << 4);
      UNR for (int j = 0; j < 9; ++j) {
        int kr = j + rot; kr -= kr >= 9 ? 9 : 0;
        UNR for (int rg = 0; rg < 4; ++rg) dma<0>(rx, j * 16384 + w * 4096 + rg * 1024, vo[rg & 1], (128 * p + 32 * w + 16 * (rg >> 1)) * 1152 + kr * 128);
      }
      FENCE();
      __builtin_amdgcn_sched_barrier(0);
      asm volatile("s_waitcnt lgkmcnt(0)\n\ts_barrier" : "+s"(tag)::"memory");
      __builtin_amdgcn_sched_barrier(0);
      STAMP(1);
      u32 a0 = w * 4096 + (i16 >> 3) * 1024 + (i16 & 7) * 128 + ((q ^ (i16 >> 1)) << 4), a1 = a0 ^ 64, pa = CNT(C_PROG) + 4 * w;
      f4 gS[2] = {f4{0, 0, 0, 0}, f4{0, 0, 0, 0}}, gQ[2] = {f4{0, 0, 0, 0}, f4{0, 0, 0, 0}};
      const u4 ones = {0x3F803F80u, 0x3F803F80u, 0x3F803F80u, 0x3F803F80u};
#define X0STEP(j)                                                                                                                      \
  {                                                                                                                                    \
    u32 pv = j + 1;                                                                                                                    \
    asm volatile("s_waitcnt vmcnt(%8)\n\tds_read_b128 %0, %4\n\tds_read_b128 %1, %4 offset:2048\n\tds_read_b128 %2, %5\n\t"            \
                 "ds_read_b128 %3, %5 offset:2048\n\ts_waitcnt lgkmcnt(0)\n\tds_write_b32 %6, %7"                                       \
                 : "=&v"(B[4 * j]), "=&v"(B[4 * j + 1]), "=&v"(B[4 * j + 2]), "=&v"(B[4 * j + 3])                                        \
                 : "v"(a0 + j * 16384), "v"(a1 + j * 16384), "v"(pa), "v"(pv), "n"(32 - 4 * j)                                          \
                 : "memory");                                                                                                          \
    UNR for (int i = 0; i < 4; ++i) { gS[i & 1] = mfma(ones, B[4 * j + i], gS[i & 1]); gQ[i & 1] = mfma(B[4 * j + i], B[4 * j + i], gQ[i & 1]); } \
  }
      X0STEP(0) X0STEP(1) X0STEP(2) X0STEP(3) X0STEP(4) X0STEP(5) X0STEP(6) X0STEP(7) X0STEP(8)
      STAMP(2);
      UNR for (int tb = 0; tb < 2; ++tb) {
        float d = (i16 & 2) ? ((i16 & 1) ? gQ[tb].w : gQ[tb].z) : ((i16 & 1) ? gQ[tb].y : gQ[tb].x);
        d = (q == (i16 >> 2)) ? d : 0.0f;
        d = sw16add(d, d); d = sw32add(d, d);
        f2 t = ln_stats(gS[tb].x, d);
        mS[tb] = t.x; mQ[tb] = t.y;
      }
    }
    u32 wp = 0;
    main_layer<0>(a, ws, b, w, lane, p, g, rt, c, rot, tag, B, mS, mQ, wp, dead STPASS);
    main_layer<1>(a, ws, b, w, lane, p, g, rt, c, rot, tag, B, mS, mQ, wp, dead STPASS);
  } else {
    const int pr = (w - 4) >> 1, e = w & 1;
    const u32 lH = pr ? L_H1 : L_H0, lU = pr ? L_U1 : L_U0;
    if (w == 4) {
      const int cl = b % 27, kr0 = b / 27;
      if (b < 243) {
        rsrc_t rw = mkrs(a.wqkv, 576 * 3456);
        UNR for (int rg = 0; rg < 8; ++rg)
          dma<0>(rw, L_TILE + rg * 1024, ((lane >> 3) ^ (rg & 1)) * 3456 + (((lane & 7) ^ (4 * ((rg >> 1) & 1))) << 4), (64 * kr0 + 8 * rg) * 3456 + cl * 128);
      }
      FENCE();
      asm volatile("s_waitcnt lgkmcnt(0)\n\ts_barrier" : "+s"(tag)::"memory");
      if (b < 243) {
        const int cg = lane >> 3, rg = lane & 7, sec = cl / 9;
        u32 dst[8];
        UNR for (int j = 0; j < 8; ++j) {
          int cc = 64 * (cl - 9 * sec) + 8 * cg + j, h = cc / 18, d = cc - 18 * h, hl = h & 1;
          u32 o;
          if (sec == 0) {
            int hi = d >= 9, qq = 2 * hl + hi, s = d - 9 * hi;
            int row = s < 8 ? 16 * (s >> 2) + 4 * qq + (s & 3) : 32 + qq, key = s < 8 ? (row >> 1) & 7 : 0;
            o = L_WQ + kr0 * 4608 + row * 128 + ((rg ^ key) << 4);
          } else {
            int qq = 2 * (sec - 1) + hl;
            int row = d < 16 ? 16 * (d >> 2) + 4 * qq + (d & 3) : 48 + 2 * qq + d, key = d < 16 ? (row >> 1) & 7 : 2 * qq;
            o = kr0 * 9216 + row * 128 + ((rg ^ key) << 4);
          }
          dst[j] = O_WT + (h >> 1) * WT_G + o;
        }
        vmwait0();
        u4 R[8];
        const u32 rb = L_TILE + rg * 1024 + ((cg ^ (4 * ((rg >> 1) & 1))) << 4);
        UNR for (int i = 0; i < 8; ++i) R[i] = ld<u4>(rb + ((i ^ (rg & 1)) << 7));
        float cs[8];
        UNR for (int j = 0; j < 8; ++j) {
          u4 o;
          UNR for (int d = 0; d < 4; ++d) o[d] = __builtin_amdgcn_perm(R[2 * d + 1][j >> 1], R[2 * d][j >> 1], (j & 1) ? 0x07060302u : 0x05040100u);
          gst4(o, ws, dst[j], 0);
          cs[j] = dot2(o.x, 0x3F803F80u, dot2(o.y, 0x3F803F80u, 0.0f)) + dot2(o.z, 0x3F803F80u, dot2(o.w, 0x3F803F80u, 0.0f));
        }
        float sel = 0;
        UNR for (int j = 0; j < 8; ++j) {
          float v = cs[j];
          v += __uint_as_float(__builtin_amdgcn_update_dpp(0, __float_as_uint(v), 0xB1, 0xF, 0xF, true));
          v += __uint_as_float(__builtin_amdgcn_update_dpp(0, __float_as_uint(v), 0x4E, 0xF, 0xF, true));
          v += __uint_as_float(__builtin_amdgcn_update_dpp(0, __float_as_uint(v), 0x141, 0xF, 0xF, true));
          sel = rg == j ? v : sel;
        }
        gst1(__float_as_uint(sel), ws, O_CS + lane * 4, kr0 * 6912 + cl * 256);
        vmwait0();
        setflag(ws, O_FW + b * 128, tag, lane);
      }
      STAMP(1);
      {
        int l6 = lane < 54 ? lane : 0, kr = l6 / 6, i = l6 - 6 * kr;
        int ln = 9 * (i >> 1) + ((36 * g + 35 * (i & 1)) >> 6);
        poll(ws, O_FW + (kr * 27 + ln) * 128, tag, dead);
      }
      lset(CNT(C_WOK), 1);
      STAMP(2);
      u32 xp = 0;
      auto need_x = [&](u32 n) {
        if (xp < n) {
          NOUNR for (int i = dead ? SPIN_L : 0; i < SPIN_L; ++i) {
            u4 v;
            asm volatile("ds_read_b128 %0, %1\n\ts_waitcnt lgkmcnt(0)" : "=v"(v) : "v"(CNT(C_PROG)) : "memory");
            xp = rfl(umin(umin(v.x, v.y), umin(v.z, v.w)));
            if (xp >= n) return;
          }
          dead = 1;
        }
      };
      UNR for (int j = 0; j < 9; ++j) {
        need_x(((j + 1) * 9216 - 1) / 16384 + 1);
        int kr = j + rot; kr -= kr >= 9 ? 9 : 0;
        UNR for (int i = 0; i < 9; ++i) dma<SC1>(ws, j * 9216 + i * 1024, O_WT + lane * 16, g * WT_G + kr * 9216 + i * 1024);
        if (j >= 3) { vmwait<27>(); lset(CNT(C_WPROG), j - 2); }
      }
      need_x(8);
      UNR for (int i = 0; i < 41; ++i) {
        u32 o = i * 1024 + lane * 16 + rot * 4608;
        o -= o >= 41472 ? 41472 : 0;
        if (i < 40 || lane < 32) dma<SC1>(ws, L_WQ + i * 1024, O_WT + L_WQ + o, g * WT_G);
        if (i == 8) { vmwait<27>(); lset(CNT(C_WPROG), 7); }
        if (i == 17) { vmwait<27>(); lset(CNT(C_WPROG), 8); }
        if (i == 26) { vmwait<27>(); lset(CNT(C_WPROG), 9); }
      }
      vmwait0();
      lset(CNT(C_WPROG), 10);
      STAMP(3);
    } else if (w < 7) {
      KVin in0, in1;
      Hin hin;
      const int T = (w - 5) * 128 + lane, chn = 2 * b + (w - 5);
      const u32 mH = w == 5 ? L_H0 : L_H1, mU = w == 5 ? L_U0 : L_U1;
      kv_load(a, T, rt, c, in0);
      kv_load(a, T + 64, rt, c, in1);
      h_load(a, chn, lane, hin);
      FENCE();
      __builtin_amdgcn_sched_barrier(0);
      asm volatile("s_waitcnt lgkmcnt(0)\n\ts_barrier" : "+s"(tag)::"memory");
      __builtin_amdgcn_sched_barrier(0);
      kv_compute(ws, T, rt, c, in0);
      kv_compute(ws, T + 64, rt, c, in1);
      STAMP(1);
      vmwait0();
      if (ladd_rtn(CNT(C_KVC), lane) == 2) setflag(ws, O_FK + b * 128, tag, lane);
      UNR for (int i = 0; i < 4; ++i) lwait(CNT(C_PROG) + 4 * i, 9, dead);
      lwait(CNT(C_WOK), 1, dead);
      h_stage(mH, mU, lane, hin);
      poll(ws, O_FK + wg_of(lane >> 2, 4 * (lane & 3) + (2 * b) / 144) * 128, tag, dead);
      STAMP(2);
      u_stage(ws, mU, chn, lane);
      vmwait0();
      ladd(w == 5 ? CNT(C_PU0) : CNT(C_PU1), lane);
      STAMP(3);
    } else {
      if (lane < 32) lset(L_CNT + lane * 4, 0);
      const int cl = b % 9, gg = b / 9;
      if (b < 144) {
        rsrc_t rw = mkrs(a.wo, 576 * 1152);
        UNR for (int m = 0; m < 5; ++m)
          if (m < 4 || lane < 32) dma<0>(rw, L_WOTILE + m * 1024, (lane >> 3) * 1152 + (lane & 7) * 16, (36 * gg + 8 * m) * 1152 + cl * 128);
      }
      rsrc_t rp = mkrs(a.wphi, 32 * 648);
      u4 p0 = gld4(rp, lane * 16, g * 1296, 0), p1 = gld4(rp, lane < 17 ? 1024 + lane * 16 : 0x80000000u, g * 1296, 0);
      KVin in;
      if (lane < 32) kv_load(a, 256 + lane, rt, c, in);
      FENCE();
      __builtin_amdgcn_sched_barrier(0);
      asm volatile("s_waitcnt lgkmcnt(0)\n\ts_barrier" : "+s"(tag)::"memory");
      __builtin_amdgcn_sched_barrier(0);
      const u4 s4 = {SENT, SENT, SENT, SENT};
      UNR for (int i = 0; i < 3; ++i) gst4(s4, ws, (i < 2 || lane < 48) ? O_SP + i * 1024 + lane * 16 : 0x80000000u, g * SP_G + p * SP_P);
      UNR for (int i = 0; i < 9; ++i) gst4(s4, ws, O_OB + lane * 16, p * OB_P + i * 16384 + g * 1024);
      gst4(s4, ws, lane < 48 ? O_ST + q * 65536 + i16 * 16 : 0x80000000u, rt * 1024 + c * 256);
      if (lane < 32) kv_compute(ws, 256 + lane, rt, c, in);
      if (b < 144) {
        const u32 ta = L_WOTILE + (18 * (q >> 1) + 8 * (q & 1) + (i16 >> 2)) * 128 + (i16 & 3) * 8;
        UNR for (int G = 0; G < 4; ++G) gst4(trd2(ta + G * 32, ta + G * 32 + 512), ws, O_WOT + lane * 16, (4 * cl + G) * 18432 + gg * 1024);
        u2 lf = trd(L_WOTILE + (16 + ((i16 >> 2) & 1) + 18 * (i16 >> 3)) * 128 + q * 32 + (i16 & 3) * 8);
        gst2(lf, ws, O_WOT + q * 18432 + i16 * 16, 4 * cl * 18432 + (16 + (gg >> 3)) * 1024 + ((gg >> 1) & 3) * 256 + (gg & 1) * 8);
      }
      FENCE();
      asm volatile("s_waitcnt lgkmcnt(0)" ::: "memory");
      st<u4>(L_WOTILE + lane * 16, p0);
      if (lane < 17) st<u4>(L_WOTILE + 1024 + lane * 16, p1);
      FENCE();
      auto wp16 = [&](int hd, int d, int f) -> u32 { u32 v = ld<u16>(L_WOTILE + (hd * 324 + d * 18 + (f < 18 ? f : 0)) * 2); return f < 18 ? v : 0u; };
      UNR for (int rd = 0; rd < 2; ++rd) {
        int t = rd * 64 + lane, tt = t < 80 ? t : 79, m = tt >> 4, cc = (tt >> 3) & 1, hd = (tt >> 2) & 1, f = 4 * m + (tt & 3);
        u4 v;
        UNR for (int d = 0; d < 4; ++d) v[d] = wp16(hd, 8 * cc + 2 * d, f) | (wp16(hd, 8 * cc + 2 * d + 1, f) << 16);
        st<u4>(L_TK + (tt >> 3) * 144 + (tt & 7) * 16, v);
        int hq = (tt >> 3) & 1, dh = (tt >> 2) & 1;
        UNR for (int d = 0; d < 4; ++d) v[d] = wp16(hq, 9 * dh + 2 * d, f) | (wp16(hq, 9 * dh + 2 * d + 1, f) << 16);
        st<u4>(L_TQ + m * 272 + (tt & 15) * 16, v);
        st<u32>(L_TQ2 + m * 68 + (tt & 15) * 4, wp16(hq, 9 * dh + 8, f));
      }
      {
        int tt = lane < 40 ? lane : 39, m = tt >> 3, hd = (tt >> 2) & 1, f = 4 * m + (tt & 3);
        st<u32>(L_TK2 + m * 36 + (tt & 7) * 4, wp16(hd, 16, f) | (wp16(hd, 17, f) << 16));
        u4 z = {0, 0, 0, 0};
        if (lane < 10) st<u4>(L_TK + lane * 144 + 128, z);
        if (lane < 5) { st<u4>(L_TQ + lane * 272 + 256, z); st<u32>(L_TK2 + lane * 36 + 32, 0u); st<u32>(L_TQ2 + lane * 68 + 64, 0u); }
      }
      FENCE();
      vmwait0();
      setflag(ws, O_FA + b * 128, tag, lane);
      if (ladd_rtn(CNT(C_KVC), lane) == 2) setflag(ws, O_FK + b * 128, tag, lane);
      STAMP(1);
      lwait(CNT(C_WOK), 1, dead);
      UNR for (int rd = 0; rd < 2; ++rd) {
        int blk = rd * 4 + q, qq = i16 >> 2, ee = i16 & 3;
        int col;
        if (rd == 0) col = 576 + 576 * (qq >> 1) + 36 * g + 18 * (qq & 1) + 4 * blk + ee;
        else col = blk == 4 ? 576 + 576 * (qq >> 1) + 36 * g + 18 * (qq & 1) + 16 + (ee & 1) : 36 * g + 9 * qq + (blk == 7 ? 8 : 4 * (blk - 5) + ee);
        float s = 0;
        UNR for (int k = 0; k < 9; ++k) s += __uint_as_float(gld1(ws, O_CS + col * 4, k * 6912, 1));
        st<float>(L_CSL + rd * 256 + lane * 4, s);
      }
      if (lane < 18) st<u4>(L_ZERO + lane * 16, u4{0, 0, 0, 0});
      FENCE();
      ladd(CNT(C_TBL), lane);
      STAMP(2);
      {
        poll(ws, O_FA + wg_of(lane < 16 ? lane : p, lane < 16 ? g : (lane & 15)) * 128, tag, dead);
        int ln = (144 * c + 48 * q + (q == 3 ? -1 : 0)) >> 6;
        poll(ws, O_FA + (i16 * 9 + ln) * 128, tag, dead);
      }
      lset(CNT(C_FAOK), 1);
      lwait(CNT(C_WPROG), 10, dead);
      UNR for (int i = 0; i < 9; ++i) {
        u32 o = i * 1024 + lane * 16;
        o += o >= 4608 ? 18432 - 4608 : 0;
        gst4(s4, ws, O_X2 + o, 2 * rt * 18432 + c * 4608);
      }
      UNR for (int i = 0; i < 3; ++i) gst4(s4, ws, (i < 2 || lane < 48) ? O_SP + i * 1024 + lane * 16 : 0x80000000u, SP_L + g * SP_G + p * SP_P);
      UNR for (int i = 0; i < 9; ++i) gst4(s4, ws, O_OB + lane * 16, OB_L + p * OB_P + i * 16384 + g * 1024);
      STAMP(3);
    }
    u4 wo[18], wo8[18];
    lwait(CNT(C_WPROG), 10, dead);
    lwait(CNT(C_FAOK), 1, dead);
    UNR for (int i = 0; i < 18; ++i) wo[i] = gld4(ws, O_WOT + lane * 16, (9 * c + w) * 18432 + i * 1024, 1);
    lwait(pr ? CNT(C_PU1) : CNT(C_PU0), 1, dead);
    STAMP(4);
    conv_pair(a, ws, lH, lU, 2 * b + pr, e, pr ? CNT(C_PA1) : CNT(C_PA0), pr ? CNT(C_PB1) : CNT(C_PB0), 2, lane, dead);
    STAMP(5);
    vmwait0();
    if (w == 7) setflag(ws, O_FB + b * 128, tag, lane);
    if (ladd_rtn(CNT(C_FYC), lane) == 3) setflag(ws, O_FY + b * 128, tag, lane);
    state_sum<0>(a, ws, b, w, lane, g, dead STPASS);
      {
        if (c == 0 && w < 6) {
          const int ch = 512 + rt;
          lwait(CNT(C_FYC), 4, dead);
          if (w == 5) {
            Hin hin;
            h_load(a, ch, lane, hin);
            poll(ws, O_FK + wg_of(lane >> 2, 4 * (lane & 3) + 3) * 128, tag, dead);
            u_stage(ws, L_U0, ch, lane);
            h_stage(L_H0, L_U0, lane, hin);
            vmwait0();
            ladd(CNT(C_PU0), lane);
          }
          lwait(CNT(C_PU0), 2, dead);
          conv_pair(a, ws, L_H0, L_U0, ch, e, CNT(C_PA0), CNT(C_PB0), 4, lane, dead);
          vmwait0();
          if (ladd_rtn(CNT(C_FZC), lane) == 1) setflag(ws, O_FZ + (ch - 512) * 128, tag, lane);
        }
        if (w < 6) UNR for (int i = 0; i < 18; ++i) wo8[i] = gld4(ws, O_WOT + lane * 16, (9 * c + 8) * 18432 + i * 1024, 1);
        if (w == 7) poll(ws, O_FB + wg_of(lane < 16 ? lane : p, lane < 16 ? g : (lane & 15)) * 128, tag, dead);
        STAMP(8);
      }
    tail<true, 0>(a, ws, b, w, lane, g, rt, c, rot, tag, wo, wo8, dead STPASS);
    state_sum<1>(a, ws, b, w, lane, g, dead STPASS);
    tail<true, 1>(a, ws, b, w, lane, g, rt, c, rot, tag, wo, wo8, dead STPASS);
    if (b == 0 && w == 7) {
      UNR for (int i = 0; i < 4; ++i) poll(ws, O_FA + (i * 64 + lane) * 128, tag, dead);
      setflag(ws, 0, tag, lane);
    }
  }
  STAMP(31); STAMPC(30);
}

__global__ void k_lead(unsigned* p) { unsigned t = (unsigned)__builtin_amdgcn_s_memrealtime(); if (threadIdx.x == 0) p[O_STAMP / 4 + 1024] = t; }
__global__ void k_trail(unsigned* p) { unsigned t0 = p[O_STAMP / 4 + 1024]; for (int z = 0; z < 100000; ++z) { if ((unsigned)__builtin_amdgcn_s_memrealtime() - t0 >= 8000u) break; __builtin_amdgcn_s_sleep(2); } if (threadIdx.x == 0) p[O_STAMP / 4 + 2048] = t0; }
extern "C" void kernel_launch(void* const* d_in, const int* in_sizes, int n_in, void* d_out, int out_size, void* d_ws, size_t ws_size, hipStream_t stream) {
  Args a;
  a.x = (const u16*)d_in[0]; a.wqkv = (const u16*)d_in[1]; a.wphi = (const u16*)d_in[2]; a.wo = (const u16*)d_in[3];
  a.bk = (const u16*)d_in[5]; a.bv = (const u16*)d_in[6]; a.sk = (const u16*)d_in[7]; a.ssm = (const u16*)d_in[8]; a.dv = (const u16*)d_in[9];
  a.out = (u16*)d_out; a.ws = (unsigned char*)d_ws;
  (void)hipFuncSetAttribute((const void*)fused_kernel, hipFuncAttributeMaxDynamicSharedMemorySize, LDS_BYTES);
  k_lead<<<1, 64, 0, stream>>>((unsigned*)d_ws);
  fused_kernel<<<256, 512, LDS_BYTES, stream>>>(a);
  k_trail<<<1, 64, 0, stream>>>((unsigned*)d_ws);
}
```

```cpp
#include <hip/hip_runtime.h>
#include <hip/hip_bf16.h>

typedef unsigned u32;
typedef unsigned short u16;
typedef float f2 __attribute__((ext_vector_type(2)));
typedef float f4 __attribute__((ext_vector_type(4)));
typedef u32 u2 __attribute__((ext_vector_type(2)));
typedef u32 u4 __attribute__((ext_vector_type(4)));
typedef u32 u2a __attribute__((ext_vector_type(2), aligned(4)));
typedef __bf16 bf2 __attribute__((ext_vector_type(2)));
typedef __bf16 bf8 __attribute__((ext_vector_type(8)));
typedef short s4v __attribute__((ext_vector_type(4)));
typedef __amdgpu_buffer_rsrc_t rsrc_t;
#define LAS __attribute__((address_space(3)))
#define DEV __device__ __forceinline__
#define UNR _Pragma("unroll")
#define NOUNR _Pragma("unroll 1")
#define FENCE() asm volatile("" ::: "memory")
#define SC1 16

#define WV3 1
constexpr u32 O_FW = 4096, O_FA = O_FW + 32768, O_FK = O_FA + 32768, O_FY = O_FK + 32768;
constexpr u32 O_WT = 262144, WT_G = 124416;
constexpr u32 O_CS = O_WT + 16 * WT_G;
constexpr u32 O_WOT = O_CS + 62464;
constexpr u32 O_KVT = O_WOT + 663552;
constexpr u32 O_YT = O_KVT + 2359296;
constexpr u32 O_SP = O_YT + 2359296, SP_L = 720896, SP_G = 45056, SP_P = 2816;
constexpr u32 O_OB = O_SP + 2 * SP_L, OB_L = 2359296, OB_P = 147456;
constexpr u32 O_ST = O_OB + 2 * OB_L;
constexpr u32 O_X2 = O_ST + 3 * 65536;
constexpr u32 O_STAMP = O_X2 + 2359296;
constexpr u32 WS_BYTES = O_STAMP + 256 * 8 * 32 * 8;
constexpr u32 SLOT = 9728, L_WQ = 9 * SLOT, L_STG = 5 * SLOT, L_STB = L_STG + 30720;
constexpr u32 L_H0 = 124416, L_H1 = 132736, L_U0 = 141056, L_U1 = 145632, L_H2 = 150528, L_U2 = 154688, L_ZERO = L_U2 + 4096, L_CSL = 159264;
constexpr u32 L_KT = 131072, L_VT = 141152, L_TILE = 150528, L_PT = 158720;
constexpr u32 L_REDA = 124416, L_REDB = 126464, L_Y = 128512, L_KEEP = 139264;
constexpr u32 L_TK = 160256, L_TK2 = 161696, L_TQ = 161888, L_TQ2 = 163248, L_CNT = 163712, LDS_BYTES = 163840;
enum { C_PROG = 0, C_WPROG = 4, C_WOK, C_TBL, C_KVC, C_PHI, C_STB, C_PU0, C_PU1, C_PU2, C_PA0, C_PA1, C_PB0, C_PB1, C_QA, C_QB, C_REDA, C_REDB, C_FYC, C_FAOK, C_GEM, C_PUB, C_KVL, C_KVR, C_WT, C_CLR, C_G2 };
#define CNT(i) (L_CNT + 4 * (i))
constexpr u32 SENT = 0xFFFFFFFFu;
constexpr int SPIN_G = 1 << 17, SPIN_L = 1 << 21;

#define MAP 1
__device__ __forceinline__ int wg_of(int p, int g) {
  return (p >> 2) * 2 + (g >> 3) + 8 * ((p & 3) * 8 + (g & 7));
}
struct Args {
  const u16 *x, *wqkv, *wphi, *wo, *bk, *bv, *sk, *ssm, *dv;
  u16* out;
  unsigned char* ws;
};

DEV int opq(int v) { asm volatile("" : "+v"(v)); return v; }
DEV u32 rfl(u32 v) { return __builtin_amdgcn_readfirstlane(v); }
DEV rsrc_t mkrs(const void* p, u32 n) { return __builtin_amdgcn_make_buffer_rsrc((void*)p, 0, n, 0x00020000); }
DEV u32 pk(float a, float b) { f2 v = {a, b}; return __builtin_bit_cast(u32, __builtin_convertvector(v, bf2)); }
DEV u32 pk(f2 v) { return __builtin_bit_cast(u32, __builtin_convertvector(v, bf2)); }
DEV f2 up(u32 p) { f2 r = {__uint_as_float(p << 16), __uint_as_float(p & 0xffff0000u)}; return r; }
DEV f2 rnd(f2 v) { return up(pk(v)); }
DEV float rnd1(float v) { return __uint_as_float(pk(v, v) & 0xffff0000u); }
DEV f4 mfma(u4 a, u4 b, f4 c) { return __builtin_amdgcn_mfma_f32_16x16x32_bf16(__builtin_bit_cast(bf8, a), __builtin_bit_cast(bf8, b), c, 0, 0, 0); }
DEV u32 umin(u32 a, u32 b) { return a < b ? a : b; }
DEV u32 umax(u32 a, u32 b) { return a > b ? a : b; }
DEV u32 umax3(u32 a, u32 b, u32 c) { return umax(umax(a, b), c); }
DEV u32 san(u32 v) { return umin(v, 0xFFFFFFFEu); }
DEV u4 san(u4 v) { u4 r = {san(v.x), san(v.y), san(v.z), san(v.w)}; return r; }
DEV u32 vm(u32 m, u4 v) { return umax3(umax3(m, v.x, v.y), v.z, v.w); }
DEV bool anyl(bool c) { return __builtin_amdgcn_ballot_w64(c) != 0; }
DEV float dot2(u32 a, u32 b, float c) { return __builtin_amdgcn_fdot2_f32_bf16(__builtin_bit_cast(bf2, a), __builtin_bit_cast(bf2, b), c, false); }
template <class T> DEV T ld(u32 o) { return *(LAS T*)(uintptr_t)o; }
template <class T> DEV void st(u32 o, T v) { *(LAS T*)(uintptr_t)o = v; }
DEV u2 trd(u32 o) { return __builtin_bit_cast(u2, __builtin_amdgcn_ds_read_tr16_b64_v4i16((LAS s4v*)(uintptr_t)o)); }
DEV u4 trd2(u32 o0, u32 o1) { u2 a = trd(o0), b = trd(o1); u4 r = {a.x, a.y, b.x, b.y}; return r; }
DEV u4 gld4(rsrc_t r, u32 v, u32 s, int aux) { return aux ? __builtin_amdgcn_raw_buffer_load_b128(r, v, s, SC1) : __builtin_amdgcn_raw_buffer_load_b128(r, v, s, 0); }
DEV u2 gld2(rsrc_t r, u32 v, u32 s, int aux) { return aux ? __builtin_amdgcn_raw_buffer_load_b64(r, v, s, SC1) : __builtin_amdgcn_raw_buffer_load_b64(r, v, s, 0); }
DEV u32 gld1(rsrc_t r, u32 v, u32 s, int aux) { return aux ? __builtin_amdgcn_raw_buffer_load_b32(r, v, s, SC1) : __builtin_amdgcn_raw_buffer_load_b32(r, v, s, 0); }
DEV void gst4(u4 d, rsrc_t r, u32 v, u32 s) { __builtin_amdgcn_raw_buffer_store_b128(d, r, v + s, 0, SC1); }
DEV void gst2(u2 d, rsrc_t r, u32 v, u32 s) { __builtin_amdgcn_raw_buffer_store_b64(d, r, v, s, SC1); }
DEV void gst1(u32 d, rsrc_t r, u32 v, u32 s) { __builtin_amdgcn_raw_buffer_store_b32(d, r, v, s, SC1); }
template <int AUX> DEV void dma(rsrc_t r, u32 l, u32 v, u32 s) {
#if defined(__HIP_DEVICE_COMPILE__)
  __builtin_amdgcn_raw_ptr_buffer_load_lds(r, (LAS void*)(uintptr_t)l, 16, v, s, 0, AUX);
#endif
}
DEV void vmwait0() { asm volatile("s_waitcnt vmcnt(0)" ::: "memory"); }
template <int N> DEV void vmwait() { asm volatile("s_waitcnt vmcnt(%0)" ::"n"(N) : "memory"); }
DEV void bar() { asm volatile("s_waitcnt lgkmcnt(0)\n\ts_barrier" ::: "memory"); }
DEV void lset(u32 a, u32 v) { asm volatile("ds_write_b32 %0, %1" ::"v"(a), "v"(v) : "memory"); }
DEV void ladd(u32 a, int lane) { if (lane == 0) { u32 one = 1; asm volatile("ds_add_u32 %0, %1" ::"v"(a), "v"(one) : "memory"); } }
DEV u32 ladd_rtn(u32 a, int lane) {
  u32 r = 0;
  if (lane == 0) { u32 one = 1; asm volatile("ds_add_rtn_u32 %0, %1, %2\n\ts_waitcnt lgkmcnt(0)" : "=v"(r) : "v"(a), "v"(one) : "memory"); }
  return rfl(r);
}
DEV u32 lget(u32 a) { u32 r; asm volatile("ds_read_b32 %0, %1\n\ts_waitcnt lgkmcnt(0)" : "=v"(r) : "v"(a) : "memory"); return rfl(r); }
DEV u32 lwait(u32 a, u32 t, int& dead) {
  u32 r = lget(a);
  if (__builtin_expect(r < t, 0)) {
    NOUNR for (int i = dead ? SPIN_L : 0; i < SPIN_L; ++i) { r = lget(a); if (r >= t) return r; __builtin_amdgcn_s_sleep(1); }
    dead = 1;
  }
  return r;
}
DEV void poll(rsrc_t ws, u32 off, u32 tag, int& dead) {
  NOUNR for (int i = dead ? SPIN_G : 0; i < SPIN_G; ++i) {
    FENCE();
    u32 v = gld1(ws, off, 0, 1);
    if (!anyl(v != tag)) { FENCE(); return; }
    __builtin_amdgcn_s_sleep(1);
  }
  dead = 1;
}
DEV void setflag(rsrc_t ws, u32 off, u32 tag, int lane) { if (lane == 0) gst1(tag, ws, off, 0); }
#define SW16(a, b) __builtin_amdgcn_permlane16_swap(a, b, false, false)
#define SW32(a, b) __builtin_amdgcn_permlane32_swap(a, b, false, false)
DEV float sw16add(float a, float b) { auto r = SW16(__float_as_uint(a), __float_as_uint(b)); return __uint_as_float(r[0]) + __uint_as_float(r[1]); }
DEV float sw32add(float a, float b) { auto r = SW32(__float_as_uint(a), __float_as_uint(b)); return __uint_as_float(r[0]) + __uint_as_float(r[1]); }

#define STAMP(i)
#define STAMPC(i)
#define STARG
#define STPASS

DEV f2 ln_stats(float s, float sq) {
  float mean = s * (1.0f / 576.0f);
  float var = fmaxf(sq * (1.0f / 576.0f) - mean * mean, 0.0f);
  float sd = rnd1(__builtin_amdgcn_sqrtf(rnd1(rnd1(var) + 1e-5f)));
  f2 r = {rnd1(mean), __builtin_amdgcn_rcpf(sd)};
  return r;
}
DEV void stats_from(u4 v, float (&m)[2], float (&rs)[2]) {
  float s = __uint_as_float(v.x) + __uint_as_float(v.z), sq = __uint_as_float(v.y) + __uint_as_float(v.w);
  s = sw16add(s, s); sq = sw16add(sq, sq);
  f2 t = ln_stats(s, sq);
  auto r0 = SW32(__float_as_uint(t.x), __float_as_uint(t.x));
  auto r1 = SW32(__float_as_uint(t.y), __float_as_uint(t.y));
  m[0] = __uint_as_float(r0[0]); m[1] = __uint_as_float(r0[1]); rs[0] = __uint_as_float(r1[0]); rs[1] = __uint_as_float(r1[1]);
}
DEV u4 ld_stats(rsrc_t ws, u32 off) { u2 a = gld2(ws, off, 0, 1), b = gld2(ws, off + 256, 0, 1); return u4{a.x, a.y, b.x, b.y}; }
DEV u4 poll_stats(rsrc_t ws, u32 off, u4 v, int& dead) {
  if (anyl(vm(0, v) == SENT)) {
    NOUNR for (int i = dead ? SPIN_G : 0; i < SPIN_G; ++i) {
      FENCE();
      v = ld_stats(ws, off);
      if (!anyl(vm(0, v) == SENT)) { FENCE(); return v; }
    }
    dead = 1;
  }
  FENCE();
  return v;
}

DEV void pub_stats(rsrc_t ws, float s0, float s1, float q0, float q1, u32 red, u32 cnt, u32 last, u32 dst, int slot, int lane) {
  float t = sw16add(sw32add(s0, s1), sw32add(q0, q1));
  st<float>(red + slot * 256 + (16 * (lane >> 5) + (lane & 15)) * 8 + ((lane >> 4) & 1) * 4, t);
  FENCE();
  if (ladd_rtn(cnt, lane) == last) {
    f4 r = {0, 0, 0, 0};
    UNR for (int i = 0; i < 6; ++i) r += ld<f4>(red + i * 256 + (lane & 15) * 16);
    gst4(san(__builtin_bit_cast(u4, r)), ws, lane < 16 ? dst + lane * 16 : 0x80000000u, 0);
  }
}

struct KVin { u32 k[11], v[8]; };
DEV u4 kv_w(const Args& a, int T, int c) { return gld4(mkrs(a.sk, 4608), (144 * c + 2 * (T >> 2)) * 8, 0, 0); }
DEV void kv_read(int T, KVin& in) {
  const u32 base = (T & 3) * 2304 + (T >> 2) * 4;
  UNR for (int i = 0; i < 11; ++i) in.k[i] = ld<u32>(L_KT + base + i * 288);
  UNR for (int i = 0; i < 8; ++i) in.v[i] = ld<u32>(L_VT + base + i * 288);
}
DEV void kv_compute(rsrc_t ws, int T, int rt, int c, const KVin& in, u4 w) {
  int cp = T >> 2, sub = T & 3, ch = 144 * c + 2 * cp, s0 = 32 * rt + 8 * sub;
  f2 w0 = {__uint_as_float(w.x << 16), __uint_as_float(w.z << 16)}, w1 = {__uint_as_float(w.x & 0xffff0000u), __uint_as_float(w.z & 0xffff0000u)};
  f2 w2 = {__uint_as_float(w.y << 16), __uint_as_float(w.w << 16)}, w3 = {__uint_as_float(w.y & 0xffff0000u), __uint_as_float(w.w & 0xffff0000u)};
  f2 kf[11];
  UNR for (int i = 0; i < 11; ++i) kf[i] = up(in.k[i]);
  u32 P[8];
  UNR for (int s = 0; s < 8; ++s) {
    f2 t = rnd(kf[s] * w3);
    t = rnd(t + rnd(kf[s + 1] * w2));
    t = rnd(t + rnd(kf[s + 2] * w1));
    t = rnd(t + rnd(kf[s + 3] * w0));
    P[s] = pk(t * up(in.v[s]));
  }
  u4 o0, o1;
  UNR for (int d = 0; d < 4; ++d) {
    o0[d] = __builtin_amdgcn_perm(P[6 - 2 * d], P[7 - 2 * d], 0x05040100u);
    o1[d] = __builtin_amdgcn_perm(P[6 - 2 * d], P[7 - 2 * d], 0x07060302u);
  }
  u32 off = O_KVT + ch * 4096 + (2040 - s0) * 2;
  gst4(o0, ws, off, 0);
  gst4(o1, ws, off, 4096);
}

struct Hin { u4 h[4]; u32 n[4]; };
DEV void h_load(const Args& a, int ch, int lane, Hin& in) {
  rsrc_t rh = mkrs(a.ssm, 2359296);
  UNR for (int m = 0; m < 4; ++m) { in.h[m] = gld4(rh, ch * 4096 + lane * 16, m * 1024, 0); in.n[m] = gld1(rh, ch * 4096 + lane * 16 + 16, m * 1024, 0); }
}
DEV void h_stage(u32 lH, u32 lU, int lane, const Hin& in) {
  UNR for (int m = 0; m < 4; ++m) {
    u4 h = in.h[m];
    u4 s = {__builtin_amdgcn_alignbit(h.y, h.x, 16), __builtin_amdgcn_alignbit(h.z, h.y, 16), __builtin_amdgcn_alignbit(h.w, h.z, 16), __builtin_amdgcn_alignbit(in.n[m], h.w, 16)};
    st<u4>(lH + 32 + m * 1024 + lane * 16, h);
    st<u4>(lH + 4160 + 32 + m * 1024 + lane * 16, s);
  }
  u32 h0 = rfl(in.h[0].x) << 16;
  u4 z = {0, 0, 0, lane == 3 ? h0 : 0u};
  if (lane < 4) st<u4>(lH + (lane >> 1) * 4160 + (lane & 1) * 16, z);
  u4 zz = {0, 0, 0, 0};
  if (lane < 30) st<u4>(lU + 4096 + lane * 16, zz);
}
DEV void u_stage(rsrc_t ws, u32 lU, int ch, int lane) {
  UNR for (int m = 0; m < 4; ++m) dma<SC1>(ws, lU + m * 1024, O_KVT + ch * 4096 + lane * 16, m * 1024);
}
DEV float load_dv(const Args& a, int ch) {
  u32 v = gld1(mkrs(a.dv, 1152), (ch >> 1) * 4, 0, 0);
  return __uint_as_float((ch & 1) ? v & 0xffff0000u : v << 16);
}
DEV u2 y_pack(f4 s, u32 lU, int j, float dv, int q, int i16) {
  u2 uu = ld<u2>(lU + 2 * (2044 - 256 * j - 16 * i16 - 4 * q));
  f2 u32_ = up(uu.x), u10 = up(uu.y);
  f2 g01 = {u10.y * dv, u10.x * dv}, g23 = {u32_.y * dv, u32_.x * dv};
  f2 s01 = {s.x, s.y}, s23 = {s.z, s.w};
  return u2{pk(rnd(s01) + rnd(g01)), pk(rnd(s23) + rnd(g23))};
}
#define SB() __builtin_amdgcn_sched_barrier(0)
DEV void conv_pair(rsrc_t ws, u32 lH, u32 lU, int ch, int e, u32 cA, u32 cB, float dv, u32 woff, u4 (&wo)[36], int lane, int& dead) {
  const int q = lane >> 4, i16 = lane & 15;
  f4 acc[8];
  UNR for (int i = 0; i < 8; ++i) acc[i] = f4{0, 0, 0, 0};
  const u32 aB = lU + 2 * (2032 - 16 * i16 + 8 * q) - 3584 + 256 * e;
  const u32 aA = lH + ((i16 & 1) ? 0 : 4160) + 4 * (((i16 + 1) >> 1) + 4 * q) + 256 * e;
  const u32 aA4[4] = {aA, aA + 1024, aA + 2048, aA + 3072};
  const u32 wv = O_WOT + lane * 16;
  u4 B[8];
  u2 Al[4], Ah[4];
#define LA(s, t, bb)                                                                                                     \
  asm volatile("ds_read2_b32 %0, %2 offset0:%3 offset1:%4\n\tds_read2_b32 %1, %2 offset0:%5 offset1:%6"                  \
               : "=&v"(Al[s]), "=&v"(Ah[s])                                                                              \
               : "v"(aA4[(t) >> 1]), "n"(16 * (bb) + 128 * ((t) & 1)), "n"(16 * (bb) + 128 * ((t) & 1) + 1),              \
                 "n"(16 * (bb) + 128 * ((t) & 1) + 2), "n"(16 * (bb) + 128 * ((t) & 1) + 3)                               \
               : "memory")
#define LB(v, bb) asm volatile("ds_read_b128 %0, %1 offset:%2" : "=&v"(B[v]) : "v"(aB), "n"(512 * (7 - (v)) + 64 * (bb)) : "memory")
#define WA(n, s) asm volatile("s_waitcnt lgkmcnt(%4)" : "+v"(Al[s]), "+v"(Ah[s]), "+v"(Al[(s) + 1]), "+v"(Ah[(s) + 1]) : "n"(n))
#define WB(n, v) asm volatile("s_waitcnt lgkmcnt(%1)" : "+v"(B[v]) : "n"(n))
#define MM(s, t, v) do { acc[(t) + (v)] = mfma(u4{Al[s].x, Al[s].y, Ah[s].x, Ah[s].y}, B[v], acc[(t) + (v)]); SB(); } while (0)
#define LW(i) do { wo[i] = gld4(ws, wv, woff + (i) * 1024, 1); SB(); } while (0)
  LB(7, 0); LB(6, 0); LB(5, 0); LA(0, 0, 0); LA(1, 7, 0); LB(4, 0); LB(3, 0); LB(2, 0); LB(1, 0); LB(0, 0);
  UNR for (int bb = 0; bb < 4; ++bb) {
    const int nb = bb < 3 ? 1 : 0;
    LA(2, 1, bb); LA(3, 6, bb);
    asm volatile("s_waitcnt lgkmcnt(9)" : "+v"(Al[0]), "+v"(Ah[0]), "+v"(Al[1]), "+v"(Ah[1]), "+v"(B[7]), "+v"(B[6]), "+v"(B[5]));
    SB();
    MM(0, 0, 7); if (nb) LB(7, bb + 1);
    MM(0, 0, 6); MM(0, 0, 5); LW(9 * bb);
    WB(8 + nb, 4); MM(0, 0, 4); WB(7 + nb, 3); MM(0, 0, 3); WB(6 + nb, 2); MM(0, 0, 2); LW(9 * bb + 1);
    WB(5 + nb, 1); MM(0, 0, 1); WB(4 + nb, 0); MM(0, 0, 0); MM(1, 7, 0);
    LA(0, 2, bb); LA(1, 5, bb); WA(4 + nb, 2); SB();
    MM(2, 1, 5); MM(2, 1, 6); if (nb) LB(6, bb + 1);
    MM(2, 1, 4); LW(9 * bb + 2); MM(2, 1, 3); MM(2, 1, 2); MM(2, 1, 1); LW(9 * bb + 3); MM(2, 1, 0); MM(3, 6, 1); MM(3, 6, 0);
    LA(2, 3, bb); LA(3, 4, bb); WA(4 + nb, 0); SB();
    MM(0, 2, 5); if (nb) LB(5, bb + 1);
    MM(0, 2, 4); MM(0, 2, 3); LW(9 * bb + 4); MM(0, 2, 2); MM(0, 2, 1); MM(0, 2, 0); LW(9 * bb + 5); MM(1, 5, 2); MM(1, 5, 1); MM(1, 5, 0);
    if (nb) { LA(0, 0, bb + 1); LA(1, 7, bb + 1); }
    WA(5 * nb, 2); SB();
    MM(2, 3, 4); if (nb) LB(4, bb + 1);
    MM(2, 3, 3); LW(9 * bb + 6); MM(2, 3, 2); MM(2, 3, 1); LW(9 * bb + 7); MM(2, 3, 0);
    MM(3, 4, 3); if (nb) LB(3, bb + 1);
    MM(3, 4, 2); if (nb) LB(2, bb + 1);
    MM(3, 4, 1); if (nb) LB(1, bb + 1);
    MM(3, 4, 0); if (nb) LB(0, bb + 1);
    LW(9 * bb + 8);
  }
  FENCE();
  ladd(cA, lane);
  lwait(cA, 2, dead);
  f4 keep[4];
  UNR for (int i = 0; i < 4; ++i) {
    keep[i] = e ? acc[4 + i] : acc[i];
    f4 snd = e ? acc[i] : acc[4 + i];
    st<f4>(lH + e * 4096 + i * 1024 + lane * 16, snd);
  }
  FENCE();
  ladd(cB, lane);
  lwait(cB, 2, dead);
  u2 Y[4];
  UNR for (int i = 0; i < 4; ++i) Y[i] = y_pack(keep[i] + ld<f4>(lH + (1 - e) * 4096 + i * 1024 + lane * 16), lU, 4 * e + i, dv, q, i16);
  UNR for (int i = 0; i < 2; ++i) {
    auto r0 = SW16(Y[2 * i].x, Y[2 * i + 1].x);
    auto r1 = SW16(Y[2 * i].y, Y[2 * i + 1].y);
    u4 o = {r0[0], r1[0], r0[1], r1[1]};
    gst4(o, ws, O_YT + ch * 4096 + 2 * (256 * (4 * e + 2 * i + (q & 1)) + 16 * i16 + 8 * (q >> 1)), 0);
  }
}
DEV void conv_quarter(rsrc_t ws, int ch, int iq, int h, float dv, int lane, int& dead) {
  const int q = lane >> 4, i16 = lane & 15;
  const u32 aB = L_U2 + 2 * (2032 - 16 * i16 + 8 * q) + 128 * h;
  const u32 aA = L_H2 + 4 * (((i16 + 1) >> 1) + 4 * q) + 128 * h;
  const u32 sh = (i16 & 1) ? 0 : 16;
  f4 m[5];
  UNR for (int i = 0; i < 5; ++i) m[i] = f4{0, 0, 0, 0};
  u2 a0[3][2], a1[3][2];
  u32 a2[3][2];
  u4 bq[3][2];
#define LQ(n)                                                                                                                     \
  {                                                                                                                               \
    const int t_ = (n) <= iq ? (n) : (n)-iq - 1, v_ = (n) <= iq ? iq - (n) : 8 - (n);                                             \
    const u32 pa = aA + 512 * t_, pb = aB - 512 * v_;                                                                             \
    asm volatile("ds_read2_b32 %0, %8 offset0:0 offset1:1\n\tds_read2_b32 %1, %8 offset0:2 offset1:3\n\tds_read_b32 %2, %8 offset:16\n\t" \
                 "ds_read_b128 %3, %9\n\tds_read2_b32 %4, %8 offset0:16 offset1:17\n\tds_read2_b32 %5, %8 offset0:18 offset1:19\n\t"  \
                 "ds_read_b32 %6, %8 offset:80\n\tds_read_b128 %7, %9 offset:64"                                                  \
                 : "=&v"(a0[(n) % 3][0]), "=&v"(a1[(n) % 3][0]), "=&v"(a2[(n) % 3][0]), "=&v"(bq[(n) % 3][0]), "=&v"(a0[(n) % 3][1]), \
                   "=&v"(a1[(n) % 3][1]), "=&v"(a2[(n) % 3][1]), "=&v"(bq[(n) % 3][1])                                            \
                 : "v"(pa), "v"(pb)                                                                                               \
                 : "memory");                                                                                                     \
  }
  LQ(0)
  UNR for (int n = 0; n < 9; ++n) {
    if (n < 8) LQ(n + 1)
    const int s = n % 3;
    asm volatile("s_waitcnt lgkmcnt(%8)" : "+v"(a0[s][0]), "+v"(a1[s][0]), "+v"(a2[s][0]), "+v"(bq[s][0]), "+v"(a0[s][1]), "+v"(a1[s][1]), "+v"(a2[s][1]), "+v"(bq[s][1]) : "n"(n < 8 ? 8 : 0));
    UNR for (int k = 0; k < 2; ++k) {
      u4 A = {__builtin_amdgcn_alignbit(a0[s][k].y, a0[s][k].x, sh), __builtin_amdgcn_alignbit(a1[s][k].x, a0[s][k].y, sh),
              __builtin_amdgcn_alignbit(a1[s][k].y, a1[s][k].x, sh), __builtin_amdgcn_alignbit(a2[s][k], a1[s][k].y, sh)};
      m[n < 4 ? n : 4] = mfma(A, bq[s][k], m[n < 4 ? n : 4]);
    }
    SB();
  }
  f4 s1 = m[0], s2 = m[4];
  UNR for (int n = 1; n < 4; ++n) { if (n <= iq) s1 += m[n]; else s2 += m[n]; }
  FENCE();
  ladd(CNT(C_QA), lane);
  lwait(CNT(C_QA), 4, dead);
  st<f4>(L_H0 + h * 2048 + lane * 16, s1);
  st<f4>(L_H0 + h * 2048 + 1024 + lane * 16, s2);
  FENCE();
  ladd(CNT(C_QB), lane);
  if (h < 2) {
    lwait(CNT(C_QB), 4, dead);
    f4 s = ld<f4>(L_H0 + h * 1024 + lane * 16);
    UNR for (int i = 1; i < 4; ++i) s += ld<f4>(L_H0 + i * 2048 + h * 1024 + lane * 16);
    const int j = h ? 7 - iq : iq;
    u2 Y = y_pack(s, L_U2, j, dv, q, i16);
    auto r0 = SW16(Y.x, Y.x);
    auto r1 = SW16(Y.y, Y.y);
    gst4(u4{r0[0], r1[0], r0[1], r1[1]}, ws, (q & 1) ? 0x80000000u : O_YT + ch * 4096 + 2 * (256 * j + 16 * i16 + 8 * (q >> 1)), 0);
  }
}

template <bool HELP> struct Keep { u2 x[HELP ? 4 : 1], y[HELP ? 4 : 1]; };
template <bool HELP, int L>
DEV void tail(const Args& a, rsrc_t ws, int w, int lane0, int rt, int c, u32 tag, const u4 (&wo)[HELP ? 36 : 18], Keep<HELP>& kp, int& dead STARG) {
  constexpr int NB = HELP ? 2 : 1, NU = HELP ? 4 : 1;
  const int lane = opq(lane0), q = lane >> 4, i16 = lane & 15;
  const int bl0 = HELP ? 2 * (w - 4) : 8, slot = HELP ? w - 4 : 4 + w;
  if (L == 0 && HELP) {
    rsrc_t rx = mkrs(a.x, 2359296);
    const u32 o = (32 * rt + i16) * 1152 + (144 * c + 4 * q) * 2;
    UNR for (int u = 0; u < NU; ++u) kp.x[u] = gld2(rx, o, (bl0 + (u >> 1)) * 32 + (u & 1) * 16 * 1152, 0);
  }
  if (HELP) { bar(); STAMP(13 + L * 13); }
  u32 fyoff = 0, fy = tag;
  if (L == 0) {
    const int chl = 144 * c + 16 * bl0 + (lane & (16 * NB - 1)), j = rt >> 3;
    fyoff = O_FY + (chl < 512 ? chl >> 1 : 4 * (chl - 512) + (j < 4 ? j : 7 - j)) * 128;
    fy = gld1(ws, fyoff, 0, 1);
  }
  f4 acc[NU];
  UNR for (int u = 0; u < NU; ++u) acc[u] = f4{0, 0, 0, 0};
  if (HELP) {
    const u32 bo = L_STG + lane * 16;
    u4 R[5];
    UNR for (int n = 0; n < 5; ++n) R[n] = ld<u4>(bo + (n & 1) * 18432 + (n >> 1) * 1024);
    SB();
    UNR for (int n = 0; n < 36; ++n) {
      const int ks = n >> 1, tb = n & 1, nn = n + 5;
      acc[tb] = mfma(wo[ks], R[n % 5], acc[tb]);
      acc[NU - 2 + tb] = mfma(wo[(NB - 1) * 18 + ks], R[n % 5], acc[NU - 2 + tb]);
      SB();
      if (nn < 36) R[n % 5] = ld<u4>(bo + (nn & 1) * 18432 + (nn >> 1) * 1024);
      SB();
    }
  } else {
    f4 a2 = {0, 0, 0, 0};
    const u32 bo = L_STG + w * 18432 + lane * 16;
    u4 R[18];
    UNR for (int ks = 0; ks < 12; ++ks) R[ks] = ld<u4>(bo + ks * 1024);
    SB();
    UNR for (int ks = 0; ks < 18; ks += 2) {
      acc[0] = mfma(wo[ks], R[ks], acc[0]); a2 = mfma(wo[ks + 1], R[ks + 1], a2);
      if (ks < 6) { R[ks + 12] = ld<u4>(bo + (ks + 12) * 1024); R[ks + 13] = ld<u4>(bo + (ks + 13) * 1024); }
      SB();
    }
    acc[0] += a2;
  }
  FENCE();
  if (L == 0) ladd(CNT(C_G2), lane);
  u4 yv[NB];
  if (L == 0) {
    if (anyl(fy != tag)) poll(ws, fyoff, tag, dead);
    FENCE();
    UNR for (int i = 0; i < NB; ++i) yv[i] = gld4(ws, O_YT + (lane >> 2) * 4096 + (lane & 3) * 16, (144 * c + 16 * (bl0 + i)) * 4096 + rt * 64, 1);
  }
  f2 x1[NU][2];
  float S[2] = {0, 0}, Q[2] = {0, 0};
  UNR for (int u = 0; u < NU; ++u) {
    f2 lo = {acc[u].x, acc[u].y}, hi = {acc[u].z, acc[u].w};
    x1[u][0] = rnd(rnd(lo) + up(kp.x[u].x)); x1[u][1] = rnd(rnd(hi) + up(kp.x[u].y));
  }
  if (HELP) {
    UNR for (int tb = 0; tb < 2; ++tb) {
      f2 s = (x1[tb][0] + x1[tb][1]) + (x1[NU - 2 + tb][0] + x1[NU - 2 + tb][1]);
      f2 sq = x1[tb][0] * x1[tb][0] + x1[tb][1] * x1[tb][1] + x1[NU - 2 + tb][0] * x1[NU - 2 + tb][0] + x1[NU - 2 + tb][1] * x1[NU - 2 + tb][1];
      S[tb] = s.x + s.y; Q[tb] = sq.x + sq.y;
    }
  } else {
    f2 s = x1[0][0] + x1[0][1], sq = x1[0][0] * x1[0][0] + x1[0][1] * x1[0][1];
    S[0] = w ? 0.0f : s.x + s.y; S[1] = w ? s.x + s.y : 0.0f; Q[0] = w ? 0.0f : sq.x + sq.y; Q[1] = w ? sq.x + sq.y : 0.0f;
  }
  const u32 stoff = O_ST + L * 2 * 65536 + rt * 1024 + (q & 1) * 512 + (16 * (q >> 1) + i16) * 8;
  pub_stats(ws, S[0], S[1], Q[0], Q[1], L_REDA, CNT(C_REDA), 6 * L + 5, O_ST + L * 2 * 65536 + rt * 1024 + c * 256, slot, lane);
  STAMP(14 + L * 13);
  u4 sv = ld_stats(ws, stoff);
  if (L == 0) {
    const u32 yb = L_Y + (HELP ? bl0 : 8 + w) * 1024;
    UNR for (int i = 0; i < NB; ++i) st<u4>(yb + i * 1024 + lane * 16, yv[i]);
    FENCE();
    const u32 ya = yb + (4 * q + (i16 >> 2)) * 64 + (i16 & 3) * 8;
    UNR for (int u = 0; u < NU; ++u) kp.y[u] = trd(ya + (u >> 1) * 1024 + (HELP ? u & 1 : w) * 32);
  }
  sv = poll_stats(ws, stoff, sv, dead);
  STAMP(15 + L * 13);
  float m[2], rs[2];
  stats_from(sv, m, rs);
  UNR for (int u = 0; u < NU; ++u) {
    const float mu = HELP ? m[u & 1] : (w ? m[1] : m[0]), ru = HELP ? rs[u & 1] : (w ? rs[1] : rs[0]);
    UNR for (int h = 0; h < 2; ++h) {
      f2 xn = rnd(rnd(x1[u][h] - mu) * ru);
      kp.x[u][h] = pk(x1[u][h] + rnd(xn * up(kp.y[u][h])));
    }
  }
  UNR for (int i = 0; i < NB; ++i) {
    auto r0 = SW16(kp.x[2 * i].x, kp.x[HELP ? 2 * i + 1 : 0].x);
    auto r1 = SW16(kp.x[2 * i].y, kp.x[HELP ? 2 * i + 1 : 0].y);
    const u4 o = {r0[0], r1[0], r0[1], r1[1]};
    const int tb = HELP ? q & 1 : w;
    const bool on = HELP || !(q & 1);
    if (L == 0) gst4(san(o), ws, on ? O_X2 + tb * 18432 + (q >> 1) * 256 + i16 * 16 : 0x80000000u, 2 * rt * 18432 + (9 * c + bl0 + i) * 512);
    else __builtin_amdgcn_raw_buffer_store_b128(o, mkrs(a.out, 2359296), on ? (32 * rt + 16 * tb + i16) * 1152 + (q >> 1) * 16 + 288 * c + 32 * (bl0 + i) : 0x80000000u, 0, 0);
  }
  if (L == 0) {
    f2 s[NU], sq[NU];
    UNR for (int u = 0; u < NU; ++u) { f2 lo = up(kp.x[u].x), hi = up(kp.x[u].y); s[u] = lo + hi; sq[u] = lo * lo + hi * hi; }
    if (HELP) {
      UNR for (int tb = 0; tb < 2; ++tb) { f2 t = s[tb] + s[NU - 2 + tb], tq = sq[tb] + sq[NU - 2 + tb]; S[tb] = t.x + t.y; Q[tb] = tq.x + tq.y; }
    } else {
      S[0] = w ? 0.0f : s[0].x + s[0].y; S[1] = w ? s[0].x + s[0].y : 0.0f; Q[0] = w ? 0.0f : sq[0].x + sq[0].y; Q[1] = w ? sq[0].x + sq[0].y : 0.0f;
    }
    pub_stats(ws, S[0], S[1], Q[0], Q[1], L_REDB, CNT(C_REDB), 5, O_ST + 65536 + rt * 1024 + c * 256, slot, lane);
    if (HELP) STAMP(16);
  }
}

DEV void w_slot(rsrc_t ws, int j, int rot, int g, int lane) {
  int kr = j + rot; kr -= kr >= 9 ? 9 : 0;
  UNR for (int i = 0; i < 9; ++i) dma<SC1>(ws, j * SLOT + i * 1024, O_WT + lane * 16, g * WT_G + kr * SLOT + i * 1024);
  if (lane < 32) dma<SC1>(ws, j * SLOT + 9216, O_WT + lane * 16, g * WT_G + kr * SLOT + 9216);
}

template <int L>
DEV void main_layer(const Args& a, rsrc_t ws, int b, int w, int lane0, int p, int g, int rt, int c, int rot, u32 tag, u4 (&B)[36], float (&mS)[2], float (&mQ)[2], u32& wp, int& dead STARG) {
    constexpr float LOG2E = 1.4426950408889634f;
    int lane = opq(lane0), q = lane >> 4, i16 = lane & 15;
    u32 aF[2], a4[2];
    const int r4 = 64 + 3 * (i16 >> 2) + ((i16 & 3) < 2 ? (i16 & 3) : 2);
    UNR for (int u = 0; u < 2; ++u) {
      aF[u] = i16 * 128 + (((4 * u + q) ^ (i16 >> 1)) << 4);
      a4[u] = r4 * 128 + (((4 * u + q) ^ ((r4 >> 1) & 7)) << 4);
    }
    {
      f4 acc[5][2];
      u4 sv = {0, 0, 0, 0};
      const u32 stoff = O_ST + 65536 + (4 * p + w) * 1024 + (q & 1) * 512 + (16 * (q >> 1) + i16) * 8;
      if (L == 1) {
        u32 G = lane < 36 ? lane : (lane < 40 ? 9 * (lane - 36) + 8 : 0);
        u32 po = O_X2 + (8 * p + 2 * w + (lane >= 36 && lane < 40 ? 1 : 0)) * 18432 + G * 512;
        NOUNR for (int i = dead ? SPIN_G : 0; i < SPIN_G; ++i) {
          FENCE();
          u32 v = gld1(ws, po, 0, 1);
          if (!anyl(v == SENT)) break;
        }
        FENCE();
        STAMP(16);
      }
      f4 cs[5];
      bool svbad = false;
      for (int pass = 0;; ++pass) {
        if (L == 1) {
          sv = ld_stats(ws, stoff);
          UNR for (int j = 0; j < 9; ++j) {
            int kr = j + rot; kr -= kr >= 9 ? 9 : 0;
            UNR for (int i = 0; i < 4; ++i) B[4 * j + i] = gld4(ws, O_X2 + lane * 16 + (i >> 1) * 1024, (8 * p + 2 * w + (i & 1)) * 18432 + kr * 2048, 1);
          }
          SB();
        }
        UNR for (int i = 0; i < 5; ++i) acc[i][0] = acc[i][1] = f4{0, 0, 0, 0};
        u32 vmx = 0, fl = 0;
        if (L == 0) { wp = lwait(CNT(C_WPROG), 1, dead); STAMP(3); }
        u32 b0[2] = {aF[0], aF[1]}, b4[2] = {a4[0], a4[1]};
        u4 A[5];
        UNR for (int i = 0; i < 5; ++i) A[i] = ld<u4>(i < 4 ? b0[0] + i * 2048 : b4[0]);
        SB();
        UNR for (int s = 0; s < 18; ++s) {
          const int j = (s + 1) >> 1, u = (s + 1) & 1, jo = j < 5 ? j : j - 5;
          const bool chk = L == 0 || j == 5 || (j == 4 && u == 1);
          if (s == 9) { UNR for (int k = 0; k < 2; ++k) { b0[k] = opq(b0[k] + 5 * SLOT); b4[k] = opq(b4[k] + 5 * SLOT); } }
          UNR for (int i = 0; i < 5; ++i) {
            acc[i][0] = mfma(A[i], B[2 * s], acc[i][0]);
            acc[i][1] = mfma(A[i], B[2 * s + 1], acc[i][1]);
            SB();
            if (i == 0 && u == 0 && chk && s < 17) {
              const u32 need = L ? 11u : (u32)(j + 1);
              wp = umax(wp, rfl(fl));
              if (wp < need) wp = lwait(CNT(C_WPROG), need, dead);
            }
            if (s < 17) A[i] = ld<u4>((i < 4 ? b0[u] + i * 2048 : b4[u]) + jo * SLOT);
            else {
              if (L == 0 && i == 0) lwait(CNT(C_TBL), 1, dead);
              cs[i] = ld<f4>(L_CSL + i * 64 + q * 16);
            }
            if (i == 0 && u == 1 && chk && s < 16) fl = *(volatile LAS u32*)(uintptr_t)CNT(C_WPROG);
            if (L == 1 && (i == 1 || i == 3)) { vmx = vm(vmx, B[2 * s + (i >> 1)]); asm volatile("" : "+v"(vmx)); }
            if (L == 1 && s == 4 && i == 3) { svbad = anyl(vm(0, sv) == SENT); stats_from(sv, mS, mQ); }
            SB();
          }
        }
        if (L == 0 || !anyl(vmx == SENT) || dead) break;
        if (pass >= 20000) { dead = 1; break; }
        FENCE();
      }
      FENCE();
      ladd(CNT(C_GEM), lane);
      STAMP(4 + L * 13);
      lane = opq(lane); q = lane >> 4; i16 = lane & 15;
      if (L == 1 && svbad) { sv = poll_stats(ws, stoff, sv, dead); stats_from(sv, mS, mQ); }
      u32 ql[2];
      {
        const bool val = (i16 >> 2) == q;
        const u32 tk = L_TK + (val ? ((q & 1) * 4 + (i16 & 3)) * 16 : 128), tk2 = L_TK2 + (val ? ((q & 1) * 4 + (i16 & 3)) * 4 : 32);
        u4 t0[5], t1[5];
        u32 t2[5];
        u32 kv[2][9];
        UNR for (int i = 0; i < 5; ++i) {
          f2 c01 = {cs[i].x, cs[i].y}, c23 = {cs[i].z, cs[i].w};
          UNR for (int tb = 0; tb < 2; ++tb) {
            f2 lo = {acc[i][tb].x, acc[i][tb].y}, hi = {acc[i][tb].z, acc[i][tb].w};
            kv[tb][i < 4 ? 2 * i : 8] = pk((lo - c01 * mS[tb]) * mQ[tb]);
            if (i < 4) kv[tb][2 * i + 1] = pk((hi - c23 * mS[tb]) * mQ[tb]);
            else ql[tb] = pk((hi.x - c23.x * mS[tb]) * mQ[tb], 0.0f);
          }
          t0[i] = ld<u4>(tk + i * 288); t1[i] = ld<u4>(tk + i * 288 + 144); t2[i] = ld<u32>(tk2 + i * 36);
          SB();
        }
        u32 K[9], V[9];
        UNR for (int i = 0; i < 9; ++i) { auto t = SW32(kv[0][i], kv[1][i]); K[i] = t[0]; V[i] = t[1]; }
        const u4 k0 = {K[0], K[1], K[2], K[3]}, k1 = {K[4], K[5], K[6], K[7]}, k2 = {K[8], 0, 0, 0};
        f4 P[5];
        SB();
        UNR for (int m = 0; m < 5; ++m) P[m] = mfma(t0[m], k0, f4{0, 0, 0, 0});
        SB();
        UNR for (int m = 0; m < 5; ++m) P[m] = mfma(t1[m], k1, P[m]);
        SB();
        UNR for (int m = 0; m < 5; ++m) P[m] = mfma(u4{t2[m], 0, 0, 0}, k2, P[m]);
        SB();
        const u32 row = L_STG + (32 * w + 16 * (q >> 1) + i16) * 232;
        lwait(CNT(C_GEM), 4 * (L + 1), dead);
        {
          u32 vo = row + 144 + (q & 1) * 40;
          UNR for (int i = 0; i < 4; ++i) st<u2>(vo + 8 * i, u2{V[2 * i], V[2 * i + 1]});
          st<u2>(vo + 32, u2{V[8], 0x00003F80u});
        }
        SB();
        f2 z[9];
        UNR for (int i = 0; i < 9; ++i) z[i] = f2{P[i >> 1][2 * (i & 1)], P[i >> 1][2 * (i & 1) + 1]};
        float mx = fmaxf(z[0].x, z[0].y), mn = fminf(z[0].x, z[0].y);
        UNR for (int i = 1; i < 9; ++i) { mx = fmaxf(fmaxf(mx, z[i].x), z[i].y); mn = fminf(fminf(mn, z[i].x), z[i].y); }
        const float ma = -mx * LOG2E, mb = mn * LOG2E;
        f2 e1[9], e2[9], s1 = {0, 0}, s2 = {0, 0};
        UNR for (int i = 0; i < 9; ++i) {
          f2 t1 = z[i] * LOG2E + ma, t2 = mb - z[i] * LOG2E;
          e1[i] = f2{__builtin_amdgcn_exp2f(t1.x), __builtin_amdgcn_exp2f(t1.y)};
          e2[i] = f2{__builtin_amdgcn_exp2f(t2.x), __builtin_amdgcn_exp2f(t2.y)};
          s1 += e1[i]; s2 += e2[i];
        }
        const float r1 = __builtin_amdgcn_rcpf(s1.x + s1.y), r2 = __builtin_amdgcn_rcpf(s2.x + s2.y);
        u32 fo = row + (q & 1) * 72;
        UNR for (int i = 0; i < 4; ++i) {
          st<u2>(fo + 8 * i, u2{pk(e1[2 * i] * r1), pk(e1[2 * i + 1] * r1)});
          st<u2>(fo + 32 + 8 * i, u2{pk(e2[2 * i] * r2), pk(e2[2 * i + 1] * r2)});
        }
        st<u2>(fo + 64, u2{pk(e1[8] * r1), pk(e2[8] * r2)});
        FENCE();
        ladd(CNT(C_PHI), lane);
        lwait(CNT(C_PHI), 4 * (L + 1), dead);
        STAMP(5 + L * 13);
        lane = opq(lane); q = lane >> 4; i16 = lane & 15;
        const int hd = w >> 1, db = w & 1;
        const u32 ta = L_STG + (16 * (q & 1) + 4 * (i16 >> 2) + (q >> 1)) * 232 + (i16 & 3) * 8;
        f4 Dd[3] = {f4{0, 0, 0, 0}, f4{0, 0, 0, 0}, f4{0, 0, 0, 0}};
        const u32 tf = ta + 72 * hd, tv = ta + 144 + 40 * hd + 32 * db;
        u4 bv[4], af[4][3];
        UNR for (int ks = 0; ks < 4; ++ks) {
          bv[ks] = trd2(tv + ks * 7424, tv + ks * 7424 + 464);
          UNR for (int fb = 0; fb < 3; ++fb) af[ks][fb] = trd2(tf + ks * 7424 + fb * 32, tf + ks * 7424 + fb * 32 + 464);
          if (ks & 1) SB();
          if (ks == 1) { UNR for (int fb = 0; fb < 3; ++fb) Dd[fb] = mfma(af[0][fb], bv[0], Dd[fb]); SB(); }
        }
        UNR for (int ks = 1; ks < 4; ++ks) UNR for (int fb = 0; fb < 3; ++fb) Dd[fb] = mfma(af[ks][fb], bv[ks], Dd[fb]);
        auto s0 = SW16(pk(Dd[0].x, Dd[0].y), pk(Dd[1].x, Dd[1].y));
        auto s1b = SW16(pk(Dd[0].z, Dd[0].w), pk(Dd[1].z, Dd[1].w));
        u4 o = {s0[0], s1b[0], s0[1], s1b[1]};
        const int d = 16 * db + i16;
        const u32 so = L * SP_L + g * SP_G + p * SP_P;
        gst4(san(o), ws, d < 19 ? O_SP + (hd * 19 + d) * 64 + (q & 1) * 32 + (q >> 1) * 16 : 0x80000000u, so);
        gst2(u2{san(pk(Dd[2].x, Dd[2].y)), san(pk(Dd[2].z, Dd[2].w))}, ws, (d < 19 && q == 0) ? O_SP + 2432 + (hd * 19 + d) * 8 : 0x80000000u, so);
      }
      ladd(CNT(C_PUB), lane);
      STAMP(6 + L * 13);
      u32 fq[2][9];
      {
        if (wp < 10) wp = lwait(CNT(C_WPROG), 10, dead);
        f4 aq[2][2];
        UNR for (int i = 0; i < 2; ++i) aq[i][0] = aq[i][1] = f4{0, 0, 0, 0};
        lane = opq(lane); q = lane >> 4; i16 = lane & 15;
        const u32 bq[2] = {(u32)opq(L_WQ + i16 * 128 + ((q ^ (i16 >> 1)) << 4)), (u32)opq(L_WQ + i16 * 128 + (((4 + q) ^ (i16 >> 1)) << 4))};
        const bool val = (i16 >> 3) == (q >> 1);
        const u32 tq = L_TQ + (val ? (q * 4 + (i16 & 3)) * 16 : 256), tq2 = L_TQ2 + (val ? (q * 4 + (i16 & 3)) * 4 : 64);
        u4 A[4], t0[5];
        u32 t1[5];
        f4 c5, c6;
        UNR for (int n = 0; n < 4; ++n) A[n] = ld<u4>(bq[n >> 1] + (n & 1) * 2048);
        SB();
        UNR for (int n = 0; n < 36; ++n) {
          const int s = n >> 1, i = n & 1, sn = s + 2;
          aq[i][0] = mfma(A[n & 3], B[2 * s], aq[i][0]);
          aq[i][1] = mfma(A[n & 3], B[2 * s + 1], aq[i][1]);
          SB();
          if (n < 32) A[n & 3] = ld<u4>(bq[sn & 1] + (sn >> 1) * 4096 + i * 2048);
          if (n == 30) { c5 = ld<f4>(L_CSL + 5 * 64 + q * 16); c6 = ld<f4>(L_CSL + 6 * 64 + q * 16); }
          if (n >= 31) { t0[n - 31] = ld<u4>(tq + (n - 31) * 272); t1[n - 31] = ld<u32>(tq2 + (n - 31) * 68); }
          SB();
        }
        STAMP(7 + L * 13);
        u4 q0[2];
        UNR for (int tb = 0; tb < 2; ++tb) {
          q0[tb].x = pk((f2{aq[0][tb].x, aq[0][tb].y} - f2{c5.x, c5.y} * mS[tb]) * mQ[tb]);
          q0[tb].y = pk((f2{aq[0][tb].z, aq[0][tb].w} - f2{c5.z, c5.w} * mS[tb]) * mQ[tb]);
          q0[tb].z = pk((f2{aq[1][tb].x, aq[1][tb].y} - f2{c6.x, c6.y} * mS[tb]) * mQ[tb]);
          q0[tb].w = pk((f2{aq[1][tb].z, aq[1][tb].w} - f2{c6.z, c6.w} * mS[tb]) * mQ[tb]);
        }
        f4 P[2][5];
        SB();
        UNR for (int tb = 0; tb < 2; ++tb) {
          UNR for (int m = 0; m < 5; ++m) P[tb][m] = mfma(u4{t1[m], 0, 0, 0}, u4{ql[tb], 0, 0, 0}, f4{0, 0, 0, 0});
          SB();
          UNR for (int m = 0; m < 5; ++m) P[tb][m] = mfma(t0[m], q0[tb], P[tb][m]);
          SB();
        }
        const float ls = (q & 1) ? -LOG2E : LOG2E;
        UNR for (int tb = 0; tb < 2; ++tb) {
          f2 z[9];
          UNR for (int i = 0; i < 9; ++i) z[i] = f2{P[tb][i >> 1][2 * (i & 1)], P[tb][i >> 1][2 * (i & 1) + 1]} * ls;
          float mx = fmaxf(z[0].x, z[0].y);
          UNR for (int i = 1; i < 9; ++i) mx = fmaxf(fmaxf(mx, z[i].x), z[i].y);
          f2 s1 = {0, 0};
          UNR for (int i = 0; i < 9; ++i) { f2 t = z[i] - mx; z[i] = f2{__builtin_amdgcn_exp2f(t.x), __builtin_amdgcn_exp2f(t.y)}; s1 += z[i]; }
          const float r1 = __builtin_amdgcn_rcpf(s1.x + s1.y);
          UNR for (int i = 0; i < 9; ++i) fq[tb][i] = pk(z[i] * r1);
        }
      }
      STAMP(8 + L * 13);
      lane = opq(lane); q = lane >> 4; i16 = lane & 15;
      {
        u4 sel;
        UNR for (int d = 0; d < 4; ++d) sel[d] = ((i16 & 7) >> 1) == d ? ((i16 & 1) ? 0x3F800000u : 0x00003F80u) : 0u;
        u4 v[12];
        u32 off[3];
        UNR for (int gi = 0; gi < 3; ++gi) off[gi] = O_SP + q * SP_P + umin(16 * umin(w + 4 * gi, 10) + i16, 170) * 16;
        if (L == 0) lwait(CNT(C_FAOK), 1, dead);
        u32 bad = 0xFFF;
        NOUNR for (int it = dead ? SPIN_G - 1 : 0;; ++it) {
          FENCE();
          UNR for (int i = 0; i < 12; ++i) if (bad & (1u << i)) v[i] = gld4(ws, off[i >> 2], L * SP_L + g * SP_G + 4 * (i & 3) * SP_P, 1);
          bad = 0;
          UNR for (int i = 0; i < 12; ++i) bad |= anyl(vm(0, v[i]) == SENT) ? 1u << i : 0u;
          if (!bad) break;
          if (it >= SPIN_G) { dead = 1; break; }
        }
        FENCE();
        STAMP(9 + L * 13);
        UNR for (int gi = 0; gi < 3; ++gi) {
          f4 s = {0, 0, 0, 0};
          UNR for (int pp = 0; pp < 4; ++pp) s = mfma(sel, v[4 * gi + pp], s);
          int ck = 16 * umin(w + 4 * gi, 10) + i16;
          if (q < 2 && ck < 171) st<u2>(L_STB + ck * 16 + q * 8, u2{pk(s.x, s.y), pk(s.z, s.w)});
        }
        FENCE();
        ladd(CNT(C_STB), lane);
      }
      lwait(CNT(C_STB), 4 * (L + 1), dead);
      STAMP(10 + L * 13);
      {
        const bool val = (i16 >> 3) == (q >> 1);
        const int d01 = (q >> 1) * 19 + 8 * ((i16 >> 2) & 1) + (i16 & 3), d2 = (q >> 1) * 19 + 16 + ((i16 & 3) < 2 ? (i16 & 3) : 2);
        const u32 m01 = val ? L_STB + d01 * 64 + (q & 1) * 32 : L_ZERO, m2 = val ? L_STB + d2 * 64 + (q & 1) * 32 : L_ZERO;
        const u32 l01 = val ? L_STB + 2432 + d01 * 8 + (q & 1) * 4 : L_ZERO, l2 = val ? L_STB + 2432 + d2 * 8 + (q & 1) * 4 : L_ZERO;
        f4 O[2][3];
        u4 t0[3], t1[3];
        u32 t2[3];
        UNR for (int k = 0; k < 3; ++k) { const int m = (k + 2) % 3; t0[m] = ld<u4>(m < 2 ? m01 + m * 256 : m2); }
        UNR for (int k = 0; k < 3; ++k) { const int m = (k + 2) % 3; t1[m] = ld<u4>((m < 2 ? m01 + m * 256 : m2) + 16); }
        UNR for (int k = 0; k < 3; ++k) { const int m = (k + 2) % 3; t2[m] = ld<u32>(m < 2 ? l01 + m * 32 : l2); }
        SB();
        UNR for (int k = 0; k < 3; ++k) UNR for (int tb = 0; tb < 2; ++tb) { const int m = (k + 2) % 3; O[tb][m] = mfma(t0[m], u4{fq[tb][0], fq[tb][1], fq[tb][2], fq[tb][3]}, f4{0, 0, 0, 0}); }
        SB();
        UNR for (int k = 0; k < 3; ++k) UNR for (int tb = 0; tb < 2; ++tb) { const int m = (k + 2) % 3; O[tb][m] = mfma(t1[m], u4{fq[tb][4], fq[tb][5], fq[tb][6], fq[tb][7]}, O[tb][m]); }
        SB();
        UNR for (int k = 0; k < 3; ++k) UNR for (int tb = 0; tb < 2; ++tb) { const int m = (k + 2) % 3; O[tb][m] = mfma(u4{t2[m], 0, 0, 0}, u4{fq[tb][8], 0, 0, 0}, O[tb][m]); }
        SB();
        u32 lo[2];
        if (L == 0) lwait(CNT(C_CLR), 1, dead);
        UNR for (int tb = 0; tb < 2; ++tb) {
          const float inv = __builtin_amdgcn_rcpf(O[tb][2].z + 1e-6f);
          u4 o = {pk(f2{O[tb][0].x, O[tb][0].y} * inv), pk(f2{O[tb][0].z, O[tb][0].w} * inv), pk(f2{O[tb][1].x, O[tb][1].y} * inv), pk(f2{O[tb][1].z, O[tb][1].w} * inv)};
          gst4(san(o), ws, O_OB + lane * 16, L * OB_L + p * OB_P + (2 * w + tb) * 16384 + g * 1024);
          lo[tb] = pk(f2{O[tb][2].x, O[tb][2].y} * inv);
        }
        const u32 lv = san((q & 1) ? lo[1] : lo[0]);
        auto hh = SW32(lv, lv);
        const u32 n0 = __builtin_amdgcn_update_dpp(0u, hh[0], 0xB1, 0xF, 0xF, true), n1 = __builtin_amdgcn_update_dpp(0u, hh[1], 0xB1, 0xF, 0xF, true);
        gst4(u4{hh[0], hh[1], n0, n1}, ws, (q < 2 && !(i16 & 1)) ? O_OB + 131072 + (16 * q + i16) * 8 : 0x80000000u, L * OB_L + p * OB_P + g * 1024 + w * 256);
        ladd(CNT(C_PUB), lane);
      }
      STAMP(11 + L * 13);
      lane = opq(lane); q = lane >> 4; i16 = lane & 15;
      u4 wo[18];
      Keep<false> kp;
      if (w < 2) {
        UNR for (int i = 0; i < 18; ++i) wo[i] = gld4(ws, O_WOT + lane * 16, (9 * c + 8) * 18432 + i * 1024, 1);
        if (L == 0) kp.x[0] = gld2(mkrs(a.x, 2359296), (32 * rt + i16) * 1152 + (144 * c + 4 * q) * 2, 256 + w * 16 * 1152, 0);
        else { u4 t = ld<u4>(L_KEEP + w * 1024 + lane * 16); kp.x[0] = u2{t.x, t.y}; kp.y[0] = u2{t.z, t.w}; }
      }
      {
        const u32 pb = L * OB_L + p * OB_P, tbg = 2 * (rt & 3) + (w >> 1);
        u4 v[9];
        u32 bad = 0x1FF;
        NOUNR for (int it = dead ? SPIN_G - 1 : 0;; ++it) {
          FENCE();
          UNR for (int i = 0; i < 8; ++i) if (bad & (1u << i)) v[i] = gld4(ws, O_OB + lane * 16, pb + tbg * 16384 + (8 * (w & 1) + i) * 1024, 1);
          if (bad & 0x100) {
            u32 o = O_OB + 131072 + q * 2048 + i16 * 8, so = pb + (w & 1) * 8192 + tbg * 128;
            u2 l0 = gld2(ws, o, so, 1), l1 = gld2(ws, o + 1024, so, 1);
            v[8] = u4{l0.x, l0.y, l1.x, l1.y};
          }
          bad = 0;
          UNR for (int i = 0; i < 9; ++i) bad |= anyl(vm(0, v[i]) == SENT) ? 1u << i : 0u;
          if (!bad) break;
          if (it >= SPIN_G) { dead = 1; break; }
        }
        FENCE();
        STAMP(12 + L * 13);
        lwait(CNT(C_PUB), 8 * L + 8, dead);
        const u32 sb = L_STG + (w >> 1) * 18432 + lane * 16;
        UNR for (int i = 0; i < 8; ++i) st<u4>(sb + (8 * (w & 1) + i) * 1024, v[i]);
        st<u4>(sb + (16 + (w & 1)) * 1024, v[8]);
      }
      bar();
      STAMP(13 + L * 13);
      if (w < 2) {
        tail<false, L>(a, ws, w, lane, rt, c, tag, wo, kp, dead STPASS);
        if (L == 0) st<u4>(L_KEEP + w * 1024 + opq(lane) * 16, u4{kp.x[0].x, kp.x[0].y, kp.y[0].x, kp.y[0].y});
      }
      else if (L == 0 && w == 2) {
        lwait(CNT(C_G2), 6, dead);
        UNR for (int j = 5; j < 9; ++j) w_slot(ws, j, rot, g, lane);
        vmwait0();
        lset(CNT(C_WPROG), 11);
      }
    }
}

__global__ void __launch_bounds__(512, 2) fused_kernel(Args a) {
  const int tid = threadIdx.x, lane = tid & 63, w = rfl(tid >> 6), b = blockIdx.x;
  const int q = lane >> 4, i16 = lane & 15;
  const int xcd = b & 7, r = b >> 3;
  const int p = 4 * (xcd >> 1) + (r >> 3), g = 8 * (xcd & 1) + (r & 7), rot = ((r & 7) + 2 * (r >> 3)) % 9;
  const int rt = 4 * p + (g >> 2), c = g & 3;
  rsrc_t ws = mkrs(a.ws, WS_BYTES);
  int dead = 0;
  STAMP(0); STAMPC(29);
  const u32 tagv = gld1(ws, 0, 0, 1);

  if (w < 4) {
    __builtin_amdgcn_s_setprio(2);
    u4 B[36];
    float mS[2], mQ[2];
    {
      rsrc_t rx = mkrs(a.x, 2359296);
      u32 vo[2];
      UNR for (int par = 0; par < 2; ++par) vo[par] = (8 * par + (lane >> 3)) * 1152 + (((lane & 7) ^ (4 * par + (lane >> 4))) << 4);
      auto x_step = [&](int j) {
        int kr = j + rot; kr -= kr >= 9 ? 9 : 0;
        UNR for (int rg = 0; rg < 4; ++rg) dma<0>(rx, j * 16384 + w * 4096 + rg * 1024, vo[rg & 1], (128 * p + 32 * w + 16 * (rg >> 1)) * 1152 + kr * 128);
      };
      SB();
      bar();
      SB();
      UNR for (int j = 0; j < 9; ++j) x_step(j);
      FENCE();
      SB();
      STAMP(1);
      u32 a0 = w * 4096 + (i16 >> 3) * 1024 + (i16 & 7) * 128 + ((q ^ (i16 >> 1)) << 4), a1 = a0 ^ 64, pa = CNT(C_PROG) + 4 * w;
      f4 gS[2] = {f4{0, 0, 0, 0}, f4{0, 0, 0, 0}}, gQ[2] = {f4{0, 0, 0, 0}, f4{0, 0, 0, 0}};
      const u4 ones = {0x3F803F80u, 0x3F803F80u, 0x3F803F80u, 0x3F803F80u};
#define X0STEP(j, n)                                                                                                                   \
  {                                                                                                                                    \
    u32 pv = j + 1;                                                                                                                    \
    asm volatile("s_waitcnt vmcnt(%8)\n\tds_read_b128 %0, %4\n\tds_read_b128 %1, %4 offset:2048\n\tds_read_b128 %2, %5\n\t"            \
                 "ds_read_b128 %3, %5 offset:2048\n\ts_waitcnt lgkmcnt(0)\n\tds_write_b32 %6, %7"                                       \
                 : "=&v"(B[4 * j]), "=&v"(B[4 * j + 1]), "=&v"(B[4 * j + 2]), "=&v"(B[4 * j + 3])                                        \
                 : "v"(a0 + j * 16384), "v"(a1 + j * 16384), "v"(pa), "v"(pv), "n"(n)                                                   \
                 : "memory");                                                                                                          \
    UNR for (int i = 0; i < 4; ++i) { gS[i & 1] = mfma(ones, B[4 * j + i], gS[i & 1]); gQ[i & 1] = mfma(B[4 * j + i], B[4 * j + i], gQ[i & 1]); } \
  }
      X0STEP(0, 32) X0STEP(1, 28) X0STEP(2, 24) X0STEP(3, 20) X0STEP(4, 16) X0STEP(5, 12) X0STEP(6, 8) X0STEP(7, 4) X0STEP(8, 0)
      STAMP(2);
      UNR for (int tb = 0; tb < 2; ++tb) {
        float d = (i16 & 2) ? ((i16 & 1) ? gQ[tb].w : gQ[tb].z) : ((i16 & 1) ? gQ[tb].y : gQ[tb].x);
        d = (q == (i16 >> 2)) ? d : 0.0f;
        d = sw16add(d, d); d = sw32add(d, d);
        f2 t = ln_stats(gS[tb].x, d);
        mS[tb] = t.x; mQ[tb] = t.y;
      }
    }
    const u32 tag = rfl(tagv) + 1;
    u32 wp = 0;
    main_layer<0>(a, ws, b, w, lane, p, g, rt, c, rot, tag, B, mS, mQ, wp, dead STPASS);
    main_layer<1>(a, ws, b, w, lane, p, g, rt, c, rot, tag, B, mS, mQ, wp, dead STPASS);
  } else {
    const int pr = (w - 4) >> 1, e = w & 1;
    const u32 lH = pr ? L_H1 : L_H0, lU = pr ? L_U1 : L_U0;
    u32 tag;
    if (w == 4) {
      const int cl = b % 27, kr0 = b / 27;
      if (b < 243) {
        rsrc_t rw = mkrs(a.wqkv, 576 * 3456);
        UNR for (int rg = 0; rg < 8; ++rg)
          dma<0>(rw, L_TILE + rg * 1024, ((lane >> 3) ^ (rg & 1)) * 3456 + (((lane & 7) ^ (4 * ((rg >> 1) & 1))) << 4), (64 * kr0 + 8 * rg) * 3456 + cl * 128);
      }
      FENCE();
      SB();
      bar();
      SB();
      __builtin_amdgcn_s_setprio(3);
      if (b < 243) {
        const int cg = lane >> 3, rg = lane & 7, sec = cl / 9;
        u32 dst[8];
        UNR for (int j = 0; j < 8; ++j) {
          int cc = 64 * (cl - 9 * sec) + 8 * cg + j, h = cc / 18, d = cc - 18 * h, hl = h & 1;
          int hi = d >= 9, qq = sec ? 2 * (sec - 1) + hl : 2 * hl + hi, s = sec ? d : d - 9 * hi;
          bool left = s >= (sec ? 16 : 8);
          int row = left ? 64 + 3 * qq + (sec ? s - 16 : 2) : 16 * (s >> 2) + 4 * qq + (s & 3);
          u32 o = (sec == 0 && !left) ? L_WQ + kr0 * 4096 : kr0 * SLOT;
          dst[j] = O_WT + (h >> 1) * WT_G + o + row * 128 + ((rg ^ ((row >> 1) & 7)) << 4);
        }
        SB();
        vmwait0();
        u4 R[8];
        const u32 rb = L_TILE + rg * 1024 + ((cg ^ (4 * ((rg >> 1) & 1))) << 4);
        UNR for (int i = 0; i < 8; ++i) R[i] = ld<u4>(rb + ((i ^ (rg & 1)) << 7));
        FENCE();
        lset(CNT(C_WT), 1);
        u4 o[8];
        UNR for (int j = 0; j < 8; ++j) {
          UNR for (int d = 0; d < 4; ++d) o[j][d] = __builtin_amdgcn_perm(R[2 * d + 1][j >> 1], R[2 * d][j >> 1], (j & 1) ? 0x07060302u : 0x05040100u);
          gst4(o[j], ws, dst[j], 0);
        }
        SB();
        float sel = 0;
        UNR for (int j = 0; j < 8; ++j) {
          float v = dot2(o[j].x, 0x3F803F80u, dot2(o[j].y, 0x3F803F80u, 0.0f)) + dot2(o[j].z, 0x3F803F80u, dot2(o[j].w, 0x3F803F80u, 0.0f));
          v += __uint_as_float(__builtin_amdgcn_update_dpp(0, __float_as_uint(v), 0xB1, 0xF, 0xF, true));
          v += __uint_as_float(__builtin_amdgcn_update_dpp(0, __float_as_uint(v), 0x4E, 0xF, 0xF, true));
          v += __uint_as_float(__builtin_amdgcn_update_dpp(0, __float_as_uint(v), 0x141, 0xF, 0xF, true));
          sel = rg == j ? v : sel;
        }
        gst1(__float_as_uint(sel), ws, O_CS + lane * 4, kr0 * 6912 + cl * 256);
        vmwait0();
      } else lset(CNT(C_WT), 1);
      tag = rfl(tagv) + 1;
      if (b < 243) setflag(ws, O_FW + b * 128, tag, lane);
      STAMP(1);
      {
        int l6 = lane < 54 ? lane : 0, kr = l6 / 6, i = l6 - 6 * kr;
        int ln = 9 * (i >> 1) + ((36 * g + 35 * (i & 1)) >> 6);
        poll(ws, O_FW + (kr * 27 + ln) * 128, tag, dead);
      }
      lset(CNT(C_WOK), 1);
      STAMP(2);
      u32 xp = 0;
      auto need_x = [&](u32 n) {
        if (xp < n) {
          NOUNR for (int i = dead ? SPIN_L : 0; i < SPIN_L; ++i) {
            u4 v;
            asm volatile("ds_read_b128 %0, %1\n\ts_waitcnt lgkmcnt(0)" : "=v"(v) : "v"(CNT(C_PROG)) : "memory");
            xp = rfl(umin(umin(v.x, v.y), umin(v.z, v.w)));
            if (xp >= n) return;
          }
          dead = 1;
        }
      };
      UNR for (int j = 0; j < 9; ++j) {
        need_x(((j + 1) * SLOT - 1) / 16384 + 1);
        w_slot(ws, j, rot, g, lane);
        if (j >= 4) { vmwait<40>(); lset(CNT(C_WPROG), j - 3); }
      }
      need_x(8);
      UNR for (int j = 0; j < 9; ++j) {
        int kr = j + rot; kr -= kr >= 9 ? 9 : 0;
        UNR for (int i = 0; i < 4; ++i) dma<SC1>(ws, L_WQ + j * 4096 + i * 1024, O_WT + L_WQ + lane * 16, g * WT_G + kr * 4096 + i * 1024);
        if (j == 1) { vmwait<38>(); lset(CNT(C_WPROG), 6); }
        if (j == 3) { vmwait<36>(); lset(CNT(C_WPROG), 7); }
        if (j == 6) { vmwait<38>(); lset(CNT(C_WPROG), 8); }
      }
      vmwait<36>(); lset(CNT(C_WPROG), 9);
      vmwait0();
      lset(CNT(C_WPROG), 10);
      __builtin_amdgcn_s_setprio(0);
      STAMP(3);
    } else if (w < 7) {
      const int T = (w - 5) * 128 + lane, chn = 2 * b + (w - 5);
      const u32 mH = w == 5 ? L_H0 : L_H1, mU = w == 5 ? L_U0 : L_U1;
      const u4 sw0 = kv_w(a, T, c), sw1 = kv_w(a, T + 64, c);
      FENCE();
      SB();
      bar();
      SB();
      {
        const int r3 = lane / 18;
        const u32 vo = r3 * 1152 + (lane - 18 * r3) * 16 + 288 * c;
        UNR for (int i = 0; i < 4; ++i) lwait(CNT(C_PROG) + 4 * i, 9, dead);
        if (w == 5) {
          rsrc_t rk = mkrs(a.bk, 2359296);
          if (lane < 54) {
            dma<0>(rk, L_KT, rt ? vo + (32 * rt - 3) * 1152 : 0x80000000u, 0);
            UNR for (int i = 1; i < 11; ++i) dma<0>(rk, L_KT + i * 864, vo, (32 * rt - 3 + 3 * i) * 1152);
          }
          if (lane < 36) dma<0>(rk, L_KT + 11 * 864, vo, (32 * rt + 30) * 1152);
        } else {
          rsrc_t rv = mkrs(a.bv, 2359296);
          if (lane < 54) { UNR for (int i = 0; i < 10; ++i) dma<0>(rv, L_VT + i * 864, vo, (32 * rt + 3 * i) * 1152); }
          if (lane < 36) dma<0>(rv, L_VT + 10 * 864, vo, (32 * rt + 30) * 1152);
        }
      }
      vmwait0();
      ladd(CNT(C_KVL), lane);
      lwait(CNT(C_KVL), 2, dead);
      KVin in0, in1;
      kv_read(T, in0);
      kv_read(T + 64, in1);
      FENCE();
      ladd(CNT(C_KVR), lane);
      kv_compute(ws, T, rt, c, in0, sw0);
      kv_compute(ws, T + 64, rt, c, in1, sw1);
      STAMP(1);
      vmwait0();
      tag = rfl(tagv) + 1;
      if (ladd_rtn(CNT(C_KVC), lane) == 2) setflag(ws, O_FK + b * 128, tag, lane);
      Hin hin;
      h_load(a, chn, lane, hin);
      lwait(CNT(C_KVR), 3, dead);
      h_stage(mH, mU, lane, hin);
      poll(ws, O_FK + wg_of(lane >> 2, 4 * (lane & 3) + (2 * b) / 144) * 128, tag, dead);
      STAMP(2);
      u_stage(ws, mU, chn, lane);
      vmwait0();
      ladd(w == 5 ? CNT(C_PU0) : CNT(C_PU1), lane);
      STAMP(3);
    } else {
      if (lane < 32) lset(L_CNT + lane * 4, 0);
      const int cl = b % 9, gg = b / 9;
      rsrc_t rp = mkrs(a.wphi, 32 * 648);
      u4 p0 = gld4(rp, lane * 16, g * 1296, 0), p1 = gld4(rp, lane < 17 ? 1024 + lane * 16 : 0x80000000u, g * 1296, 0);
      const u4 sw = kv_w(a, 256 + (lane & 31), c);
      FENCE();
      SB();
      bar();
      SB();
      const u4 s4 = {SENT, SENT, SENT, SENT};
      st<u4>(L_PT + lane * 16, p0);
      if (lane < 17) st<u4>(L_PT + 1024 + lane * 16, p1);
      FENCE();
      auto wp16 = [&](int hd, int d, int f) -> u32 { u32 v = ld<u16>(L_PT + (hd * 324 + d * 18 + (f < 18 ? f : 0)) * 2); return f < 18 ? v : 0u; };
      UNR for (int rd = 0; rd < 2; ++rd) {
        int t = rd * 64 + lane, tt = t < 80 ? t : 79, m = tt >> 4, cc = (tt >> 3) & 1, hd = (tt >> 2) & 1, f = 4 * m + (tt & 3);
        u4 v;
        UNR for (int d = 0; d < 4; ++d) v[d] = wp16(hd, 8 * cc + 2 * d, f) | (wp16(hd, 8 * cc + 2 * d + 1, f) << 16);
        st<u4>(L_TK + (tt >> 3) * 144 + (tt & 7) * 16, v);
        int hq = (tt >> 3) & 1, dh = (tt >> 2) & 1;
        UNR for (int d = 0; d < 4; ++d) v[d] = wp16(hq, 9 * dh + 2 * d, f) | (wp16(hq, 9 * dh + 2 * d + 1, f) << 16);
        st<u4>(L_TQ + m * 272 + (tt & 15) * 16, v);
        st<u32>(L_TQ2 + m * 68 + (tt & 15) * 4, wp16(hq, 9 * dh + 8, f));
      }
      {
        int tt = lane < 40 ? lane : 39, m = tt >> 3, hd = (tt >> 2) & 1, f = 4 * m + (tt & 3);
        st<u32>(L_TK2 + m * 36 + (tt & 7) * 4, wp16(hd, 16, f) | (wp16(hd, 17, f) << 16));
        u4 z = {0, 0, 0, 0};
        if (lane < 10) st<u4>(L_TK + lane * 144 + 128, z);
        if (lane < 5) { st<u4>(L_TQ + lane * 272 + 256, z); st<u32>(L_TK2 + lane * 36 + 32, 0u); st<u32>(L_TQ2 + lane * 68 + 64, 0u); }
      }
      FENCE();
      lwait(CNT(C_KVL), 2, dead);
      {
        KVin in;
        if (lane < 32) kv_read(256 + lane, in);
        FENCE();
        ladd(CNT(C_KVR), lane);
        if (lane < 32) kv_compute(ws, 256 + lane, rt, c, in, sw);
      }
      vmwait0();
      tag = rfl(tagv) + 1;
      if (ladd_rtn(CNT(C_KVC), lane) == 2) setflag(ws, O_FK + b * 128, tag, lane);
      lwait(CNT(C_WT), 1, dead);
      if (b < 144) {
        rsrc_t rw = mkrs(a.wo, 576 * 1152);
        UNR for (int m = 0; m < 5; ++m)
          if (m < 4 || lane < 32) dma<0>(rw, L_TILE + m * 1024, (lane >> 3) * 1152 + (lane & 7) * 16, (36 * gg + 8 * m) * 1152 + cl * 128);
      }
      UNR for (int i = 0; i < 3; ++i) gst4(s4, ws, (i < 2 || lane < 48) ? O_SP + i * 1024 + lane * 16 : 0x80000000u, g * SP_G + p * SP_P);
      UNR for (int i = 0; i < 9; ++i) gst4(s4, ws, O_OB + lane * 16, p * OB_P + i * 16384 + g * 1024);
      if (b < 144) {
        vmwait<12>();
        const u32 ta = L_TILE + (18 * (q >> 1) + 8 * (q & 1) + (i16 >> 2)) * 128 + (i16 & 3) * 8;
        UNR for (int G = 0; G < 4; ++G) gst4(trd2(ta + G * 32, ta + G * 32 + 512), ws, O_WOT + lane * 16, (4 * cl + G) * 18432 + gg * 1024);
        u2 lf = trd(L_TILE + (16 + ((i16 >> 2) & 1) + 18 * (i16 >> 3)) * 128 + q * 32 + (i16 & 3) * 8);
        gst2(lf, ws, O_WOT + q * 18432 + i16 * 16, 4 * cl * 18432 + (16 + (gg >> 3)) * 1024 + ((gg >> 1) & 3) * 256 + (gg & 1) * 8);
      }
      FENCE();
      vmwait0();
      setflag(ws, O_FA + b * 128, tag, lane);
      STAMP(1);
      {
        rsrc_t rh = mkrs(a.ssm, 2359296);
        UNR for (int m = 0; m < 4; ++m) dma<0>(rh, L_H2 + 32 + m * 1024, lane * 16, (512 + (b >> 2)) * 4096 + m * 1024);
        if (lane < 32) st<u4>(lane < 2 ? L_H2 + lane * 16 : L_ZERO + (lane - 2) * 16, u4{0, 0, 0, 0});
      }
      lwait(CNT(C_WOK), 1, dead);
      UNR for (int rd = 0; rd < 2; ++rd) {
        int blk = rd * 4 + q, qq = i16 >> 2, ee = i16 & 3;
        int kvc = 576 + 576 * (qq >> 1) + 36 * g + 18 * (qq & 1), qc = 36 * g + 9 * qq;
        int col = rd == 0 ? kvc + 4 * blk + ee : (blk == 4 ? (ee < 2 ? kvc + 16 + ee : qc + 8) : qc + 4 * ((blk - 5) & 1) + ee);
        float s = 0;
        UNR for (int k = 0; k < 9; ++k) s += __uint_as_float(gld1(ws, O_CS + col * 4, k * 6912, 1));
        if (rd == 0 || q < 3) st<float>(L_CSL + rd * 256 + lane * 4, s);
      }
      FENCE();
      ladd(CNT(C_TBL), lane);
      STAMP(2);
      poll(ws, O_FK + wg_of(lane >> 2, 4 * (lane & 3) + 3) * 128, tag, dead);
      u_stage(ws, L_U2, 512 + (b >> 2), lane);
      {
        poll(ws, O_FA + wg_of(lane < 16 ? lane : p, lane < 16 ? g : (lane & 15)) * 128, tag, dead);
        int ln = (144 * c + 48 * q + (q == 3 ? -1 : 0)) >> 6;
        poll(ws, O_FA + (i16 * 9 + ln) * 128, tag, dead);
      }
      lset(CNT(C_FAOK), 1);
      lset(CNT(C_PU2), 1);
      lwait(CNT(C_WPROG), 9, dead);
      gst4(s4, ws, lane < 48 ? O_ST + q * 65536 + i16 * 16 : 0x80000000u, rt * 1024 + c * 256);
      UNR for (int i = 0; i < 9; ++i) {
        u32 o = i * 1024 + lane * 16;
        o += o >= 4608 ? 18432 - 4608 : 0;
        gst4(s4, ws, O_X2 + o, 2 * rt * 18432 + c * 4608);
      }
      UNR for (int i = 0; i < 3; ++i) gst4(s4, ws, (i < 2 || lane < 48) ? O_SP + i * 1024 + lane * 16 : 0x80000000u, SP_L + g * SP_G + p * SP_P);
      UNR for (int i = 0; i < 9; ++i) gst4(s4, ws, O_OB + lane * 16, OB_L + p * OB_P + i * 16384 + g * 1024);
      vmwait0();
      lset(CNT(C_CLR), 1);
      STAMP(3);
    }
    u4 wo[36];
    float dv = load_dv(a, 2 * b + pr), dq = load_dv(a, 512 + (b >> 2));
    asm volatile("" : "+v"(dv), "+v"(dq));
    lwait(CNT(C_WPROG), 10, dead);
    lwait(CNT(C_FAOK), 1, dead);
    lwait(pr ? CNT(C_PU1) : CNT(C_PU0), 1, dead);
    STAMP(4);
    conv_pair(ws, lH, lU, 2 * b + pr, e, pr ? CNT(C_PA1) : CNT(C_PA0), pr ? CNT(C_PB1) : CNT(C_PB0), dv, (9 * c + 2 * (w - 4)) * 18432, wo, lane, dead);
    STAMP(5);
    conv_quarter(ws, 512 + (b >> 2), b & 3, w - 4, dq, lane, dead);
    vmwait0();
    if (ladd_rtn(CNT(C_FYC), lane) == 3) setflag(ws, O_FY + b * 128, tag, lane);
    STAMP(6);
    __builtin_amdgcn_s_setprio(3);
    Keep<true> kp;
    tail<true, 0>(a, ws, w, lane, rt, c, tag, wo, kp, dead STPASS);
    tail<true, 1>(a, ws, w, lane, rt, c, tag, wo, kp, dead STPASS);
    if (b == 0 && w == 7) setflag(ws, 0, tag, lane);
  }
  STAMP(31); STAMPC(30);
}

extern "C" void kernel_launch(void* const* d_in, const int* in_sizes, int n_in, void* d_out, int out_size, void* d_ws, size_t ws_size, hipStream_t stream) {
  Args a;
  a.x = (const u16*)d_in[0]; a.wqkv = (const u16*)d_in[1]; a.wphi = (const u16*)d_in[2]; a.wo = (const u16*)d_in[3];
  a.bk = (const u16*)d_in[5]; a.bv = (const u16*)d_in[6]; a.sk = (const u16*)d_in[7]; a.ssm = (const u16*)d_in[8]; a.dv = (const u16*)d_in[9];
  a.out = (u16*)d_out; a.ws = (unsigned char*)d_ws;
  (void)hipFuncSetAttribute((const void*)fused_kernel, hipFuncAttributeMaxDynamicSharedMemorySize, LDS_BYTES);
  fused_kernel<<<256, 512, LDS_BYTES, stream>>>(a);
}
```

```cpp
#include <hip/hip_runtime.h>
#include <hip/hip_bf16.h>
#include <utility>

typedef unsigned u32;
typedef unsigned short u16;
typedef unsigned long long u64;
typedef float f2 __attribute__((ext_vector_type(2)));
typedef float f4 __attribute__((ext_vector_type(4)));
typedef u32 u2 __attribute__((ext_vector_type(2)));
typedef u32 u4 __attribute__((ext_vector_type(4)));
typedef u32 u2a __attribute__((ext_vector_type(2), aligned(4)));
typedef __bf16 bf2 __attribute__((ext_vector_type(2)));
typedef __bf16 bf8 __attribute__((ext_vector_type(8)));
typedef short s4v __attribute__((ext_vector_type(4)));
typedef __amdgpu_buffer_rsrc_t rsrc_t;
#define LAS __attribute__((address_space(3)))
#define CAS __attribute__((address_space(4)))
#define DEV __device__ __forceinline__
#define UNR _Pragma("unroll")
#define NOUNR _Pragma("unroll 1")
#define FENCE() asm volatile("" ::: "memory")
#define SC1 16

#define WV3 1
constexpr u32 O_FW = 4096, O_FA = O_FW + 32768, O_FK = O_FA + 32768, O_FY = O_FK + 32768, O_FO = O_FY + 32768, O_FB = O_FO + 32768;
constexpr u32 O_WT = 262144, WT_G = 124416;
constexpr u32 O_CS = O_WT + 16 * WT_G;
constexpr u32 O_WOT = O_CS + 62464;
constexpr u32 O_KVT = O_WOT + 663552;
constexpr u32 O_YT = O_KVT + 2359296;
constexpr u32 O_SP = O_YT + 2359296, SP_L = 720896, SP_G = 45056, SP_P = 2816;
constexpr u32 O_OB = O_SP + 2 * SP_L, OB_L = 2359296, OB_P = 147456;
constexpr u32 O_ST = O_OB + 2 * OB_L;
constexpr u32 O_X2 = O_ST + 3 * 65536;
constexpr u32 O_STAMP = O_X2 + 2359296;
constexpr u32 WS_BYTES = O_STAMP + 256 * 8 * 32 * 8;
constexpr u32 SLOT = 9728, L_WQ = 9 * SLOT, L_STG = 5 * SLOT, L_STB = L_STG + 30720;
constexpr u32 L_H0 = 124416, L_H1 = 132736, L_U0 = 141056, L_U1 = 145632, L_H2 = 150528, L_U2 = 154688, L_ZERO = L_U2 + 4096, L_CSL = 159264;
constexpr u32 L_KT = 131072, L_VT = 141152, L_TILE = 150528, L_PT = 158720;
constexpr u32 L_REDA = 124416, L_REDB = 126464, L_Y = 128512, L_KEEP = 139264;
constexpr u32 L_TK = 160256, L_TK2 = 161696, L_TQ = 161888, L_TQ2 = 163248, L_CNT = 163712, LDS_BYTES = 163840;
enum { C_PROG = 0, C_WPROG = 4, C_WOK, C_TBL, C_KVC, C_PHI, C_STB, C_PU0, C_PU1, C_PU2, C_PA0, C_PA1, C_PB0, C_PB1, C_QA, C_QB, C_REDA, C_REDB, C_FYC, C_FAOK, C_GEM, C_PUB, C_KVL, C_KVR, C_WT, C_CLR, C_G2, C_FBOK };
#define CNT(i) (L_CNT + 4 * (i))
constexpr u32 SENT = 0xFFFFFFFFu;
constexpr int SPIN_G = 1 << 17, SPIN_L = 1 << 21;

#define CLRAT 7
#define KVAT 4
#define XLA 5
#define HAT 7
#define FAT 10
#define CLB 2
#define PAT 10
#define PLATE 1
#define KVS 2
#define PF 0
#define CPF 1
#define GR3 0
#define NA 5
#define MAP 1
__device__ __forceinline__ int wg_of(int p, int g) {
  return (p >> 2) * 2 + (g >> 3) + 8 * ((p & 3) * 8 + (g & 7));
}
struct Args {
  const u16 *x, *wqkv, *wphi, *wo, *bk, *bv, *sk, *ssm, *dv;
  u16* out;
  unsigned char* ws;
};

template <int I> struct IC { static constexpr int v = I; };
template <class F, int... I> DEV void sfor_(F&& f, std::integer_sequence<int, I...>) { (f(IC<I>{}), ...); }
template <int N, class F> DEV void sfor(F&& f) { sfor_(f, std::make_integer_sequence<int, N>{}); }
#define INL __attribute__((always_inline))
template <class T> DEV void pin(T& v) { asm volatile("" : "+v"(v)); }
template <class T> DEV void pins(T& v) { asm volatile("" : "+s"(v)); }
DEV int opq(int v) { asm volatile("" : "+v"(v)); return v; }
DEV int lane_id() { u32 m = ~0u; asm volatile("" : "+s"(m)); return __builtin_amdgcn_mbcnt_hi(m, __builtin_amdgcn_mbcnt_lo(m, 0)); }
DEV u32 rfl(u32 v) { return __builtin_amdgcn_readfirstlane(v); }
DEV rsrc_t mkrs(const void* p, u32 n) { return __builtin_amdgcn_make_buffer_rsrc((void*)p, 0, n, 0x00020000); }
DEV u32 pk(float a, float b) { f2 v = {a, b}; return __builtin_bit_cast(u32, __builtin_convertvector(v, bf2)); }
DEV u32 pk(f2 v) { return __builtin_bit_cast(u32, __builtin_convertvector(v, bf2)); }
DEV f2 up(u32 p) { f2 r = {__uint_as_float(p << 16), __uint_as_float(p & 0xffff0000u)}; return r; }
DEV f2 rnd(f2 v) { return up(pk(v)); }
DEV float rnd1(float v) { return __uint_as_float(pk(v, v) & 0xffff0000u); }
DEV f4 mfma(u4 a, u4 b, f4 c) { return __builtin_amdgcn_mfma_f32_16x16x32_bf16(__builtin_bit_cast(bf8, a), __builtin_bit_cast(bf8, b), c, 0, 0, 0); }
DEV u32 umin(u32 a, u32 b) { return a < b ? a : b; }
DEV u32 umax(u32 a, u32 b) { return a > b ? a : b; }
DEV u32 umax3(u32 a, u32 b, u32 c) { return umax(umax(a, b), c); }
DEV u32 san(u32 v) { return umin(v, 0xFFFFFFFEu); }
DEV u4 san(u4 v) { u4 r = {san(v.x), san(v.y), san(v.z), san(v.w)}; return r; }
DEV u32 vm(u32 m, u4 v) { return umax3(umax3(m, v.x, v.y), v.z, v.w); }
DEV bool anyl(bool c) { return __builtin_amdgcn_ballot_w64(c) != 0; }
DEV float dot2(u32 a, u32 b, float c) { return __builtin_amdgcn_fdot2_f32_bf16(__builtin_bit_cast(bf2, a), __builtin_bit_cast(bf2, b), c, false); }
template <class T> DEV T ld(u32 o) { return *(LAS T*)(uintptr_t)o; }
template <class T> DEV void st(u32 o, T v) { *(LAS T*)(uintptr_t)o = v; }
DEV u2 trd(u32 o) { return __builtin_bit_cast(u2, __builtin_amdgcn_ds_read_tr16_b64_v4i16((LAS s4v*)(uintptr_t)o)); }
DEV u4 trd2(u32 o0, u32 o1) { u2 a = trd(o0), b = trd(o1); u4 r = {a.x, a.y, b.x, b.y}; return r; }
DEV u4 gld4(rsrc_t r, u32 v, u32 s, int aux) { return aux ? __builtin_amdgcn_raw_buffer_load_b128(r, v, s, SC1) : __builtin_amdgcn_raw_buffer_load_b128(r, v, s, 0); }
DEV u2 gld2(rsrc_t r, u32 v, u32 s, int aux) { return aux ? __builtin_amdgcn_raw_buffer_load_b64(r, v, s, SC1) : __builtin_amdgcn_raw_buffer_load_b64(r, v, s, 0); }
DEV u32 gld1(rsrc_t r, u32 v, u32 s, int aux) { return aux ? __builtin_amdgcn_raw_buffer_load_b32(r, v, s, SC1) : __builtin_amdgcn_raw_buffer_load_b32(r, v, s, 0); }
DEV void gst4(u4 d, rsrc_t r, u32 v, u32 s) { __builtin_amdgcn_raw_buffer_store_b128(d, r, v + s, 0, SC1); }
DEV void gst2(u2 d, rsrc_t r, u32 v, u32 s) { __builtin_amdgcn_raw_buffer_store_b64(d, r, v, s, SC1); }
DEV void gst1(u32 d, rsrc_t r, u32 v, u32 s) { __builtin_amdgcn_raw_buffer_store_b32(d, r, v, s, SC1); }
template <int AUX> DEV void dma(rsrc_t r, u32 l, u32 v, u32 s) {
#if defined(__HIP_DEVICE_COMPILE__)
  __builtin_amdgcn_raw_ptr_buffer_load_lds(r, (LAS void*)(uintptr_t)l, 16, v, s, 0, AUX);
#endif
}
DEV void vmwait0() { asm volatile("s_waitcnt vmcnt(0)" ::: "memory"); }
template <int N> DEV void vmwait() { asm volatile("s_waitcnt vmcnt(%0)" ::"n"(N) : "memory"); }
DEV void bar() { asm volatile("s_waitcnt lgkmcnt(0)\n\ts_barrier" ::: "memory"); }
DEV void lset(u32 a, u32 v) { asm volatile("ds_write_b32 %0, %1" ::"v"(a), "v"(v) : "memory"); }
DEV void ladd(u32 a, int lane) { if (lane == 0) { u32 one = 1; asm volatile("ds_add_u32 %0, %1" ::"v"(a), "v"(one) : "memory"); } }
DEV u32 ladd_rtn(u32 a, int lane) {
  u32 r = 0;
  if (lane == 0) { u32 one = 1; asm volatile("ds_add_rtn_u32 %0, %1, %2\n\ts_waitcnt lgkmcnt(0)" : "=v"(r) : "v"(a), "v"(one) : "memory"); }
  return rfl(r);
}
DEV u32 lget(u32 a) { u32 r; asm volatile("ds_read_b32 %0, %1\n\ts_waitcnt lgkmcnt(0)" : "=v"(r) : "v"(a) : "memory"); return rfl(r); }
DEV u32 lwait(u32 a, u32 t, int& dead) {
  u32 r = lget(a);
  if (__builtin_expect(r < t, 0)) {
    NOUNR for (int i = dead ? SPIN_L : 0; i < SPIN_L; ++i) { r = lget(a); if (r >= t) return r; __builtin_amdgcn_s_sleep(1); }
    dead = 1;
  }
  return r;
}
DEV void poll(rsrc_t ws, u32 off, u32 tag, int& dead) {
  NOUNR for (int i = dead ? SPIN_G : 0; i < SPIN_G; ++i) {
    FENCE();
    u32 v = gld1(ws, off, 0, 1);
    if (!anyl(v != tag)) { FENCE(); return; }
    __builtin_amdgcn_s_sleep(1);
  }
  dead = 1;
}
DEV void setflag(rsrc_t ws, u32 off, u32 tag, int lane) { if (lane == 0) gst1(tag, ws, off, 0); }
#define SW16(a, b) __builtin_amdgcn_permlane16_swap(a, b, false, false)
#define SW32(a, b) __builtin_amdgcn_permlane32_swap(a, b, false, false)
DEV float sw16add(float a, float b) { auto r = SW16(__float_as_uint(a), __float_as_uint(b)); return __uint_as_float(r[0]) + __uint_as_float(r[1]); }
DEV float sw32add(float a, float b) { auto r = SW32(__float_as_uint(a), __float_as_uint(b)); return __uint_as_float(r[0]) + __uint_as_float(r[1]); }

#define STAMP(i)
#define STAMPC(i)
#define STARG
#define STPASS

#define STAMPD(i)
#define STAMPL(i) STAMP(i + L * 13)
DEV f2 ln_stats(float s, float sq) {
  float mean = s * (1.0f / 576.0f);
  float var = fmaxf(sq * (1.0f / 576.0f) - mean * mean, 0.0f);
  float sd = rnd1(__builtin_amdgcn_sqrtf(rnd1(rnd1(var) + 1e-5f)));
  f2 r = {rnd1(mean), __builtin_amdgcn_rcpf(sd)};
  return r;
}
DEV void stats_from(u4 v, float (&m)[2], float (&rs)[2]) {
  float s = __uint_as_float(v.x) + __uint_as_float(v.z), sq = __uint_as_float(v.y) + __uint_as_float(v.w);
  s = sw16add(s, s); sq = sw16add(sq, sq);
  f2 t = ln_stats(s, sq);
  auto r0 = SW32(__float_as_uint(t.x), __float_as_uint(t.x));
  auto r1 = SW32(__float_as_uint(t.y), __float_as_uint(t.y));
  m[0] = __uint_as_float(r0[0]); m[1] = __uint_as_float(r0[1]); rs[0] = __uint_as_float(r1[0]); rs[1] = __uint_as_float(r1[1]);
}
DEV u4 ld_stats(rsrc_t ws, u32 off) { u2 a = gld2(ws, off, 0, 1), b = gld2(ws, off + 256, 0, 1); return u4{a.x, a.y, b.x, b.y}; }
DEV u4 poll_stats(rsrc_t ws, u32 off, u4 v, int& dead) {
  if (anyl(vm(0, v) == SENT)) {
    NOUNR for (int i = dead ? SPIN_G : 0; i < SPIN_G; ++i) {
      FENCE();
      v = ld_stats(ws, off);
      if (!anyl(vm(0, v) == SENT)) { FENCE(); return v; }
    }
    dead = 1;
  }
  FENCE();
  return v;
}

DEV void pub_stats(rsrc_t ws, float s0, float s1, float q0, float q1, u32 wa, u32 ra, u32 cnt, u32 last, u32 dst) {
  float t = sw16add(sw32add(s0, s1), sw32add(q0, q1));
  u32 n, one = 1;
  u64 ex;
  f4 r[6];
  asm volatile("ds_write_b32 %8, %9\n\ts_mov_b64 %7, exec\n\ts_mov_b64 exec, 1\n\tds_add_rtn_u32 %6, %10, %11\n\ts_mov_b64 exec, %7\n\t"
               "ds_read_b128 %0, %12\n\tds_read_b128 %1, %12 offset:256\n\tds_read_b128 %2, %12 offset:512\n\tds_read_b128 %3, %12 offset:768\n\t"
               "ds_read_b128 %4, %12 offset:1024\n\tds_read_b128 %5, %12 offset:1280\n\ts_waitcnt lgkmcnt(0)"
               : "=&v"(r[0]), "=&v"(r[1]), "=&v"(r[2]), "=&v"(r[3]), "=&v"(r[4]), "=&v"(r[5]), "=&v"(n), "=&s"(ex)
               : "v"(wa), "v"(t), "v"(cnt), "v"(one), "v"(ra)
               : "memory");
  if (rfl(n) == last) gst4(san(__builtin_bit_cast(u4, ((r[0] + r[1]) + (r[2] + r[3])) + (r[4] + r[5]))), ws, dst, 0);
}

struct KVin { u32 k[11], v[8]; };
DEV u4 kv_w(const Args& a, int T, int c) { return gld4(mkrs(a.sk, 4608), (144 * c + 2 * (T >> 2)) * 8, 0, 0); }
DEV void kv_read_asm(int T, KVin& in) {
  const u32 base = (T & 3) * 2304 + (T >> 2) * 4;
  UNR for (int i = 0; i < 11; ++i) asm volatile("ds_read_b32 %0, %1 offset:%2" : "=v"(in.k[i]) : "v"(L_KT + base), "n"(i * 288) : "memory");
  UNR for (int i = 0; i < 8; ++i) asm volatile("ds_read_b32 %0, %1 offset:%2" : "=v"(in.v[i]) : "v"(L_VT + base), "n"(i * 288) : "memory");
  asm volatile("s_waitcnt lgkmcnt(0)"
               : "+v"(in.k[0]), "+v"(in.k[1]), "+v"(in.k[2]), "+v"(in.k[3]), "+v"(in.k[4]), "+v"(in.k[5]), "+v"(in.k[6]), "+v"(in.k[7]), "+v"(in.k[8]),
                 "+v"(in.k[9]), "+v"(in.k[10]), "+v"(in.v[0]), "+v"(in.v[1]), "+v"(in.v[2]), "+v"(in.v[3]), "+v"(in.v[4]), "+v"(in.v[5]), "+v"(in.v[6]),
                 "+v"(in.v[7])
               :
               : "memory");
}
DEV void kv_read(int T, KVin& in) {
  const u32 base = (T & 3) * 2304 + (T >> 2) * 4;
  UNR for (int i = 0; i < 11; ++i) in.k[i] = ld<u32>(L_KT + base + i * 288);
  UNR for (int i = 0; i < 8; ++i) in.v[i] = ld<u32>(L_VT + base + i * 288);
}
#define SB() __builtin_amdgcn_sched_barrier(0)
DEV void add_rnd8(float (&t)[8], float (&a)[8]) {
  asm volatile("v_cvt_pk_bf16_f32 %8, %0, %8\n\tv_cvt_pk_bf16_f32 %9, %1, %9\n\tv_cvt_pk_bf16_f32 %10, %2, %10\n\tv_cvt_pk_bf16_f32 %11, %3, %11\n\t"
               "v_cvt_pk_bf16_f32 %12, %4, %12\n\tv_cvt_pk_bf16_f32 %13, %5, %13\n\tv_cvt_pk_bf16_f32 %14, %6, %14\n\tv_cvt_pk_bf16_f32 %15, %7, %15\n\t"
               "v_dot2_f32_bf16 %0, %8, %16, 0\n\tv_dot2_f32_bf16 %1, %9, %16, 0\n\tv_dot2_f32_bf16 %2, %10, %16, 0\n\tv_dot2_f32_bf16 %3, %11, %16, 0\n\t"
               "v_dot2_f32_bf16 %4, %12, %16, 0\n\tv_dot2_f32_bf16 %5, %13, %16, 0\n\tv_dot2_f32_bf16 %6, %14, %16, 0\n\tv_dot2_f32_bf16 %7, %15, %16, 0\n\ts_nop 2"
               : "+v"(t[0]), "+v"(t[1]), "+v"(t[2]), "+v"(t[3]), "+v"(t[4]), "+v"(t[5]), "+v"(t[6]), "+v"(t[7]), "+v"(a[0]), "+v"(a[1]), "+v"(a[2]),
                 "+v"(a[3]), "+v"(a[4]), "+v"(a[5]), "+v"(a[6]), "+v"(a[7])
               : "s"(0x3F803F80u));
}
DEV void kv_compute(rsrc_t ws, int T, int rt, int c, const KVin& in, u4 w, bool on) {
  const int ch = 144 * c + 2 * (T >> 2), s0 = 32 * rt + 8 * (T & 3);
  f2 w0 = {__uint_as_float(w.x << 16), __uint_as_float(w.z << 16)}, w1 = {__uint_as_float(w.x & 0xffff0000u), __uint_as_float(w.z & 0xffff0000u)};
  f2 w2 = {__uint_as_float(w.y << 16), __uint_as_float(w.w << 16)}, w3 = {__uint_as_float(w.y & 0xffff0000u), __uint_as_float(w.w & 0xffff0000u)};
  f2 kf[11];
  UNR for (int i = 0; i < 11; ++i) kf[i] = up(in.k[i]);
  float t0[8], t1[8], a0[8], a1[8];
  UNR for (int s = 0; s < 8; ++s) { const f2 x = kf[s] * w3, y = kf[s + 1] * w2; t0[s] = x.x; t1[s] = x.y; a0[s] = y.x; a1[s] = y.y; }
  add_rnd8(t0, a0); add_rnd8(t1, a1);
  UNR for (int s = 0; s < 8; ++s) { const f2 y = kf[s + 2] * w1; a0[s] = y.x; a1[s] = y.y; }
  add_rnd8(t0, a0); add_rnd8(t1, a1);
  UNR for (int s = 0; s < 8; ++s) { const f2 y = kf[s + 3] * w0; a0[s] = y.x; a1[s] = y.y; }
  add_rnd8(t0, a0); add_rnd8(t1, a1);
  u32 P[8];
  UNR for (int s = 0; s < 8; ++s) P[s] = pk(t0[s], t1[s]);
  SB();
  UNR for (int s = 0; s < 8; ++s) kf[s] = up(P[s]) * up(in.v[s]);
  SB();
  UNR for (int s = 0; s < 8; ++s) P[s] = pk(kf[s]);
  u4 o0, o1;
  UNR for (int d = 0; d < 4; ++d) {
    o0[d] = __builtin_amdgcn_perm(P[6 - 2 * d], P[7 - 2 * d], 0x05040100u);
    o1[d] = __builtin_amdgcn_perm(P[6 - 2 * d], P[7 - 2 * d], 0x07060302u);
  }
  const u32 off = on ? O_KVT + ch * 4096 + (2040 - s0) * 2 : 0x80000000u;
  gst4(o0, ws, off, 0);
  gst4(o1, ws, off, 4096);
}

struct Hin { u4 h[4]; };
DEV void h_load(const Args& a, int ch, int lane, Hin& in) {
  rsrc_t rh = mkrs(a.ssm, 2359296);
  UNR for (int m = 0; m < 4; ++m) in.h[m] = gld4(rh, ch * 4096 + lane * 16, m * 1024, 0);
}
DEV void h_stage(u32 lH, u32 lU, int lane, const Hin& in) {
  UNR for (int m = 0; m < 4; ++m) {
    const u4 h = in.h[m];
    const u32 nx = __builtin_amdgcn_update_dpp(m < 3 ? rfl(in.h[m < 3 ? m + 1 : m].x) : 0u, h.x, 0x130, 0xF, 0xF, false);
    u4 s = {__builtin_amdgcn_alignbit(h.y, h.x, 16), __builtin_amdgcn_alignbit(h.z, h.y, 16), __builtin_amdgcn_alignbit(h.w, h.z, 16), __builtin_amdgcn_alignbit(nx, h.w, 16)};
    st<u4>(lH + 32 + m * 1024 + lane * 16, h);
    st<u4>(lH + 4160 + 32 + m * 1024 + lane * 16, s);
  }
  u32 h0 = rfl(in.h[0].x) << 16;
  u4 z = {0, 0, 0, lane == 3 ? h0 : 0u};
  if (lane < 4) st<u4>(lH + (lane >> 1) * 4160 + (lane & 1) * 16, z);
  u4 zz = {0, 0, 0, 0};
  if (lane < 30) st<u4>(lU + 4096 + lane * 16, zz);
}
DEV void u_stage(rsrc_t ws, u32 lU, int ch, int lane) {
  UNR for (int m = 0; m < 4; ++m) dma<SC1>(ws, lU + m * 1024, O_KVT + ch * 4096 + lane * 16, m * 1024);
}
DEV float load_dv(const Args& a, int ch) {
  const u32 v = ((const CAS u32*)a.dv)[ch >> 1];
  return __uint_as_float((ch & 1) ? v & 0xffff0000u : v << 16);
}
DEV void late_fill(rsrc_t ws, int h, int p, int g, int rt, int c, int lane) {
  const u4 s4 = {SENT, SENT, SENT, SENT};
  if (h < 2) { UNR for (int i = 0; i < 9; ++i) gst4(s4, ws, O_OB + lane * 16, h * OB_L + p * OB_P + i * 16384 + g * 1024); }
  else if (h == 2) {
    UNR for (int i = 0; i < 9; ++i) {
      u32 o = i * 1024 + lane * 16;
      o += o >= 4608 ? 18432 - 4608 : 0;
      gst4(s4, ws, O_X2 + o, 2 * rt * 18432 + c * 4608);
    }
  } else {
    gst4(s4, ws, lane < 48 ? O_ST + (lane >> 4) * 65536 + (lane & 15) * 16 : 0x80000000u, rt * 1024 + c * 256);
    UNR for (int i = 0; i < 3; ++i) gst4(s4, ws, (i < 2 || lane < 48) ? O_SP + i * 1024 + lane * 16 : 0x80000000u, SP_L + g * SP_G + p * SP_P);
  }
}
DEV u2 y_pack(f4 s, u32 lU, int j, float dv, int q, int i16) {
  u2 uu = ld<u2>(lU + 2 * (2044 - 256 * j - 16 * i16 - 4 * q));
  f2 u32_ = up(uu.x), u10 = up(uu.y);
  f2 g01 = {u10.y * dv, u10.x * dv}, g23 = {u32_.y * dv, u32_.x * dv};
  f2 s01 = {s.x, s.y}, s23 = {s.z, s.w};
  return u2{pk(rnd(s01) + rnd(g01)), pk(rnd(s23) + rnd(g23))};
}
DEV void conv_pair(rsrc_t ws, u32 lH, u32 lU, int h, u32 cB, u32 fboff, u32 tag, f4 (&keep)[4], int lane, int& dead STARG) {
  const int q = lane >> 4, i16 = lane & 15, e = h & 1;
  f4 acc[8];
  UNR for (int i = 0; i < 8; ++i) acc[i] = f4{0, 0, 0, 0};
  const u32 aB = lU + 2 * (2032 - 16 * i16 + 8 * q) - 3584 + 256 * e;
  const u32 aA = lH + ((i16 & 1) ? 0 : 4160) + 4 * (((i16 + 1) >> 1) + 4 * q) + 256 * e;
  const u32 aA4[4] = {aA, aA + 1024, aA + 2048, aA + 3072};
  u4 B[8];
  u2 Al[4], Ah[4];
#define LA(s, t, bb)                                                                                                     \
  asm volatile("ds_read2_b32 %0, %2 offset0:%3 offset1:%4\n\tds_read2_b32 %1, %2 offset0:%5 offset1:%6"                  \
               : "=&v"(Al[s]), "=&v"(Ah[s])                                                                              \
               : "v"(aA4[(t) >> 1]), "n"(16 * (bb) + 128 * ((t) & 1)), "n"(16 * (bb) + 128 * ((t) & 1) + 1),              \
                 "n"(16 * (bb) + 128 * ((t) & 1) + 2), "n"(16 * (bb) + 128 * ((t) & 1) + 3)                               \
               : "memory")
#define LB(v, bb) asm volatile("ds_read_b128 %0, %1 offset:%2" : "=&v"(B[v]) : "v"(aB), "n"(512 * (7 - (v)) + 64 * (bb)) : "memory")
#define WA(n, s) asm volatile("s_waitcnt lgkmcnt(%4)" : "+v"(Al[s]), "+v"(Ah[s]), "+v"(Al[(s) + 1]), "+v"(Ah[(s) + 1]) : "n"(n))
#define WB(n, v) asm volatile("s_waitcnt lgkmcnt(%1)" : "+v"(B[v]) : "n"(n))
#define MM(s, t, v) do { acc[(t) + (v)] = mfma(u4{Al[s].x, Al[s].y, Ah[s].x, Ah[s].y}, B[v], acc[(t) + (v)]); SB(); } while (0)
  LB(7, 0); LB(6, 0); LB(5, 0); LA(0, 0, 0); LA(1, 7, 0); LB(4, 0); LB(3, 0); LB(2, 0); LB(1, 0); LB(0, 0);
  UNR for (int bb = 0; bb < 4; ++bb) {
    const int nb = bb < 3 ? 1 : 0;
    LA(2, 1, bb); LA(3, 6, bb);
    asm volatile("s_waitcnt lgkmcnt(9)" : "+v"(Al[0]), "+v"(Ah[0]), "+v"(Al[1]), "+v"(Ah[1]), "+v"(B[7]), "+v"(B[6]), "+v"(B[5]));
    SB();
    MM(0, 0, 7); if (nb) LB(7, bb + 1);
    MM(0, 0, 6); MM(0, 0, 5);
    WB(8 + nb, 4); MM(0, 0, 4); WB(7 + nb, 3); MM(0, 0, 3); WB(6 + nb, 2); MM(0, 0, 2);
    WB(5 + nb, 1); MM(0, 0, 1); WB(4 + nb, 0); MM(0, 0, 0); MM(1, 7, 0);
    LA(0, 2, bb); LA(1, 5, bb); WA(4 + nb, 2); SB();
    MM(2, 1, 5); MM(2, 1, 6); if (nb) LB(6, bb + 1);
    MM(2, 1, 4); MM(2, 1, 3); MM(2, 1, 2); MM(2, 1, 1); MM(2, 1, 0); MM(3, 6, 1); MM(3, 6, 0);
    LA(2, 3, bb); LA(3, 4, bb); WA(4 + nb, 0); SB();
    MM(0, 2, 5); if (nb) LB(5, bb + 1);
    MM(0, 2, 4); MM(0, 2, 3); MM(0, 2, 2); MM(0, 2, 1); MM(0, 2, 0); MM(1, 5, 2); MM(1, 5, 1); MM(1, 5, 0);
    if (nb) { LA(0, 0, bb + 1); LA(1, 7, bb + 1); }
    WA(5 * nb, 2); SB();
    MM(2, 3, 4); if (nb) LB(4, bb + 1);
    MM(2, 3, 3); MM(2, 3, 2); MM(2, 3, 1); MM(2, 3, 0);
    MM(3, 4, 3); if (nb) LB(3, bb + 1);
    MM(3, 4, 2); if (nb) LB(2, bb + 1);
    MM(3, 4, 1); if (nb) LB(1, bb + 1);
    MM(3, 4, 0); if (nb) LB(0, bb + 1);
    if (bb == CLB) { vmwait0(); if (h == 2) lset(CNT(C_PU1), 2); if (ladd_rtn(CNT(C_CLR), lane) == 3) setflag(ws, fboff, tag, lane); SB(); }
    STAMPD(17 + bb);

  }
  lwait(CNT(C_PUB), 4, dead);
  UNR for (int i = 0; i < 4; ++i) {
    keep[i] = e ? acc[4 + i] : acc[i];
    st<f4>(L_STG + h * 4096 + i * 1024 + lane * 16, e ? acc[i] : acc[4 + i]);
  }
  FENCE();
  ladd(cB, lane);
}
DEV void conv_pair_out(rsrc_t ws, u32 lU, int ch, int h, u32 cB, float dv, const f4 (&keep)[4], int lane, int& dead) {
  const int q = lane >> 4, i16 = lane & 15, e = h & 1;
  lwait(cB, 2, dead);
  u2 Y[4];
  UNR for (int i = 0; i < 4; ++i) Y[i] = y_pack(keep[i] + ld<f4>(L_STG + (h ^ 1) * 4096 + i * 1024 + lane * 16), lU, 4 * e + i, dv, q, i16);
  UNR for (int i = 0; i < 2; ++i) {
    auto r0 = SW16(Y[2 * i].x, Y[2 * i + 1].x);
    auto r1 = SW16(Y[2 * i].y, Y[2 * i + 1].y);
    u4 o = {r0[0], r1[0], r0[1], r1[1]};
    gst4(o, ws, O_YT + ch * 4096 + 2 * (256 * (4 * e + 2 * i + (q & 1)) + 16 * i16 + 8 * (q >> 1)), 0);
  }
}
DEV void conv_quarter(int iq, int h, int lane) {
  const int q = lane >> 4, i16 = lane & 15;
  const u32 aB = L_U2 + 2 * (2032 - 16 * i16 + 8 * q) + 128 * h;
  const u32 aA = L_H2 + 4 * (((i16 + 1) >> 1) + 4 * q) + 128 * h;
  const u32 sh = (i16 & 1) ? 0 : 16;
  f4 m[5];
  UNR for (int i = 0; i < 5; ++i) m[i] = f4{0, 0, 0, 0};
  u2 a0[3][2], a1[3][2];
  u32 a2[3][2];
  u4 bq[3][2];
#define LQ(n)                                                                                                                     \
  {                                                                                                                               \
    const int t_ = (n) <= iq ? (n) : (n)-iq - 1, v_ = (n) <= iq ? iq - (n) : 8 - (n);                                             \
    const u32 pa = aA + 512 * t_, pb = aB - 512 * v_;                                                                             \
    asm volatile("ds_read2_b32 %0, %8 offset0:0 offset1:1\n\tds_read2_b32 %1, %8 offset0:2 offset1:3\n\tds_read_b32 %2, %8 offset:16\n\t" \
                 "ds_read_b128 %3, %9\n\tds_read2_b32 %4, %8 offset0:16 offset1:17\n\tds_read2_b32 %5, %8 offset0:18 offset1:19\n\t"  \
                 "ds_read_b32 %6, %8 offset:80\n\tds_read_b128 %7, %9 offset:64"                                                  \
                 : "=&v"(a0[(n) % 3][0]), "=&v"(a1[(n) % 3][0]), "=&v"(a2[(n) % 3][0]), "=&v"(bq[(n) % 3][0]), "=&v"(a0[(n) % 3][1]), \
                   "=&v"(a1[(n) % 3][1]), "=&v"(a2[(n) % 3][1]), "=&v"(bq[(n) % 3][1])                                            \
                 : "v"(pa), "v"(pb)                                                                                               \
                 : "memory");                                                                                                     \
  }
  LQ(0) LQ(1)
  UNR for (int n = 0; n < 9; ++n) {
    const int s = n % 3;
    asm volatile("s_waitcnt lgkmcnt(%8)" : "+v"(a0[s][0]), "+v"(a1[s][0]), "+v"(a2[s][0]), "+v"(bq[s][0]), "+v"(a0[s][1]), "+v"(a1[s][1]), "+v"(a2[s][1]), "+v"(bq[s][1]) : "n"(n < 8 ? 8 : 0));
    if (n < 7) LQ(n + 2)
    UNR for (int k = 0; k < 2; ++k) {
      u4 A = {__builtin_amdgcn_alignbit(a0[s][k].y, a0[s][k].x, sh), __builtin_amdgcn_alignbit(a1[s][k].x, a0[s][k].y, sh),
              __builtin_amdgcn_alignbit(a1[s][k].y, a1[s][k].x, sh), __builtin_amdgcn_alignbit(a2[s][k], a1[s][k].y, sh)};
      m[n < 4 ? n : 4] = mfma(A, bq[s][k], m[n < 4 ? n : 4]);
    }
    SB();
  }
  f4 s1 = m[0], s2 = m[4];
  UNR for (int n = 1; n < 4; ++n) { if (n <= iq) s1 += m[n]; else s2 += m[n]; }
  st<f4>(L_STG + 16384 + h * 2048 + lane * 16, s1);
  st<f4>(L_STG + 16384 + h * 2048 + 1024 + lane * 16, s2);
  FENCE();
  ladd(CNT(C_QB), lane);
}
DEV void conv_quarter_out(rsrc_t ws, int ch, int iq, int h, float dv, int lane, int& dead) {
  const int q = lane >> 4, i16 = lane & 15;
  lwait(CNT(C_QB), 4, dead);
  f4 s = ld<f4>(L_STG + 16384 + h * 1024 + lane * 16);
  UNR for (int i = 1; i < 4; ++i) s += ld<f4>(L_STG + 16384 + i * 2048 + h * 1024 + lane * 16);
  const int j = h ? 7 - iq : iq;
  u2 Y = y_pack(s, L_U2, j, dv, q, i16);
  auto r0 = SW16(Y.x, Y.x);
  auto r1 = SW16(Y.y, Y.y);
  gst4(u4{r0[0], r1[0], r0[1], r1[1]}, ws, (q & 1) ? 0x80000000u : O_YT + ch * 4096 + 2 * (256 * j + 16 * i16 + 8 * (q >> 1)), 0);
}

template <bool HELP> struct Keep { u2 x[HELP ? 4 : 1], y[HELP ? 4 : 1]; };
template <bool HELP, int L>
DEV void tail(const Args& a, rsrc_t ws, int w, int rt, int c, u32 tag, const u4 (&wo)[HELP ? 36 : 18], Keep<HELP>& kp, int& dead STARG) {
  constexpr int NB = HELP ? 2 : 1, NU = HELP ? 4 : 1;
  const int lane = lane_id(), q = lane >> 4, i16 = lane & 15;
  const int bl0 = HELP ? 2 * (w - 4) : 8, slot = HELP ? w - 4 : 4 + w;
  if (L == 0 && HELP) {
    rsrc_t rx = mkrs(a.x, 2359296);
    const u32 o = (32 * rt + i16) * 1152 + (144 * c + 4 * q) * 2;
    UNR for (int u = 0; u < NU; ++u) kp.x[u] = gld2(rx, o, (bl0 + (u >> 1)) * 32 + (u & 1) * 16 * 1152, 0);
  }
  u32 fyoff = 0, fy = tag;
  if (L == 0) {
    const int chl = 144 * c + 16 * bl0 + (lane & (16 * NB - 1)), j = rt >> 3;
    fyoff = O_FY + (chl < 512 ? chl >> 1 : 4 * (chl - 512) + (j < 4 ? j : 7 - j)) * 128;
    pin(fyoff);
  }
  u32 bo0 = L_STG + (HELP ? 0 : w * 18432) + lane * 16, bo1 = bo0 + 18432;
  u32 rwa = slot * 256 + (16 * (lane >> 5) + i16) * 8 + (q & 1) * 4 + L_REDA, rra = i16 * 16 + L_REDA;
  u32 sdst = lane < 16 ? O_ST + L * 2 * 65536 + rt * 1024 + c * 256 + lane * 16 : 0x80000000u;
  u32 stoff = O_ST + L * 2 * 65536 + rt * 1024 + (q & 1) * 512 + (16 * (q >> 1) + i16) * 8;
  u32 odst;
  {
    const int tb = HELP ? q & 1 : w;
    const bool on = HELP || !(q & 1);
    if (L == 0) odst = on ? O_X2 + tb * 18432 + (q >> 1) * 256 + i16 * 16 + 2 * rt * 18432 + (9 * c + bl0) * 512 : 0x80000000u;
    else odst = on ? (32 * rt + 16 * tb + i16) * 1152 + (q >> 1) * 16 + 288 * c + 32 * bl0 : 0x80000000u;
  }
  pin(bo0); pin(rwa); pin(rra); pin(sdst); pin(stoff); pin(odst);
  if (HELP) pin(bo1);
  bar();
  STAMPL(13);
  if (L == 0) fy = gld1(ws, fyoff, 0, 1);
  f4 acc[NU];
  UNR for (int u = 0; u < NU; ++u) acc[u] = f4{0, 0, 0, 0};
  if (HELP) {
    u4 R[5];
    UNR for (int n = 0; n < 5; ++n) R[n] = ld<u4>(((n & 1) ? bo1 : bo0) + (n >> 1) * 1024);
    SB();
    UNR for (int n = 0; n < 36; ++n) {
      const int ks = n >> 1, tb = n & 1, nn = n + 5;
      acc[tb] = mfma(wo[ks], R[n % 5], acc[tb]);
      acc[NU - 2 + tb] = mfma(wo[(NB - 1) * 18 + ks], R[n % 5], acc[NU - 2 + tb]);
      SB();
      if (nn < 36) R[n % 5] = ld<u4>(((nn & 1) ? bo1 : bo0) + (nn >> 1) * 1024);
      SB();
    }
  } else {
    f4 a2 = {0, 0, 0, 0};
    const u32 bo = bo0;
    u4 R[18];
    UNR for (int ks = 0; ks < 12; ++ks) R[ks] = ld<u4>(bo + ks * 1024);
    SB();
    UNR for (int ks = 0; ks < 18; ks += 2) {
      acc[0] = mfma(wo[ks], R[ks], acc[0]); a2 = mfma(wo[ks + 1], R[ks + 1], a2);
      if (ks < 6) { R[ks + 12] = ld<u4>(bo + (ks + 12) * 1024); R[ks + 13] = ld<u4>(bo + (ks + 13) * 1024); }
      SB();
    }
    acc[0] += a2;
  }
  FENCE();
  if (L == 0) ladd(CNT(C_G2), lane);
  u4 yv[NB];
  if (L == 0) {
    if (anyl(fy != tag)) poll(ws, fyoff, tag, dead);
    FENCE();
    UNR for (int i = 0; i < NB; ++i) yv[i] = gld4(ws, O_YT + (lane >> 2) * 4096 + (lane & 3) * 16, (144 * c + 16 * (bl0 + i)) * 4096 + rt * 64, 1);
  }
  f2 x1[NU][2];
  float S[2] = {0, 0}, Q[2] = {0, 0};
  UNR for (int u = 0; u < NU; ++u) {
    f2 lo = {acc[u].x, acc[u].y}, hi = {acc[u].z, acc[u].w};
    x1[u][0] = rnd(rnd(lo) + up(kp.x[u].x)); x1[u][1] = rnd(rnd(hi) + up(kp.x[u].y));
  }
  if (HELP) {
    UNR for (int tb = 0; tb < 2; ++tb) {
      f2 s = (x1[tb][0] + x1[tb][1]) + (x1[NU - 2 + tb][0] + x1[NU - 2 + tb][1]);
      f2 sq = x1[tb][0] * x1[tb][0] + x1[tb][1] * x1[tb][1] + x1[NU - 2 + tb][0] * x1[NU - 2 + tb][0] + x1[NU - 2 + tb][1] * x1[NU - 2 + tb][1];
      S[tb] = s.x + s.y; Q[tb] = sq.x + sq.y;
    }
  } else {
    f2 s = x1[0][0] + x1[0][1], sq = x1[0][0] * x1[0][0] + x1[0][1] * x1[0][1];
    S[0] = w ? 0.0f : s.x + s.y; S[1] = w ? s.x + s.y : 0.0f; Q[0] = w ? 0.0f : sq.x + sq.y; Q[1] = w ? sq.x + sq.y : 0.0f;
  }
  pub_stats(ws, S[0], S[1], Q[0], Q[1], rwa, rra, CNT(C_REDA), 6 * L + 5, sdst);
  STAMPL(14);
  u4 sv = ld_stats(ws, stoff);
  if (L == 0) {
    const u32 yb = L_Y + (HELP ? bl0 : 8 + w) * 1024;
    UNR for (int i = 0; i < NB; ++i) st<u4>(yb + i * 1024 + lane * 16, yv[i]);
    FENCE();
    const u32 ya = yb + (4 * q + (i16 >> 2)) * 64 + (i16 & 3) * 8;
    UNR for (int u = 0; u < NU; ++u) kp.y[u] = trd(ya + (u >> 1) * 1024 + (HELP ? u & 1 : w) * 32);
  }
  sv = poll_stats(ws, stoff, sv, dead);
  STAMPL(15);
  float m[2], rs[2];
  stats_from(sv, m, rs);
  UNR for (int u = 0; u < NU; ++u) {
    const float mu = HELP ? m[u & 1] : (w ? m[1] : m[0]), ru = HELP ? rs[u & 1] : (w ? rs[1] : rs[0]);
    UNR for (int h = 0; h < 2; ++h) {
      f2 xn = rnd(rnd(x1[u][h] - mu) * ru);
      kp.x[u][h] = pk(x1[u][h] + rnd(xn * up(kp.y[u][h])));
    }
  }
  UNR for (int i = 0; i < NB; ++i) {
    auto r0 = SW16(kp.x[2 * i].x, kp.x[HELP ? 2 * i + 1 : 0].x);
    auto r1 = SW16(kp.x[2 * i].y, kp.x[HELP ? 2 * i + 1 : 0].y);
    const u4 o = {r0[0], r1[0], r0[1], r1[1]};
    if (L == 0) gst4(san(o), ws, odst, i * 512);
    else __builtin_amdgcn_raw_buffer_store_b128(o, mkrs(a.out, 2359296), odst + 32 * i, 0, 0);
  }
  if (L == 0) {
    f2 s[NU], sq[NU];
    UNR for (int u = 0; u < NU; ++u) { f2 lo = up(kp.x[u].x), hi = up(kp.x[u].y); s[u] = lo + hi; sq[u] = lo * lo + hi * hi; }
    if (HELP) {
      UNR for (int tb = 0; tb < 2; ++tb) { f2 t = s[tb] + s[NU - 2 + tb], tq = sq[tb] + sq[NU - 2 + tb]; S[tb] = t.x + t.y; Q[tb] = tq.x + tq.y; }
    } else {
      S[0] = w ? 0.0f : s[0].x + s[0].y; S[1] = w ? s[0].x + s[0].y : 0.0f; Q[0] = w ? 0.0f : sq[0].x + sq[0].y; Q[1] = w ? sq[0].x + sq[0].y : 0.0f;
    }
    pub_stats(ws, S[0], S[1], Q[0], Q[1], rwa + (L_REDB - L_REDA), rra + (L_REDB - L_REDA), CNT(C_REDB), 5, sdst + 65536);
    if (HELP) STAMP(16);
  }
}

DEV void w_slot(rsrc_t ws, int j, int rot, int g, int lane) {
  int kr = j + rot; kr -= kr >= 9 ? 9 : 0;
  UNR for (int i = 0; i < 9; ++i) dma<SC1>(ws, j * SLOT + i * 1024, O_WT + lane * 16, g * WT_G + kr * SLOT + i * 1024);
  if (lane < 32) dma<SC1>(ws, j * SLOT + 9216, O_WT + lane * 16, g * WT_G + kr * SLOT + 9216);
}

template <int L>
DEV void main_layer(const Args& a, rsrc_t ws, int b, int w, int p, int g, int rt, int c, int rot, u32 tag, u4 (&B)[36], float (&mS)[2], float (&mQ)[2], u32& wp, int& dead STARG) {
    constexpr float LOG2E = 1.4426950408889634f;
    int lane = lane_id(), q = lane >> 4, i16 = lane & 15;
    {
      f4 acc[5][2];
      u4 sv = {0, 0, 0, 0};
      auto stoff = [&]() -> u32 { return O_ST + 65536 + (4 * p + w) * 1024 + (q & 1) * 512 + (16 * (q >> 1) + i16) * 8; };
      u32 so[9], xo0 = 0, xo1 = 0, sto = 0;
      auto b_load = [&](int j) INL { UNR for (int i = 0; i < 4; ++i) B[4 * j + i] = gld4(ws, ((i & 1) ? xo1 : xo0) + (i >> 1) * 1024, so[j], 1); };
      auto b_addr = [&]() INL {
        lane = lane_id(); q = lane >> 4; i16 = lane & 15;
        xo0 = O_X2 + lane * 16 + (8 * p + 2 * w) * 18432; xo1 = xo0 + 18432; sto = stoff();
        pin(xo0); pin(xo1); pin(sto);
      };
      if (L == 1) {
        UNR for (int j = 0; j < 9; ++j) { int kr = j + rot; kr -= kr >= 9 ? 9 : 0; so[j] = kr * 2048; pins(so[j]); }
        b_addr();
        u32 G = lane < 36 ? lane : (lane < 40 ? 9 * (lane - 36) + 8 : 0);
        u32 po = O_X2 + (8 * p + 2 * w + (lane >= 36 && lane < 40 ? 1 : 0)) * 18432 + G * 512;
        NOUNR for (int i = dead ? SPIN_G : 0; i < SPIN_G; ++i) {
          FENCE();
          u32 v = gld1(ws, po, 0, 1);
          UNR for (int j = 0; j < PF; ++j) b_load(j);
          if (!anyl(v == SENT)) break;
        }
        FENCE();
        STAMP(16);
      }
      f4 cs[5];
      bool svbad = false;
      for (int pass = 0;; ++pass) {
        if (L == 1) {
          if (pass) { UNR for (int j = 0; j < PF; ++j) b_load(j); }
          UNR for (int j = PF; j < 9; ++j) { b_load(j); if (j == PF) sv = ld_stats(ws, sto); }
          SB();
        }
        lane = lane_id(); q = lane >> 4; i16 = lane & 15;
        UNR for (int i = 0; i < 5; ++i) acc[i][0] = acc[i][1] = f4{0, 0, 0, 0};
        u32 vmx = 0, fl = 0;
        if (L == 0) { wp = lwait(CNT(C_WPROG), 1, dead); STAMP(3); }
        u32 b0[2], b4[2];
        const int r4 = 64 + 3 * (i16 >> 2) + ((i16 & 3) < 2 ? (i16 & 3) : 2);
        UNR for (int u = 0; u < 2; ++u) {
          b0[u] = i16 * 128 + (((4 * u + q) ^ (i16 >> 1)) << 4);
          b4[u] = r4 * 128 + (((4 * u + q) ^ ((r4 >> 1) & 7)) << 4);
        }
        u4 A[NA];
        UNR for (int n = 0; n < NA; ++n) A[n] = ld<u4>(n % 5 < 4 ? b0[n / 5] + (n % 5) * 2048 : b4[n / 5]);
        SB();
        sfor<90>([&](auto ic) INL {
          constexpr int n = decltype(ic)::v, s = n / 5, i = n % 5, m = n + NA, j = m / 10, u = (m / 5) & 1, im = m % 5, jo = j < 5 ? j : j - 5;
          if (L == 0 && n == 5 * KVS) { vmwait0(); ladd(CNT(C_KVC), lane_id()); }
          acc[i][0] = mfma(A[n % NA], B[2 * s], acc[i][0]);
          acc[i][1] = mfma(A[n % NA], B[2 * s + 1], acc[i][1]);
          SB();
          if (m == 50) { UNR for (int k = 0; k < 2; ++k) { b0[k] = opq(b0[k] + 5 * SLOT); b4[k] = opq(b4[k] + 5 * SLOT); } }
          if (m % 10 == 0 && m < 90 && (L == 0 || j == 5)) {
            const u32 need = L ? 11u : (u32)(j + 1);
            if (m - 5 >= NA) wp = umax(wp, rfl(fl));
            if (wp < need) wp = lwait(CNT(C_WPROG), need, dead);
          }
          if (m < 90) A[n % NA] = ld<u4>((im < 4 ? b0[u] + im * 2048 : b4[u]) + jo * SLOT);
          else if (m < 95) {
            if (L == 0 && m == 90) lwait(CNT(C_TBL), 2, dead);
            if (m == 90) q = lane_id() >> 4;
            cs[m >= 90 && m < 95 ? m - 90 : 0] = ld<f4>(L_CSL + (m - 90) * 64 + q * 16);
          }
          if (m % 10 == 5 && m < 80 && (L == 0 || j == 4)) fl = *(volatile LAS u32*)(uintptr_t)CNT(C_WPROG);
          if (L == 1 && (i == 1 || i == 3)) { vmx = vm(vmx, B[2 * s + (i >> 1)]); asm volatile("" : "+v"(vmx)); }
          if (L == 1 && s == 4 && i == 3) { svbad = anyl(vm(0, sv) == SENT); stats_from(sv, mS, mQ); }
          SB();
        });
        if (L == 0 || !anyl(vmx == SENT) || dead) break;
        if (pass >= 20000) { dead = 1; break; }
        FENCE();
        b_addr();
      }
      FENCE();
      lane = lane_id(); q = lane >> 4; i16 = lane & 15;
      ladd(CNT(C_GEM), lane);
      STAMPL(4);
      if (L == 1 && svbad) { sv = poll_stats(ws, stoff(), sv, dead); stats_from(sv, mS, mQ); }
      u32 ql[2];
      {
        const bool val = (i16 >> 2) == q;
        const u32 tk = L_TK + (val ? ((q & 1) * 4 + (i16 & 3)) * 16 : 128), tk2 = L_TK2 + (val ? ((q & 1) * 4 + (i16 & 3)) * 4 : 32);
        u4 t0[5], t1[5];
        u32 t2[5];
        u32 kv[2][9];
        UNR for (int i = 0; i < 5; ++i) {
          f2 c01 = {cs[i].x, cs[i].y}, c23 = {cs[i].z, cs[i].w};
          UNR for (int tb = 0; tb < 2; ++tb) {
            f2 lo = {acc[i][tb].x, acc[i][tb].y}, hi = {acc[i][tb].z, acc[i][tb].w};
            kv[tb][i < 4 ? 2 * i : 8] = pk((lo - c01 * mS[tb]) * mQ[tb]);
            if (i < 4) kv[tb][2 * i + 1] = pk((hi - c23 * mS[tb]) * mQ[tb]);
            else ql[tb] = pk((hi.x - c23.x * mS[tb]) * mQ[tb], 0.0f);
          }
          t0[i] = ld<u4>(tk + i * 288); t1[i] = ld<u4>(tk + i * 288 + 144); t2[i] = ld<u32>(tk2 + i * 36);
          SB();
        }
        u32 K[9], V[9];
        UNR for (int i = 0; i < 9; ++i) { auto t = SW32(kv[0][i], kv[1][i]); K[i] = t[0]; V[i] = t[1]; }
        const u4 k0 = {K[0], K[1], K[2], K[3]}, k1 = {K[4], K[5], K[6], K[7]}, k2 = {K[8], 0, 0, 0};
        f4 P[5];
        SB();
        UNR for (int m = 0; m < 5; ++m) P[m] = mfma(t0[m], k0, f4{0, 0, 0, 0});
        SB();
        UNR for (int m = 0; m < 5; ++m) P[m] = mfma(t1[m], k1, P[m]);
        SB();
        UNR for (int m = 0; m < 5; ++m) P[m] = mfma(u4{t2[m], 0, 0, 0}, k2, P[m]);
        SB();
        const u32 row = L_STG + (32 * w + 16 * (q >> 1) + i16) * 232;
        lwait(CNT(C_GEM), 4 * (L + 1), dead);
        {
          u32 vo = row + 144 + (q & 1) * 40;
          UNR for (int i = 0; i < 4; ++i) st<u2>(vo + 8 * i, u2{V[2 * i], V[2 * i + 1]});
          st<u2>(vo + 32, u2{V[8], 0x00003F80u});
        }
        SB();
        f2 z[9];
        UNR for (int i = 0; i < 9; ++i) z[i] = f2{P[i >> 1][2 * (i & 1)], P[i >> 1][2 * (i & 1) + 1]};
        float mx = fmaxf(z[0].x, z[0].y), mn = fminf(z[0].x, z[0].y);
        UNR for (int i = 1; i < 9; ++i) { mx = fmaxf(fmaxf(mx, z[i].x), z[i].y); mn = fminf(fminf(mn, z[i].x), z[i].y); }
        const float ma = -mx * LOG2E, mb = mn * LOG2E;
        f2 e1[9], e2[9], s1 = {0, 0}, s2 = {0, 0};
        UNR for (int i = 0; i < 9; ++i) {
          f2 t1 = z[i] * LOG2E + ma, t2 = mb - z[i] * LOG2E;
          e1[i] = f2{__builtin_amdgcn_exp2f(t1.x), __builtin_amdgcn_exp2f(t1.y)};
          e2[i] = f2{__builtin_amdgcn_exp2f(t2.x), __builtin_amdgcn_exp2f(t2.y)};
          s1 += e1[i]; s2 += e2[i];
        }
        const float r1 = __builtin_amdgcn_rcpf(s1.x + s1.y), r2 = __builtin_amdgcn_rcpf(s2.x + s2.y);
        u32 fo = row + (q & 1) * 72;
        UNR for (int i = 0; i < 4; ++i) {
          st<u2>(fo + 8 * i, u2{pk(e1[2 * i] * r1), pk(e1[2 * i + 1] * r1)});
          st<u2>(fo + 32 + 8 * i, u2{pk(e2[2 * i] * r2), pk(e2[2 * i + 1] * r2)});
        }
        st<u2>(fo + 64, u2{pk(e1[8] * r1), pk(e2[8] * r2)});
        FENCE();
        ladd(CNT(C_PHI), lane);
        lwait(CNT(C_PHI), 4 * (L + 1), dead);
        STAMPL(5);
        lane = lane_id(); q = lane >> 4; i16 = lane & 15;
        const int hd = w >> 1, db = w & 1;
        const u32 ta = L_STG + (16 * (q & 1) + 4 * (i16 >> 2) + (q >> 1)) * 232 + (i16 & 3) * 8;
        f4 Dd[3] = {f4{0, 0, 0, 0}, f4{0, 0, 0, 0}, f4{0, 0, 0, 0}};
        const u32 tf = ta + 72 * hd, tv = ta + 144 + 40 * hd + 32 * db;
        u4 bv[4], af[4][3];
        UNR for (int ks = 0; ks < 4; ++ks) {
          bv[ks] = trd2(tv + ks * 7424, tv + ks * 7424 + 464);
          UNR for (int fb = 0; fb < 3; ++fb) af[ks][fb] = trd2(tf + ks * 7424 + fb * 32, tf + ks * 7424 + fb * 32 + 464);
          if (ks & 1) SB();
          if (ks == 1) { UNR for (int fb = 0; fb < 3; ++fb) Dd[fb] = mfma(af[0][fb], bv[0], Dd[fb]); SB(); }
        }
        UNR for (int ks = 1; ks < 4; ++ks) UNR for (int fb = 0; fb < 3; ++fb) Dd[fb] = mfma(af[ks][fb], bv[ks], Dd[fb]);
        auto s0 = SW16(pk(Dd[0].x, Dd[0].y), pk(Dd[1].x, Dd[1].y));
        auto s1b = SW16(pk(Dd[0].z, Dd[0].w), pk(Dd[1].z, Dd[1].w));
        u4 o = {s0[0], s1b[0], s0[1], s1b[1]};
        const int d = 16 * db + i16;
        const u32 so = L * SP_L + g * SP_G + p * SP_P;
        if (L == 0) lwait(CNT(C_WOK), 1, dead);
        gst4(san(o), ws, d < 19 ? O_SP + (hd * 19 + d) * 64 + (q & 1) * 32 + (q >> 1) * 16 : 0x80000000u, so);
        gst2(u2{san(pk(Dd[2].x, Dd[2].y)), san(pk(Dd[2].z, Dd[2].w))}, ws, (d < 19 && q == 0) ? O_SP + 2432 + (hd * 19 + d) * 8 : 0x80000000u, so);
      }
      ladd(CNT(C_PUB), lane);
      STAMPL(6);
      u32 fq[2][9];
      u4 v[12];
      u32 off[3];
      auto sp_round = [&]() INL -> bool {
        u64 bad[4];
        UNR for (int pp = 0; pp < 4; ++pp) { bad[pp] = __builtin_amdgcn_ballot_w64(vm(vm(vm(0, v[pp]), v[4 + pp]), v[8 + pp]) == SENT); pins(bad[pp]); }
        if (!((bad[0] | bad[1]) | (bad[2] | bad[3]))) return false;
        UNR for (int pp = 0; pp < 4; ++pp)
          if (bad[pp]) { UNR for (int gi = 0; gi < 3; ++gi) v[4 * gi + pp] = gld4(ws, off[gi], L * SP_L + g * SP_G + 4 * pp * SP_P, 1); }
        return true;
      };
      {
        if (wp < 10) wp = lwait(CNT(C_WPROG), 10, dead);
        f4 aq[2][2];
        UNR for (int i = 0; i < 2; ++i) aq[i][0] = aq[i][1] = f4{0, 0, 0, 0};
        lane = lane_id(); q = lane >> 4; i16 = lane & 15;
        const u32 bq[2] = {(u32)opq(L_WQ + i16 * 128 + ((q ^ (i16 >> 1)) << 4)), (u32)opq(L_WQ + i16 * 128 + (((4 + q) ^ (i16 >> 1)) << 4))};
        const bool val = (i16 >> 3) == (q >> 1);
        const u32 tq = L_TQ + (val ? (q * 4 + (i16 & 3)) * 16 : 256), tq2 = L_TQ2 + (val ? (q * 4 + (i16 & 3)) * 4 : 64);
        u4 A[4], t0[5];
        u32 t1[5];
        f4 c5, c6;
        UNR for (int n = 0; n < 4; ++n) A[n] = ld<u4>(bq[n >> 1] + (n & 1) * 2048);
        SB();
        UNR for (int n = 0; n < 36; ++n) {
          const int s = n >> 1, i = n & 1, sn = s + 2;
          aq[i][0] = mfma(A[n & 3], B[2 * s], aq[i][0]);
          aq[i][1] = mfma(A[n & 3], B[2 * s + 1], aq[i][1]);
          SB();
          if (n < 32) A[n & 3] = ld<u4>(bq[sn & 1] + (sn >> 1) * 4096 + i * 2048);
          if (n == 30) { c5 = ld<f4>(L_CSL + 5 * 64 + q * 16); c6 = ld<f4>(L_CSL + 6 * 64 + q * 16); }
          if (n >= 31) { t0[n - 31] = ld<u4>(tq + (n - 31) * 272); t1[n - 31] = ld<u32>(tq2 + (n - 31) * 68); }
          SB();
        }
        STAMPL(7);
        UNR for (int gi = 0; gi < 3; ++gi) off[gi] = O_SP + q * SP_P + umin(16 * umin(w + 4 * gi, 10) + i16, 170) * 16;
        if (L == 0) lwait(CNT(C_FAOK), 1, dead);
        UNR for (int i = 0; i < 12; ++i) v[4 * (i % 3) + i / 3] = gld4(ws, off[i % 3], L * SP_L + g * SP_G + 4 * (i / 3) * SP_P, 1);
        SB();
        u4 q0[2];
        UNR for (int tb = 0; tb < 2; ++tb) {
          q0[tb].x = pk((f2{aq[0][tb].x, aq[0][tb].y} - f2{c5.x, c5.y} * mS[tb]) * mQ[tb]);
          q0[tb].y = pk((f2{aq[0][tb].z, aq[0][tb].w} - f2{c5.z, c5.w} * mS[tb]) * mQ[tb]);
          q0[tb].z = pk((f2{aq[1][tb].x, aq[1][tb].y} - f2{c6.x, c6.y} * mS[tb]) * mQ[tb]);
          q0[tb].w = pk((f2{aq[1][tb].z, aq[1][tb].w} - f2{c6.z, c6.w} * mS[tb]) * mQ[tb]);
        }
        f4 P[2][5];
        SB();
        UNR for (int tb = 0; tb < 2; ++tb) {
          UNR for (int m = 0; m < 5; ++m) P[tb][m] = mfma(u4{t1[m], 0, 0, 0}, u4{ql[tb], 0, 0, 0}, f4{0, 0, 0, 0});
          SB();
          UNR for (int m = 0; m < 5; ++m) P[tb][m] = mfma(t0[m], q0[tb], P[tb][m]);
          SB();
        }
        const float ls = (q & 1) ? -LOG2E : LOG2E;
        UNR for (int tb = 0; tb < 2; ++tb) {
          if (tb == 0 || GR3) {
            UNR for (int m = 0; m < 5; ++m) { pin(P[0][m]); pin(P[1][m]); }
            FENCE();
            if (tb == 0) STAMPD(18 + 4 * L);
            bool bd = sp_round();
            FENCE();
            if (tb == 0) STAMPD(19 + 4 * L);
          }
          f2 z[9];
          UNR for (int i = 0; i < 9; ++i) z[i] = f2{P[tb][i >> 1][2 * (i & 1)], P[tb][i >> 1][2 * (i & 1) + 1]} * ls;
          float mx = fmaxf(z[0].x, z[0].y);
          UNR for (int i = 1; i < 9; ++i) mx = fmaxf(fmaxf(mx, z[i].x), z[i].y);
          f2 s1 = {0, 0};
          UNR for (int i = 0; i < 9; ++i) { f2 t = z[i] - mx; z[i] = f2{__builtin_amdgcn_exp2f(t.x), __builtin_amdgcn_exp2f(t.y)}; s1 += z[i]; }
          const float r1 = __builtin_amdgcn_rcpf(s1.x + s1.y);
          UNR for (int i = 0; i < 9; ++i) { fq[tb][i] = pk(z[i] * r1); pin(fq[tb][i]); }
        }
      }
      STAMPL(8);
      lane = lane_id(); q = lane >> 4; i16 = lane & 15;
      u32 m01, m2, l01, l2, oo, lo_;
      {
        const bool val = (i16 >> 3) == (q >> 1);
        const int d01 = (q >> 1) * 19 + 8 * ((i16 >> 2) & 1) + (i16 & 3), d2 = (q >> 1) * 19 + 16 + ((i16 & 3) < 2 ? (i16 & 3) : 2);
        m01 = val ? L_STB + d01 * 64 + (q & 1) * 32 : L_ZERO; m2 = val ? L_STB + d2 * 64 + (q & 1) * 32 : L_ZERO;
        l01 = val ? L_STB + 2432 + d01 * 8 + (q & 1) * 4 : L_ZERO; l2 = val ? L_STB + 2432 + d2 * 8 + (q & 1) * 4 : L_ZERO;
        oo = O_OB + lane * 16 + L * OB_L + p * OB_P + 2 * w * 16384 + g * 1024;
        lo_ = (q < 2 && !(i16 & 1)) ? O_OB + 131072 + (16 * q + i16) * 8 + L * OB_L + p * OB_P + g * 1024 + w * 256 : 0x80000000u;
        pin(m01); pin(m2); pin(l01); pin(l2); pin(oo); pin(lo_);
      }
      u32 fb = tag;
      u32 fbo = O_FB + wg_of(p, i16) * 128;
      if (L == 0) pin(fbo);
      {
        u4 sel;
        UNR for (int d = 0; d < 4; ++d) sel[d] = ((i16 & 7) >> 1) == d ? ((i16 & 1) ? 0x3F800000u : 0x00003F80u) : 0u;
        u32 sa[3];
        UNR for (int gi = 0; gi < 3; ++gi) { sa[gi] = L_STB + (16 * umin(w + 4 * gi, 10) + i16) * 16 + q * 8; pin(sa[gi]); }
        pin(sel);
        STAMPD(21 + 5 * L);
        NOUNR for (int it = dead ? SPIN_G - 1 : 0;; ++it) {
          FENCE();
          if (!sp_round()) break;
          if (it >= SPIN_G) { dead = 1; break; }
        }
        FENCE();
        STAMPL(9);
        f4 s[3];
        UNR for (int pp = 0; pp < 4; ++pp) UNR for (int gi = 0; gi < 3; ++gi) s[gi] = mfma(sel, v[4 * gi + pp], pp ? s[gi] : f4{0, 0, 0, 0});
        SB();
        if (q < 2) { UNR for (int gi = 0; gi < 3; ++gi) st<u2>(sa[gi], u2{pk(s[gi].x, s[gi].y), pk(s[gi].z, s[gi].w)}); }
        FENCE();
        ladd(CNT(C_STB), lane);
      }
      if (L == 0) fb = gld1(ws, fbo, 0, 1);
      u32 clr = 4;
      if (L == 0) clr = *(volatile LAS u32*)(uintptr_t)CNT(C_CLR);
      lwait(CNT(C_STB), 4 * (L + 1), dead);
      STAMPL(10);
      {
        f4 O[2][3];
        u4 t0[3], t1[3];
        u32 t2[3];
        UNR for (int k = 0; k < 3; ++k) { const int m = (k + 2) % 3; t0[m] = ld<u4>(m < 2 ? m01 + m * 256 : m2); }
        UNR for (int k = 0; k < 3; ++k) { const int m = (k + 2) % 3; t1[m] = ld<u4>((m < 2 ? m01 + m * 256 : m2) + 16); }
        UNR for (int k = 0; k < 3; ++k) { const int m = (k + 2) % 3; t2[m] = ld<u32>(m < 2 ? l01 + m * 32 : l2); }
        SB();
        UNR for (int k = 0; k < 3; ++k) UNR for (int tb = 0; tb < 2; ++tb) { const int m = (k + 2) % 3; O[tb][m] = mfma(t0[m], u4{fq[tb][0], fq[tb][1], fq[tb][2], fq[tb][3]}, f4{0, 0, 0, 0}); }
        SB();
        UNR for (int k = 0; k < 3; ++k) UNR for (int tb = 0; tb < 2; ++tb) { const int m = (k + 2) % 3; O[tb][m] = mfma(t1[m], u4{fq[tb][4], fq[tb][5], fq[tb][6], fq[tb][7]}, O[tb][m]); }
        SB();
        UNR for (int k = 0; k < 3; ++k) UNR for (int tb = 0; tb < 2; ++tb) { const int m = (k + 2) % 3; O[tb][m] = mfma(u4{t2[m], 0, 0, 0}, u4{fq[tb][8], 0, 0, 0}, O[tb][m]); }
        SB();
        u32 lo[2];
        if (L == 0 && rfl(clr) < 4) lwait(CNT(C_CLR), 4, dead);
        UNR for (int tb = 0; tb < 2; ++tb) {
          const float inv = __builtin_amdgcn_rcpf(O[tb][2].z + 1e-6f);
          u4 o = {pk(f2{O[tb][0].x, O[tb][0].y} * inv), pk(f2{O[tb][0].z, O[tb][0].w} * inv), pk(f2{O[tb][1].x, O[tb][1].y} * inv), pk(f2{O[tb][1].z, O[tb][1].w} * inv)};
          gst4(san(o), ws, oo, tb * 16384);
          lo[tb] = pk(f2{O[tb][2].x, O[tb][2].y} * inv);
        }
        const u32 lv = san((q & 1) ? lo[1] : lo[0]);
        auto hh = SW32(lv, lv);
        const u32 n0 = __builtin_amdgcn_update_dpp(0u, hh[0], 0xB1, 0xF, 0xF, true), n1 = __builtin_amdgcn_update_dpp(0u, hh[1], 0xB1, 0xF, 0xF, true);
        gst4(u4{hh[0], hh[1], n0, n1}, ws, lo_, 0);
        ladd(CNT(C_PUB), lane);
      }
      STAMPL(11);
      lane = lane_id(); q = lane >> 4; i16 = lane & 15;
      u4 wo[18];
      Keep<false> kp;
      if (w < 2) {
        UNR for (int i = 0; i < 18; ++i) wo[i] = gld4(ws, O_WOT + lane * 16, (9 * c + 8) * 18432 + i * 1024, 1);
        if (L == 0) kp.x[0] = gld2(mkrs(a.x, 2359296), (32 * rt + i16) * 1152 + (144 * c + 4 * q) * 2, 256 + w * 16 * 1152, 0);
        else { u4 t = ld<u4>(L_KEEP + w * 1024 + lane * 16); kp.x[0] = u2{t.x, t.y}; kp.y[0] = u2{t.z, t.w}; }
      }
      {
        const u32 pb = L * OB_L + p * OB_P, tbg = 2 * (rt & 3) + (w >> 1);
        if (L == 0 && anyl(fb != tag)) poll(ws, fbo, tag, dead);
        auto o_ld = [&](int i) INL {
          if (i < 8) v[i] = gld4(ws, O_OB + lane * 16, pb + tbg * 16384 + (8 * (w & 1) + i) * 1024, 1);
          else {
            u32 o = O_OB + 131072 + q * 2048 + i16 * 8, so = pb + (w & 1) * 8192 + tbg * 128;
            u2 l0 = gld2(ws, o, so, 1), l1 = gld2(ws, o + 1024, so, 1);
            v[8] = u4{l0.x, l0.y, l1.x, l1.y};
          }
        };
        FENCE();
        UNR for (int i = 0; i < 9; ++i) o_ld(i);
        NOUNR for (int it = dead ? SPIN_G - 1 : 0;; ++it) {
          FENCE();
          u64 bad[9], any = 0;
          UNR for (int i = 0; i < 9; ++i) { bad[i] = __builtin_amdgcn_ballot_w64(vm(0, v[i]) == SENT); pins(bad[i]); any |= bad[i]; }
          if (!any) break;
          UNR for (int i = 0; i < 9; ++i) if (bad[i]) o_ld(i);
          if (it >= SPIN_G) { dead = 1; break; }
        }
        FENCE();
        STAMPL(12);
        lwait(CNT(C_PUB), 8 * L + 8, dead);
        if (L == 0) lwait(CNT(C_QA), 4, dead);
        const u32 sb = L_STG + (w >> 1) * 18432 + lane * 16;
        UNR for (int i = 0; i < 8; ++i) st<u4>(sb + (8 * (w & 1) + i) * 1024, v[i]);
        st<u4>(sb + (16 + (w & 1)) * 1024, v[8]);
      }
      if (w >= 2) { bar(); STAMPL(13); }
      if (w < 2) {
        tail<false, L>(a, ws, w, rt, c, tag, wo, kp, dead STPASS);
        if (L == 0) st<u4>(L_KEEP + w * 1024 + lane_id() * 16, u4{kp.x[0].x, kp.x[0].y, kp.y[0].x, kp.y[0].y});
      }
      else if (L == 0 && w == 2) {
        lwait(CNT(C_G2), 6, dead);
        UNR for (int j = 5; j < 9; ++j) w_slot(ws, j, rot, g, lane);
        vmwait0();
        lset(CNT(C_WPROG), 11);
      }
    }
}

__global__ void __launch_bounds__(512, 2) fused_kernel(Args a) {
  const int tid = threadIdx.x, lane = tid & 63, w = rfl(tid >> 6), b = blockIdx.x;
  const int q = lane >> 4, i16 = lane & 15;
  const int xcd = b & 7, r = b >> 3;
  const int p = 4 * (xcd >> 1) + (r >> 3), g = 8 * (xcd & 1) + (r & 7), rot = ((r & 7) + 2 * (r >> 3)) % 9;
  const int rt = 4 * p + (g >> 2), c = g & 3;
  rsrc_t ws = mkrs(a.ws, WS_BYTES);
  int dead = 0;
  STAMP(0); STAMPC(29);
  u64 pc0;
  u32 clen;
  asm volatile("s_getpc_b64 %0\n.Lpf_here:\n\ts_mov_b32 %1, fused_code_end-.Lpf_here" : "=s"(pc0), "=s"(clen));
  clen = clen < 131072 ? clen : 0;
  u32 tag = *(const CAS u32*)a.ws;
#define BAR0() do { bar(); asm volatile("" : "+s"(tag)); tag += 1; } while (0)

  if (w < 4) {
    __builtin_amdgcn_s_setprio(2);
    u4 B[36];
    float mS[2], mQ[2];
    {
      rsrc_t rx = mkrs(a.x, 2359296);
      u32 vo[2];
      UNR for (int par = 0; par < 2; ++par) vo[par] = (8 * par + (lane >> 3)) * 1152 + (((lane & 7) ^ (4 * par + (lane >> 4))) << 4);
      auto x_step = [&](int j) {
        int kr = j + rot; kr -= kr >= 9 ? 9 : 0;
        UNR for (int rg = 0; rg < 4; ++rg) dma<0>(rx, (j & 7) * 16384 + w * 4096 + rg * 1024, vo[rg & 1], (128 * p + 32 * w + 16 * (rg >> 1)) * 1152 + kr * 128);
      };
      const u4 sw = kv_w(a, tid, c);
      SB();
      STAMPD(26);
      BAR0();
      STAMPD(27);
      SB();
      UNR for (int j = 0; j < XLA; ++j) x_step(j);
      FENCE();
      SB();
      STAMP(1);
      u32 a0 = w * 4096 + (i16 >> 3) * 1024 + (i16 & 7) * 128 + ((q ^ (i16 >> 1)) << 4), a1 = a0 ^ 64, pa = CNT(C_PROG) + 4 * w;
      f4 gS[2] = {f4{0, 0, 0, 0}, f4{0, 0, 0, 0}}, gQ[2] = {f4{0, 0, 0, 0}, f4{0, 0, 0, 0}};
      const u4 ones = {0x3F803F80u, 0x3F803F80u, 0x3F803F80u, 0x3F803F80u};
#define X0STEP(j)                                                                                                                      \
  {                                                                                                                                    \
    u32 pv = j + 1;                                                                                                                    \
    asm volatile("s_waitcnt vmcnt(%8)\n\tds_read_b128 %0, %4\n\tds_read_b128 %1, %4 offset:2048\n\tds_read_b128 %2, %5\n\t"            \
                 "ds_read_b128 %3, %5 offset:2048\n\ts_waitcnt lgkmcnt(0)\n\tds_write_b32 %6, %7"                                       \
                 : "=&v"(B[4 * j]), "=&v"(B[4 * j + 1]), "=&v"(B[4 * j + 2]), "=&v"(B[4 * j + 3])                                        \
                 : "v"(a0 + (j & 7) * 16384), "v"(a1 + (j & 7) * 16384), "v"(pa), "v"(pv), "n"(4 * ((j + XLA - 1 < 8 ? j + XLA - 1 : 8) - j))                                                 \
                 : "memory");                                                                                                          \
    if (j + XLA < 9) { x_step(j + XLA); FENCE(); }                                                                                                \
    UNR for (int i = 0; i < 4; ++i) { gS[i & 1] = mfma(ones, B[4 * j + i], gS[i & 1]); gQ[i & 1] = mfma(B[4 * j + i], B[4 * j + i], gQ[i & 1]); } \
  }
      X0STEP(0) X0STEP(1) X0STEP(2) X0STEP(3) X0STEP(4) X0STEP(5) X0STEP(6) X0STEP(7) X0STEP(8)
      STAMP(2);
      UNR for (int tb = 0; tb < 2; ++tb) {
        float d = (i16 & 2) ? ((i16 & 1) ? gQ[tb].w : gQ[tb].z) : ((i16 & 1) ? gQ[tb].y : gQ[tb].x);
        d = (q == (i16 >> 2)) ? d : 0.0f;
        d = sw16add(d, d); d = sw32add(d, d);
        f2 t = ln_stats(gS[tb].x, d);
        mS[tb] = t.x; mQ[tb] = t.y;
      }
      lwait(CNT(C_KVL), 2, dead);
      STAMPD(28);
      KVin in;
      kv_read_asm(tid, in);
      ladd(CNT(C_KVR), lane);
      kv_compute(ws, tid, rt, c, in, sw, true);
    }
    u32 wp = 0;
    main_layer<0>(a, ws, b, w, p, g, rt, c, rot, tag, B, mS, mQ, wp, dead STPASS);
    main_layer<1>(a, ws, b, w, p, g, rt, c, rot, tag, B, mS, mQ, wp, dead STPASS);
  } else {
    const int pr = (w - 4) >> 1, e = w & 1;
    const u32 lH = pr ? L_H1 : L_H0, lU = pr ? L_U1 : L_U0;
    if (w == 4) {
      const int cl = b % 27, kr0 = b / 27;
      if (b < 243) {
        rsrc_t rw = mkrs(a.wqkv, 576 * 3456);
        UNR for (int rg = 0; rg < 8; ++rg)
          dma<0>(rw, L_TILE + rg * 1024, ((lane >> 3) ^ (rg & 1)) * 3456 + (((lane & 7) ^ (4 * ((rg >> 1) & 1))) << 4), (64 * kr0 + 8 * rg) * 3456 + cl * 128);
      }
      FENCE();
      SB();
      STAMPD(26);
      BAR0();
      STAMPD(27);
      SB();
      __builtin_amdgcn_s_setprio(3);
      if (b < 243) {
        const int cg = lane >> 3, rg = lane & 7, sec = cl / 9;
        u32 dst[8];
        UNR for (int j = 0; j < 8; ++j) {
          int cc = 64 * (cl - 9 * sec) + 8 * cg + j, h = cc / 18, d = cc - 18 * h, hl = h & 1;
          int hi = d >= 9, qq = sec ? 2 * (sec - 1) + hl : 2 * hl + hi, s = sec ? d : d - 9 * hi;
          bool left = s >= (sec ? 16 : 8);
          int row = left ? 64 + 3 * qq + (sec ? s - 16 : 2) : 16 * (s >> 2) + 4 * qq + (s & 3);
          u32 o = (sec == 0 && !left) ? L_WQ + kr0 * 4096 : kr0 * SLOT;
          dst[j] = O_WT + (h >> 1) * WT_G + o + row * 128 + ((rg ^ ((row >> 1) & 7)) << 4);
        }
        SB();
        vmwait0();
        STAMPD(28);
        u4 R[8];
        const u32 rb = L_TILE + rg * 1024 + ((cg ^ (4 * ((rg >> 1) & 1))) << 4);
        UNR for (int i = 0; i < 8; ++i) R[i] = ld<u4>(rb + ((i ^ (rg & 1)) << 7));
        FENCE();
        lset(CNT(C_WT), 1);
        u4 o[8];
        UNR for (int j = 0; j < 8; ++j) {
          UNR for (int d = 0; d < 4; ++d) o[j][d] = __builtin_amdgcn_perm(R[2 * d + 1][j >> 1], R[2 * d][j >> 1], (j & 1) ? 0x07060302u : 0x05040100u);
          gst4(o[j], ws, dst[j], 0);
        }
        SB();
        float sel = 0;
        UNR for (int j = 0; j < 8; ++j) {
          float v = dot2(o[j].x, 0x3F803F80u, dot2(o[j].y, 0x3F803F80u, 0.0f)) + dot2(o[j].z, 0x3F803F80u, dot2(o[j].w, 0x3F803F80u, 0.0f));
          v += __uint_as_float(__builtin_amdgcn_update_dpp(0, __float_as_uint(v), 0xB1, 0xF, 0xF, true));
          v += __uint_as_float(__builtin_amdgcn_update_dpp(0, __float_as_uint(v), 0x4E, 0xF, 0xF, true));
          v += __uint_as_float(__builtin_amdgcn_update_dpp(0, __float_as_uint(v), 0x141, 0xF, 0xF, true));
          sel = rg == j ? v : sel;
        }
        gst1(__float_as_uint(sel), ws, O_CS + lane * 4, kr0 * 6912 + cl * 256);
        vmwait0();
      } else lset(CNT(C_WT), 1);
      if (b < 243) setflag(ws, O_FW + (cl * 9 + kr0) * 128, tag, lane);
      STAMP(1);
      auto need_x = [&]() {
      NOUNR for (int i = dead ? SPIN_L : 0; i < SPIN_L; ++i) {
        u4 v;
        asm volatile("ds_read_b128 %0, %1\n\ts_waitcnt lgkmcnt(0)" : "=v"(v) : "v"(CNT(C_PROG)) : "memory");
        if (rfl(umin(umin(v.x, v.y), umin(v.z, v.w))) >= 9) break;
        if (i == SPIN_L - 1) dead = 1;
        __builtin_amdgcn_s_sleep(1);
      }
      };
      if (PLATE) need_x();
      {
        int l6 = lane < 54 ? lane : 0, kr = l6 / 6, i = l6 - 6 * kr;
        int ln = 9 * (i >> 1) + ((36 * g + 35 * (i & 1)) >> 6);
        poll(ws, O_FW + (ln * 9 + kr) * 128, tag, dead);
      }
      STAMP(2);
      u32 cv[2][9];
      UNR for (int rd = 0; rd < 2; ++rd) {
        int blk = rd * 4 + q, qq = i16 >> 2, ee = i16 & 3;
        int kvc = 576 + 576 * (qq >> 1) + 36 * g + 18 * (qq & 1), qc = 36 * g + 9 * qq;
        int col = rd == 0 ? kvc + 4 * blk + ee : (blk == 4 ? (ee < 2 ? kvc + 16 + ee : qc + 8) : qc + 4 * ((blk - 5) & 1) + ee);
        UNR for (int k = 0; k < 9; ++k) asm volatile("buffer_load_dword %0, %1, %2, %3 offen sc1" : "=v"(cv[rd][k]) : "v"(O_CS + col * 4), "s"(ws), "s"(k * 6912) : "memory");
      }
      if (!PLATE) need_x();
      UNR for (int j = 0; j < 9; ++j) {
        w_slot(ws, j, rot, g, lane);
        if (j == 3) {
          asm volatile("s_waitcnt vmcnt(40)"
                       : "+v"(cv[0][0]), "+v"(cv[0][1]), "+v"(cv[0][2]), "+v"(cv[0][3]), "+v"(cv[0][4]), "+v"(cv[0][5]), "+v"(cv[0][6]), "+v"(cv[0][7]), "+v"(cv[0][8]),
                         "+v"(cv[1][0]), "+v"(cv[1][1]), "+v"(cv[1][2]), "+v"(cv[1][3]), "+v"(cv[1][4]), "+v"(cv[1][5]), "+v"(cv[1][6]), "+v"(cv[1][7]), "+v"(cv[1][8])
                       :
                       : "memory");
          lwait(CNT(C_TBL), 1, dead);
          UNR for (int rd = 0; rd < 2; ++rd) {
            float s = 0;
            UNR for (int k = 0; k < 9; ++k) s += __uint_as_float(cv[rd][k]);
            lset((rd == 0 || q < 3) ? L_CSL + rd * 256 + lane * 4 : CNT(31), __float_as_uint(s));
          }
          lset(CNT(C_TBL), 2);
        }
        if (j >= 4) { vmwait<40>(); lset(CNT(C_WPROG), j - 3); if (j == 4) STAMP(8); if (j == 6) STAMP(9); if (j == 8) STAMP(10); }
        if (j == 3) STAMP(7);
      }
      UNR for (int j = 0; j < 9; ++j) {
        int kr = j + rot; kr -= kr >= 9 ? 9 : 0;
        UNR for (int i = 0; i < 4; ++i) dma<SC1>(ws, L_WQ + j * 4096 + i * 1024, O_WT + L_WQ + lane * 16, g * WT_G + kr * 4096 + i * 1024);
        if (j == 1) { vmwait<38>(); lset(CNT(C_WPROG), 6); }
        if (j == 3) { vmwait<36>(); lset(CNT(C_WPROG), 7); }
        if (j == 6) { vmwait<38>(); lset(CNT(C_WPROG), 8); }
      }
      vmwait<36>(); lset(CNT(C_WPROG), 9);
      STAMP(12);
      vmwait0();
      lset(CNT(C_WPROG), 10);
      __builtin_amdgcn_s_setprio(0);
      STAMP(3);
    } else if (w < 7) {
      const int chn = 2 * b + (w - 5);
      const u32 mH = w == 5 ? L_H0 : L_H1, mU = w == 5 ? L_U0 : L_U1;
      auto kv_dma = [&]() {
        const int r3 = lane / 18;
        const u32 vo = r3 * 1152 + (lane - 18 * r3) * 16 + 288 * c;
        if (w == 5) {
          rsrc_t rk = mkrs(a.bk, 2359296);
          if (lane < 54) {
            dma<0>(rk, L_KT, rt ? vo + (32 * rt - 3) * 1152 : 0x80000000u, 0);
            UNR for (int i = 1; i < 11; ++i) dma<0>(rk, L_KT + i * 864, vo, (32 * rt - 3 + 3 * i) * 1152);
          }
          if (lane < 36) dma<0>(rk, L_KT + 11 * 864, vo, (32 * rt + 30) * 1152);
        } else {
          rsrc_t rv = mkrs(a.bv, 2359296);
          if (lane < 54) { UNR for (int i = 0; i < 10; ++i) dma<0>(rv, L_VT + i * 864, vo, (32 * rt + 3 * i) * 1152); }
          if (lane < 36) dma<0>(rv, L_VT + 10 * 864, vo, (32 * rt + 30) * 1152);
        }
      };
      u32 cpf = gld1(mkrs((const void*)pc0, clen), (((b >> 3) % 5) * 2 + (w - 5)) * 8192 + lane * 128, 0, 0);
      FENCE();
      SB();
      STAMPD(26);
      BAR0();
      STAMPD(27);
      SB();
      lwait(CNT(C_PROG) + 4 * (w - 5), KVAT, dead);
      kv_dma();
      STAMP(9);
      vmwait0();
      pin(cpf);
      ladd(CNT(C_KVL), lane);
      STAMP(10);
      lwait(CNT(C_WPROG), HAT, dead);
      Hin hin;
      h_load(a, chn, lane, hin);
      FENCE();
      if (w == 5) {
        lwait(CNT(C_KVC), 5, dead);
        setflag(ws, O_FK + (c * 64 + rt) * 128, tag, lane);
        STAMP(7);
      }
      lwait(CNT(C_KVR), 5, dead);
      h_stage(mH, mU, lane, hin);
      FENCE();
      ladd(CNT(C_PU0), lane);
      STAMP(8);
      if (w == 6) {
        lwait(CNT(C_WPROG), PAT, dead);
        const u32 f0 = O_FK + ((2 * b) / 144 * 64 + lane) * 128, f1 = O_FK + (192 + lane) * 128;
        const u32 v0 = gld1(ws, f0, 0, 1), v1 = gld1(ws, f1, 0, 1);
        if (anyl(v0 != tag)) poll(ws, f0, tag, dead);
        FENCE();
        STAMP(2);
        u_stage(ws, L_U0, 2 * b, lane);
        u_stage(ws, L_U1, 2 * b + 1, lane);
        if (anyl(v1 != tag)) { poll(ws, f1, tag, dead); }
        FENCE();
        lwait(CNT(C_PU0), 3, dead);
        u_stage(ws, L_U2, 512 + (b >> 2), lane);
        vmwait<4>();
        lset(CNT(C_PU1), 1);
        STAMP(3);
      }
    } else {
      if (lane < 32) lset(L_CNT + lane * 4, 0);
      const int cl = b % 9, gg = b / 9;
      rsrc_t rp = mkrs(a.wphi, 32 * 648);
      u4 p0 = gld4(rp, lane * 16, g * 1296, 0), p1 = gld4(rp, lane < 17 ? 1024 + lane * 16 : 0x80000000u, g * 1296, 0);
      const u4 sw = kv_w(a, 256 + (lane & 31), c);
      const u4 s4 = {SENT, SENT, SENT, SENT};
      UNR for (int i = 0; i < 3; ++i) gst4(s4, ws, (i < 2 || lane < 48) ? O_SP + i * 1024 + lane * 16 : 0x80000000u, g * SP_G + p * SP_P);
      FENCE();
      SB();
      STAMPD(26);
      BAR0();
      STAMPD(27);
      SB();
      st<u4>(L_PT + lane * 16, p0);
      if (lane < 17) st<u4>(L_PT + 1024 + lane * 16, p1);
      FENCE();
      auto wp16 = [&](int hd, int d, int f) -> u32 { u32 v = ld<u16>(L_PT + (hd * 324 + d * 18 + (f < 18 ? f : 0)) * 2); return f < 18 ? v : 0u; };
      UNR for (int rd = 0; rd < 2; ++rd) {
        int t = rd * 64 + lane, tt = t < 80 ? t : 79, m = tt >> 4, cc = (tt >> 3) & 1, hd = (tt >> 2) & 1, f = 4 * m + (tt & 3);
        u4 v;
        UNR for (int d = 0; d < 4; ++d) v[d] = wp16(hd, 8 * cc + 2 * d, f) | (wp16(hd, 8 * cc + 2 * d + 1, f) << 16);
        st<u4>(L_TK + (tt >> 3) * 144 + (tt & 7) * 16, v);
        int hq = (tt >> 3) & 1, dh = (tt >> 2) & 1;
        UNR for (int d = 0; d < 4; ++d) v[d] = wp16(hq, 9 * dh + 2 * d, f) | (wp16(hq, 9 * dh + 2 * d + 1, f) << 16);
        st<u4>(L_TQ + m * 272 + (tt & 15) * 16, v);
        st<u32>(L_TQ2 + m * 68 + (tt & 15) * 4, wp16(hq, 9 * dh + 8, f));
      }
      {
        int tt = lane < 40 ? lane : 39, m = tt >> 3, hd = (tt >> 2) & 1, f = 4 * m + (tt & 3);
        st<u32>(L_TK2 + m * 36 + (tt & 7) * 4, wp16(hd, 16, f) | (wp16(hd, 17, f) << 16));
        u4 z = {0, 0, 0, 0};
        if (lane < 10) st<u4>(L_TK + lane * 144 + 128, z);
        if (lane < 5) { st<u4>(L_TQ + lane * 272 + 256, z); st<u32>(L_TK2 + lane * 36 + 32, 0u); st<u32>(L_TQ2 + lane * 68 + 64, 0u); }
      }
      FENCE();
      lset(CNT(C_TBL), 1);
      vmwait0();
      lset(CNT(C_WOK), 1);
      setflag(ws, O_FA + b * 128, tag, lane);
      STAMP(7);
      lwait(CNT(C_WT), 1, dead);
      lwait(CNT(C_PROG) + 8, KVAT, dead);
      if (b < 144) {
        rsrc_t rw = mkrs(a.wo, 576 * 1152);
        UNR for (int m = 0; m < 5; ++m)
          if (m < 4 || lane < 32) dma<0>(rw, L_TILE + m * 1024, (lane >> 3) * 1152 + (lane & 7) * 16, (36 * gg + 8 * m) * 1152 + cl * 128);
      }
      vmwait0();
      lwait(CNT(C_KVL), 2, dead);
      {
        KVin in;
        kv_read(256 + (lane & 31), in);
        FENCE();
        ladd(CNT(C_KVR), lane);
        kv_compute(ws, 256 + (lane & 31), rt, c, in, sw, lane < 32);
      }
      if (b < 144) {
        const u32 ta = L_TILE + (18 * (q >> 1) + 8 * (q & 1) + (i16 >> 2)) * 128 + (i16 & 3) * 8;
        UNR for (int G = 0; G < 4; ++G) gst4(trd2(ta + G * 32, ta + G * 32 + 512), ws, O_WOT + lane * 16, (4 * cl + G) * 18432 + gg * 1024);
        u2 lf = trd(L_TILE + (16 + ((i16 >> 2) & 1) + 18 * (i16 >> 3)) * 128 + q * 32 + (i16 & 3) * 8);
        gst2(lf, ws, O_WOT + q * 18432 + i16 * 16, 4 * cl * 18432 + (16 + (gg >> 3)) * 1024 + ((gg >> 1) & 3) * 256 + (gg & 1) * 8);
      }
      FENCE();
      vmwait0();
      ladd(CNT(C_KVC), lane);
      setflag(ws, O_FO + b * 128, tag, lane);
      STAMP(8);
      if (lane < 32) st<u4>(lane < 2 ? L_H2 + lane * 16 : L_ZERO + (lane - 2) * 16, u4{0, 0, 0, 0});
      FENCE();
      lwait(CNT(C_WPROG), HAT, dead);
      UNR for (int m = 0; m < 4; ++m) dma<0>(mkrs(a.ssm, 2359296), L_H2 + 32 + m * 1024, lane * 16, (512 + (b >> 2)) * 4096 + m * 1024);
      lwait(CNT(C_WPROG), FAT, dead);
      poll(ws, lane < 16 ? O_FA + wg_of(lane, g) * 128 : O_FO + (i16 * 9 + 2 * c + q - 1) * 128, tag, dead);
      lset(CNT(C_FAOK), 1);
      ladd(CNT(C_PU0), lane);
      STAMP(3);
    }
    u4 wo[36];
    auto wo_load = [&]() { UNR for (int i = 0; i < 36; ++i) wo[i] = gld4(ws, O_WOT + lane * 16, (9 * c + 2 * (w - 4)) * 18432 + i * 1024, 1); };
    lwait(CNT(C_FAOK), 1, dead);
    if (w != 6) wo_load();
    FENCE();
    const float dv = load_dv(a, 2 * b + pr), dq = load_dv(a, 512 + (b >> 2));
    STAMP(11);
    lwait(CNT(C_PU0), 3, dead);
    lwait(CNT(C_PU1), 1, dead);
    STAMP(4);
    late_fill(ws, w - 4, p, g, rt, c, lane);
    FENCE();
    {
      f4 keep[4];
      conv_pair(ws, lH, lU, w - 4, pr ? CNT(C_PB1) : CNT(C_PB0), O_FB + b * 128, tag, keep, lane, dead STPASS);
      STAMP(5);
      lwait(CNT(C_PU1), 2, dead);
      conv_quarter(b & 3, w - 4, lane);
      STAMPD(22);
      conv_pair_out(ws, lU, 2 * b + pr, w - 4, pr ? CNT(C_PB1) : CNT(C_PB0), dv, keep, lane, dead);
      STAMPD(23);
    }
    if (w < 6) conv_quarter_out(ws, 512 + (b >> 2), b & 3, w - 4, dq, lane, dead);
    FENCE();
    ladd(CNT(C_QA), lane);
    STAMPD(24);
    vmwait0();
    if (ladd_rtn(CNT(C_FYC), lane) == 3) setflag(ws, O_FY + b * 128, tag, lane);
    STAMP(6);
    if (w == 6) wo_load();
    __builtin_amdgcn_s_setprio(3);
    Keep<true> kp;
    tail<true, 0>(a, ws, w, rt, c, tag, wo, kp, dead STPASS);
    tail<true, 1>(a, ws, w, rt, c, tag, wo, kp, dead STPASS);
    if (b == 0 && w == 7 && lane_id() == 0) gst1(tag, ws, 0, 0);
  }
  STAMP(31); STAMPC(30);
}

extern "C" __device__ __attribute__((used, noinline)) void fused_code_end() {}

extern "C" void kernel_launch(void* const* d_in, const int* in_sizes, int n_in, void* d_out, int out_size, void* d_ws, size_t ws_size, hipStream_t stream) {
  Args a;
  a.x = (const u16*)d_in[0]; a.wqkv = (const u16*)d_in[1]; a.wphi = (const u16*)d_in[2]; a.wo = (const u16*)d_in[3];
  a.bk = (const u16*)d_in[5]; a.bv = (const u16*)d_in[6]; a.sk = (const u16*)d_in[7]; a.ssm = (const u16*)d_in[8]; a.dv = (const u16*)d_in[9];
  a.out = (u16*)d_out; a.ws = (unsigned char*)d_ws;
  (void)hipFuncSetAttribute((const void*)fused_kernel, hipFuncAttributeMaxDynamicSharedMemorySize, LDS_BYTES);
  fused_kernel<<<256, 512, LDS_BYTES, stream>>>(a);
}
```

```cpp
#include <hip/hip_runtime.h>
#include <hip/hip_bf16.h>
#include <utility>

typedef unsigned u32;
typedef unsigned short u16;
typedef unsigned long long u64;
typedef float f2 __attribute__((ext_vector_type(2)));
typedef float f4 __attribute__((ext_vector_type(4)));
typedef u32 u2 __attribute__((ext_vector_type(2)));
typedef u32 u4 __attribute__((ext_vector_type(4)));
typedef u32 u2a __attribute__((ext_vector_type(2), aligned(4)));
typedef __bf16 bf2 __attribute__((ext_vector_type(2)));
typedef __bf16 bf8 __attribute__((ext_vector_type(8)));
typedef short s4v __attribute__((ext_vector_type(4)));
typedef __amdgpu_buffer_rsrc_t rsrc_t;
#define LAS __attribute__((address_space(3)))
#define CAS __attribute__((address_space(4)))
#define DEV __device__ __forceinline__
#define UNR _Pragma("unroll")
#define NOUNR _Pragma("unroll 1")
#define FENCE() asm volatile("" ::: "memory")
#define SC1 16

#define WV3 1
constexpr u32 O_FW = 4096, O_FA = O_FW + 32768, O_FK = O_FA + 32768, O_FY = O_FK + 32768, O_FO = O_FY + 32768, O_FB = O_FO + 32768;
constexpr u32 O_WT = 262144, WT_G = 124416;
constexpr u32 O_CS = O_WT + 16 * WT_G;
constexpr u32 O_WOT = O_CS + 62464;
constexpr u32 O_KVT = O_WOT + 663552;
constexpr u32 O_YT = O_KVT + 2359296;
constexpr u32 O_SP = O_YT + 2359296, SP_L = 720896, SP_G = 45056, SP_P = 2816;
constexpr u32 O_OB = O_SP + 2 * SP_L, OB_L = 2359296, OB_P = 147456;
constexpr u32 O_ST = O_OB + 2 * OB_L;
constexpr u32 O_X2 = O_ST + 3 * 65536;
constexpr u32 O_STAMP = O_X2 + 2359296;
constexpr u32 WS_BYTES = O_STAMP + 256 * 8 * 32 * 8;
constexpr u32 SLOT = 9728, L_WQ = 9 * SLOT, L_STG = 5 * SLOT, L_STB = L_STG + 30720;
constexpr u32 L_H0 = 124416, L_H1 = 132736, L_U0 = 141056, L_U1 = 145632, L_H2 = 150528, L_U2 = 154688, L_ZERO = L_U2 + 4096, L_CSL = 159264;
constexpr u32 L_KT = 131072, L_VT = 141152, L_TILE = 150528, L_PT = 158720;
constexpr u32 L_REDA = 124416, L_REDB = 126464, L_Y = 128512, L_KEEP = 139264;
constexpr u32 L_TK = 160256, L_TK2 = 161696, L_TQ = 161888, L_TQ2 = 163248, L_CNT = 163712, LDS_BYTES = 163840;
enum { C_PROG = 0, C_WPROG = 4, C_WOK, C_TBL, C_KVC, C_PHI, C_STB, C_PU0, C_PU1, C_PU2, C_PA0, C_PA1, C_PB0, C_PB1, C_QA, C_QB, C_REDA, C_REDB, C_FYC, C_FAOK, C_GEM, C_PUB, C_KVL, C_KVR, C_WT, C_CLR, C_G2, C_FBOK };
#define CNT(i) (L_CNT + 4 * (i))
constexpr u32 SENT = 0xFFFFFFFFu;
constexpr int SPIN_G = 1 << 17, SPIN_L = 1 << 21;

#define CLRAT 7
#define KVAT 4
#define XLA 5
#define HAT 7
#define FAT 10
#define CLB 2
#define PAT 10
#define PLATE 1
#define KVS 2
#define PF 0
#define CPF 3
#define KAP 1
#define GR3 0
#define NA 5
#define MAP 1
__device__ __forceinline__ int wg_of(int p, int g) {
  return (p >> 2) * 2 + (g >> 3) + 8 * ((p & 3) * 8 + (g & 7));
}
struct Args {
  const u16 *x, *wqkv, *wphi, *wo, *bk, *bv, *sk, *ssm, *dv;
  u16* out;
  unsigned char* ws;
};

template <int I> struct IC { static constexpr int v = I; };
template <class F, int... I> DEV void sfor_(F&& f, std::integer_sequence<int, I...>) { (f(IC<I>{}), ...); }
template <int N, class F> DEV void sfor(F&& f) { sfor_(f, std::make_integer_sequence<int, N>{}); }
#define INL __attribute__((always_inline))
template <class T> DEV void pin(T& v) { asm volatile("" : "+v"(v)); }
template <class T> DEV void pins(T& v) { asm volatile("" : "+s"(v)); }
DEV int opq(int v) { asm volatile("" : "+v"(v)); return v; }
DEV int lane_id() { u32 m = ~0u; asm volatile("" : "+s"(m)); return __builtin_amdgcn_mbcnt_hi(m, __builtin_amdgcn_mbcnt_lo(m, 0)); }
DEV u32 rfl(u32 v) { return __builtin_amdgcn_readfirstlane(v); }
DEV rsrc_t mkrs(const void* p, u32 n) { return __builtin_amdgcn_make_buffer_rsrc((void*)p, 0, n, 0x00020000); }
DEV u32 pk(float a, float b) { f2 v = {a, b}; return __builtin_bit_cast(u32, __builtin_convertvector(v, bf2)); }
DEV u32 pk(f2 v) { return __builtin_bit_cast(u32, __builtin_convertvector(v, bf2)); }
DEV f2 up(u32 p) { f2 r = {__uint_as_float(p << 16), __uint_as_float(p & 0xffff0000u)}; return r; }
DEV f2 rnd(f2 v) { return up(pk(v)); }
DEV float rnd1(float v) { return __uint_as_float(pk(v, v) & 0xffff0000u); }
DEV f4 mfma(u4 a, u4 b, f4 c) { return __builtin_amdgcn_mfma_f32_16x16x32_bf16(__builtin_bit_cast(bf8, a), __builtin_bit_cast(bf8, b), c, 0, 0, 0); }
DEV u32 umin(u32 a, u32 b) { return a < b ? a : b; }
DEV u32 umax(u32 a, u32 b) { return a > b ? a : b; }
DEV u32 umax3(u32 a, u32 b, u32 c) { return umax(umax(a, b), c); }
DEV u32 san(u32 v) { return umin(v, 0xFFFFFFFEu); }
DEV u4 san(u4 v) { u4 r = {san(v.x), san(v.y), san(v.z), san(v.w)}; return r; }
DEV u32 vm(u32 m, u4 v) { return umax3(umax3(m, v.x, v.y), v.z, v.w); }
DEV bool anyl(bool c) { return __builtin_amdgcn_ballot_w64(c) != 0; }
DEV float dot2(u32 a, u32 b, float c) { return __builtin_amdgcn_fdot2_f32_bf16(__builtin_bit_cast(bf2, a), __builtin_bit_cast(bf2, b), c, false); }
template <class T> DEV T ld(u32 o) { return *(LAS T*)(uintptr_t)o; }
template <class T> DEV void st(u32 o, T v) { *(LAS T*)(uintptr_t)o = v; }
DEV u2 trd(u32 o) { return __builtin_bit_cast(u2, __builtin_amdgcn_ds_read_tr16_b64_v4i16((LAS s4v*)(uintptr_t)o)); }
DEV u4 trd2(u32 o0, u32 o1) { u2 a = trd(o0), b = trd(o1); u4 r = {a.x, a.y, b.x, b.y}; return r; }
DEV u4 gld4(rsrc_t r, u32 v, u32 s, int aux) { return aux ? __builtin_amdgcn_raw_buffer_load_b128(r, v, s, SC1) : __builtin_amdgcn_raw_buffer_load_b128(r, v, s, 0); }
DEV u2 gld2(rsrc_t r, u32 v, u32 s, int aux) { return aux ? __builtin_amdgcn_raw_buffer_load_b64(r, v, s, SC1) : __builtin_amdgcn_raw_buffer_load_b64(r, v, s, 0); }
DEV u32 gld1(rsrc_t r, u32 v, u32 s, int aux) { return aux ? __builtin_amdgcn_raw_buffer_load_b32(r, v, s, SC1) : __builtin_amdgcn_raw_buffer_load_b32(r, v, s, 0); }
DEV void gst4(u4 d, rsrc_t r, u32 v, u32 s) { __builtin_amdgcn_raw_buffer_store_b128(d, r, v + s, 0, SC1); }
DEV void gst2(u2 d, rsrc_t r, u32 v, u32 s) { __builtin_amdgcn_raw_buffer_store_b64(d, r, v, s, SC1); }
DEV void gst1(u32 d, rsrc_t r, u32 v, u32 s) { __builtin_amdgcn_raw_buffer_store_b32(d, r, v, s, SC1); }
template <int AUX> DEV void dma(rsrc_t r, u32 l, u32 v, u32 s) {
#if defined(__HIP_DEVICE_COMPILE__)
  __builtin_amdgcn_raw_ptr_buffer_load_lds(r, (LAS void*)(uintptr_t)l, 16, v, s, 0, AUX);
#endif
}
DEV void vmwait0() { asm volatile("s_waitcnt vmcnt(0)" ::: "memory"); }
template <int N> DEV void vmwait() { asm volatile("s_waitcnt vmcnt(%0)" ::"n"(N) : "memory"); }
DEV void bar() { asm volatile("s_waitcnt lgkmcnt(0)\n\ts_barrier" ::: "memory"); }
DEV void lset(u32 a, u32 v) { asm volatile("ds_write_b32 %0, %1" ::"v"(a), "v"(v) : "memory"); }
DEV void ladd(u32 a, int lane) { if (lane == 0) { u32 one = 1; asm volatile("ds_add_u32 %0, %1" ::"v"(a), "v"(one) : "memory"); } }
DEV u32 ladd_rtn(u32 a, int lane) {
  u32 r = 0;
  if (lane == 0) { u32 one = 1; asm volatile("ds_add_rtn_u32 %0, %1, %2\n\ts_waitcnt lgkmcnt(0)" : "=v"(r) : "v"(a), "v"(one) : "memory"); }
  return rfl(r);
}
DEV u32 lget(u32 a) { u32 r; asm volatile("ds_read_b32 %0, %1\n\ts_waitcnt lgkmcnt(0)" : "=v"(r) : "v"(a) : "memory"); return rfl(r); }
DEV u32 lwait(u32 a, u32 t, int& dead) {
  u32 r = lget(a);
  if (__builtin_expect(r < t, 0)) {
    NOUNR for (int i = dead ? SPIN_L : 0; i < SPIN_L; ++i) { r = lget(a); if (r >= t) return r; __builtin_amdgcn_s_sleep(1); }
    dead = 1;
  }
  return r;
}
DEV void poll(rsrc_t ws, u32 off, u32 tag, int& dead) {
  NOUNR for (int i = dead ? SPIN_G : 0; i < SPIN_G; ++i) {
    FENCE();
    u32 v = gld1(ws, off, 0, 1);
    if (!anyl(v != tag)) { FENCE(); return; }
    __builtin_amdgcn_s_sleep(1);
  }
  dead = 1;
}
DEV void setflag(rsrc_t ws, u32 off, u32 tag, int lane) { if (lane == 0) gst1(tag, ws, off, 0); }
#define SW16(a, b) __builtin_amdgcn_permlane16_swap(a, b, false, false)
#define SW32(a, b) __builtin_amdgcn_permlane32_swap(a, b, false, false)
DEV float sw16add(float a, float b) { auto r = SW16(__float_as_uint(a), __float_as_uint(b)); return __uint_as_float(r[0]) + __uint_as_float(r[1]); }
DEV float sw32add(float a, float b) { auto r = SW32(__float_as_uint(a), __float_as_uint(b)); return __uint_as_float(r[0]) + __uint_as_float(r[1]); }

#define STAMP(i)
#define STAMPC(i)
#define STARG
#define STPASS

#define STAMPD(i)
#define STAMPL(i) STAMP(i + L * 13)
DEV f2 ln_stats(float s, float sq) {
  float mean = s * (1.0f / 576.0f);
  float var = fmaxf(sq * (1.0f / 576.0f) - mean * mean, 0.0f);
  float sd = rnd1(__builtin_amdgcn_sqrtf(rnd1(rnd1(var) + 1e-5f)));
  f2 r = {rnd1(mean), __builtin_amdgcn_rcpf(sd)};
  return r;
}
DEV void stats_from(u4 v, float (&m)[2], float (&rs)[2]) {
  float s = __uint_as_float(v.x) + __uint_as_float(v.z), sq = __uint_as_float(v.y) + __uint_as_float(v.w);
  s = sw16add(s, s); sq = sw16add(sq, sq);
  f2 t = ln_stats(s, sq);
  auto r0 = SW32(__float_as_uint(t.x), __float_as_uint(t.x));
  auto r1 = SW32(__float_as_uint(t.y), __float_as_uint(t.y));
  m[0] = __uint_as_float(r0[0]); m[1] = __uint_as_float(r0[1]); rs[0] = __uint_as_float(r1[0]); rs[1] = __uint_as_float(r1[1]);
}
DEV u4 ld_stats(rsrc_t ws, u32 off) { u2 a = gld2(ws, off, 0, 1), b = gld2(ws, off + 256, 0, 1); return u4{a.x, a.y, b.x, b.y}; }
DEV u4 poll_stats(rsrc_t ws, u32 off, u4 v, int& dead) {
  if (anyl(vm(0, v) == SENT)) {
    NOUNR for (int i = dead ? SPIN_G : 0; i < SPIN_G; ++i) {
      FENCE();
      v = ld_stats(ws, off);
      if (!anyl(vm(0, v) == SENT)) { FENCE(); return v; }
    }
    dead = 1;
  }
  FENCE();
  return v;
}

DEV void pub_stats(rsrc_t ws, float s0, float s1, float q0, float q1, u32 wa, u32 ra, u32 cnt, u32 last, u32 dst) {
  float t = sw16add(sw32add(s0, s1), sw32add(q0, q1));
  u32 n, one = 1;
  u64 ex;
  f4 r[6];
  asm volatile("ds_write_b32 %8, %9\n\ts_mov_b64 %7, exec\n\ts_mov_b64 exec, 1\n\tds_add_rtn_u32 %6, %10, %11\n\ts_mov_b64 exec, %7\n\t"
               "ds_read_b128 %0, %12\n\tds_read_b128 %1, %12 offset:256\n\tds_read_b128 %2, %12 offset:512\n\tds_read_b128 %3, %12 offset:768\n\t"
               "ds_read_b128 %4, %12 offset:1024\n\tds_read_b128 %5, %12 offset:1280\n\ts_waitcnt lgkmcnt(0)"
               : "=&v"(r[0]), "=&v"(r[1]), "=&v"(r[2]), "=&v"(r[3]), "=&v"(r[4]), "=&v"(r[5]), "=&v"(n), "=&s"(ex)
               : "v"(wa), "v"(t), "v"(cnt), "v"(one), "v"(ra)
               : "memory");
  if (rfl(n) == last) gst4(san(__builtin_bit_cast(u4, ((r[0] + r[1]) + (r[2] + r[3])) + (r[4] + r[5]))), ws, dst, 0);
}

struct KVin { u32 k[11], v[8]; };
DEV u4 kv_w(const Args& a, int T, int c) { return gld4(mkrs(a.sk, 4608), (144 * c + 2 * (T >> 2)) * 8, 0, 0); }
DEV void kv_read_asm(int T, KVin& in) {
  const u32 base = (T & 3) * 2304 + (T >> 2) * 4;
  UNR for (int i = 0; i < 11; ++i) asm volatile("ds_read_b32 %0, %1 offset:%2" : "=v"(in.k[i]) : "v"(L_KT + base), "n"(i * 288) : "memory");
  UNR for (int i = 0; i < 8; ++i) asm volatile("ds_read_b32 %0, %1 offset:%2" : "=v"(in.v[i]) : "v"(L_VT + base), "n"(i * 288) : "memory");
  asm volatile("s_waitcnt lgkmcnt(0)"
               : "+v"(in.k[0]), "+v"(in.k[1]), "+v"(in.k[2]), "+v"(in.k[3]), "+v"(in.k[4]), "+v"(in.k[5]), "+v"(in.k[6]), "+v"(in.k[7]), "+v"(in.k[8]),
                 "+v"(in.k[9]), "+v"(in.k[10]), "+v"(in.v[0]), "+v"(in.v[1]), "+v"(in.v[2]), "+v"(in.v[3]), "+v"(in.v[4]), "+v"(in.v[5]), "+v"(in.v[6]),
                 "+v"(in.v[7])
               :
               : "memory");
}
DEV void kv_read(int T, KVin& in) {
  const u32 base = (T & 3) * 2304 + (T >> 2) * 4;
  UNR for (int i = 0; i < 11; ++i) in.k[i] = ld<u32>(L_KT + base + i * 288);
  UNR for (int i = 0; i < 8; ++i) in.v[i] = ld<u32>(L_VT + base + i * 288);
}
#define SB() __builtin_amdgcn_sched_barrier(0)
DEV void add_rnd8(float (&t)[8], float (&a)[8]) {
  asm volatile("v_cvt_pk_bf16_f32 %8, %0, %8\n\tv_cvt_pk_bf16_f32 %9, %1, %9\n\tv_cvt_pk_bf16_f32 %10, %2, %10\n\tv_cvt_pk_bf16_f32 %11, %3, %11\n\t"
               "v_cvt_pk_bf16_f32 %12, %4, %12\n\tv_cvt_pk_bf16_f32 %13, %5, %13\n\tv_cvt_pk_bf16_f32 %14, %6, %14\n\tv_cvt_pk_bf16_f32 %15, %7, %15\n\t"
               "v_dot2_f32_bf16 %0, %8, %16, 0\n\tv_dot2_f32_bf16 %1, %9, %16, 0\n\tv_dot2_f32_bf16 %2, %10, %16, 0\n\tv_dot2_f32_bf16 %3, %11, %16, 0\n\t"
               "v_dot2_f32_bf16 %4, %12, %16, 0\n\tv_dot2_f32_bf16 %5, %13, %16, 0\n\tv_dot2_f32_bf16 %6, %14, %16, 0\n\tv_dot2_f32_bf16 %7, %15, %16, 0\n\ts_nop 2"
               : "+v"(t[0]), "+v"(t[1]), "+v"(t[2]), "+v"(t[3]), "+v"(t[4]), "+v"(t[5]), "+v"(t[6]), "+v"(t[7]), "+v"(a[0]), "+v"(a[1]), "+v"(a[2]),
                 "+v"(a[3]), "+v"(a[4]), "+v"(a[5]), "+v"(a[6]), "+v"(a[7])
               : "s"(0x3F803F80u));
}
DEV void kv_compute(rsrc_t ws, int T, int rt, int c, const KVin& in, u4 w, bool on) {
  const int ch = 144 * c + 2 * (T >> 2), s0 = 32 * rt + 8 * (T & 3);
  f2 w0 = {__uint_as_float(w.x << 16), __uint_as_float(w.z << 16)}, w1 = {__uint_as_float(w.x & 0xffff0000u), __uint_as_float(w.z & 0xffff0000u)};
  f2 w2 = {__uint_as_float(w.y << 16), __uint_as_float(w.w << 16)}, w3 = {__uint_as_float(w.y & 0xffff0000u), __uint_as_float(w.w & 0xffff0000u)};
  f2 kf[11];
  UNR for (int i = 0; i < 11; ++i) kf[i] = up(in.k[i]);
  float t0[8], t1[8], a0[8], a1[8];
  UNR for (int s = 0; s < 8; ++s) { const f2 x = kf[s] * w3, y = kf[s + 1] * w2; t0[s] = x.x; t1[s] = x.y; a0[s] = y.x; a1[s] = y.y; }
  add_rnd8(t0, a0); add_rnd8(t1, a1);
  UNR for (int s = 0; s < 8; ++s) { const f2 y = kf[s + 2] * w1; a0[s] = y.x; a1[s] = y.y; }
  add_rnd8(t0, a0); add_rnd8(t1, a1);
  UNR for (int s = 0; s < 8; ++s) { const f2 y = kf[s + 3] * w0; a0[s] = y.x; a1[s] = y.y; }
  add_rnd8(t0, a0); add_rnd8(t1, a1);
  u32 P[8];
  UNR for (int s = 0; s < 8; ++s) P[s] = pk(t0[s], t1[s]);
  SB();
  UNR for (int s = 0; s < 8; ++s) kf[s] = up(P[s]) * up(in.v[s]);
  SB();
  UNR for (int s = 0; s < 8; ++s) P[s] = pk(kf[s]);
  u4 o0, o1;
  UNR for (int d = 0; d < 4; ++d) {
    o0[d] = __builtin_amdgcn_perm(P[6 - 2 * d], P[7 - 2 * d], 0x05040100u);
    o1[d] = __builtin_amdgcn_perm(P[6 - 2 * d], P[7 - 2 * d], 0x07060302u);
  }
  const u32 off = on ? O_KVT + ch * 4096 + (2040 - s0) * 2 : 0x80000000u;
  gst4(o0, ws, off, 0);
  gst4(o1, ws, off, 4096);
}

struct Hin { u4 h[4]; };
DEV void h_load(const Args& a, int ch, int lane, Hin& in) {
  rsrc_t rh = mkrs(a.ssm, 2359296);
  UNR for (int m = 0; m < 4; ++m) in.h[m] = gld4(rh, ch * 4096 + lane * 16, m * 1024, 0);
}
DEV void h_stage(u32 lH, u32 lU, int lane, const Hin& in) {
  UNR for (int m = 0; m < 4; ++m) {
    const u4 h = in.h[m];
    const u32 nx = __builtin_amdgcn_update_dpp(m < 3 ? rfl(in.h[m < 3 ? m + 1 : m].x) : 0u, h.x, 0x130, 0xF, 0xF, false);
    u4 s = {__builtin_amdgcn_alignbit(h.y, h.x, 16), __builtin_amdgcn_alignbit(h.z, h.y, 16), __builtin_amdgcn_alignbit(h.w, h.z, 16), __builtin_amdgcn_alignbit(nx, h.w, 16)};
    st<u4>(lH + 32 + m * 1024 + lane * 16, h);
    st<u4>(lH + 4160 + 32 + m * 1024 + lane * 16, s);
  }
  u32 h0 = rfl(in.h[0].x) << 16;
  u4 z = {0, 0, 0, lane == 3 ? h0 : 0u};
  if (lane < 4) st<u4>(lH + (lane >> 1) * 4160 + (lane & 1) * 16, z);
  u4 zz = {0, 0, 0, 0};
  if (lane < 30) st<u4>(lU + 4096 + lane * 16, zz);
}
DEV void u_stage(rsrc_t ws, u32 lU, int ch, int lane) {
  UNR for (int m = 0; m < 4; ++m) dma<SC1>(ws, lU + m * 1024, O_KVT + ch * 4096 + lane * 16, m * 1024);
}
DEV float load_dv(const Args& a, int ch) {
  const u32 v = ((const CAS u32*)a.dv)[ch >> 1];
  return __uint_as_float((ch & 1) ? v & 0xffff0000u : v << 16);
}
DEV void late_fill(rsrc_t ws, int h, int p, int g, int rt, int c, int lane) {
  const u4 s4 = {SENT, SENT, SENT, SENT};
  if (h < 2) { UNR for (int i = 0; i < 9; ++i) gst4(s4, ws, O_OB + lane * 16, h * OB_L + p * OB_P + i * 16384 + g * 1024); }
  else if (h == 2) {
    UNR for (int i = 0; i < 9; ++i) {
      u32 o = i * 1024 + lane * 16;
      o += o >= 4608 ? 18432 - 4608 : 0;
      gst4(s4, ws, O_X2 + o, 2 * rt * 18432 + c * 4608);
    }
  } else {
    gst4(s4, ws, lane < 48 ? O_ST + (lane >> 4) * 65536 + (lane & 15) * 16 : 0x80000000u, rt * 1024 + c * 256);
    UNR for (int i = 0; i < 3; ++i) gst4(s4, ws, (i < 2 || lane < 48) ? O_SP + i * 1024 + lane * 16 : 0x80000000u, SP_L + g * SP_G + p * SP_P);
  }
}
DEV u2 y_pack(f4 s, u32 lU, int j, float dv, int q, int i16) {
  u2 uu = ld<u2>(lU + 2 * (2044 - 256 * j - 16 * i16 - 4 * q));
  f2 u32_ = up(uu.x), u10 = up(uu.y);
  f2 g01 = {u10.y * dv, u10.x * dv}, g23 = {u32_.y * dv, u32_.x * dv};
  f2 s01 = {s.x, s.y}, s23 = {s.z, s.w};
  return u2{pk(rnd(s01) + rnd(g01)), pk(rnd(s23) + rnd(g23))};
}
DEV void conv_pair(rsrc_t ws, u32 lH, u32 lU, int h, u32 cB, u32 fboff, u32 tag, f4 (&keep)[4], int lane, int& dead STARG) {
  const int q = lane >> 4, i16 = lane & 15, e = h & 1;
  f4 acc[8];
  UNR for (int i = 0; i < 8; ++i) acc[i] = f4{0, 0, 0, 0};
  const u32 aB = lU + 2 * (2032 - 16 * i16 + 8 * q) - 3584 + 256 * e;
  const u32 aA = lH + ((i16 & 1) ? 0 : 4160) + 4 * (((i16 + 1) >> 1) + 4 * q) + 256 * e;
  const u32 aA4[4] = {aA, aA + 1024, aA + 2048, aA + 3072};
  u4 B[8];
  u2 Al[4], Ah[4];
#define LA(s, t, bb)                                                                                                     \
  asm volatile("ds_read2_b32 %0, %2 offset0:%3 offset1:%4\n\tds_read2_b32 %1, %2 offset0:%5 offset1:%6"                  \
               : "=&v"(Al[s]), "=&v"(Ah[s])                                                                              \
               : "v"(aA4[(t) >> 1]), "n"(16 * (bb) + 128 * ((t) & 1)), "n"(16 * (bb) + 128 * ((t) & 1) + 1),              \
                 "n"(16 * (bb) + 128 * ((t) & 1) + 2), "n"(16 * (bb) + 128 * ((t) & 1) + 3)                               \
               : "memory")
#define LB(v, bb) asm volatile("ds_read_b128 %0, %1 offset:%2" : "=&v"(B[v]) : "v"(aB), "n"(512 * (7 - (v)) + 64 * (bb)) : "memory")
#define WA(n, s) asm volatile("s_waitcnt lgkmcnt(%4)" : "+v"(Al[s]), "+v"(Ah[s]), "+v"(Al[(s) + 1]), "+v"(Ah[(s) + 1]) : "n"(n))
#define WB(n, v) asm volatile("s_waitcnt lgkmcnt(%1)" : "+v"(B[v]) : "n"(n))
#define MM(s, t, v) do { acc[(t) + (v)] = mfma(u4{Al[s].x, Al[s].y, Ah[s].x, Ah[s].y}, B[v], acc[(t) + (v)]); SB(); } while (0)
  LB(7, 0); LB(6, 0); LB(5, 0); LA(0, 0, 0); LA(1, 7, 0); LB(4, 0); LB(3, 0); LB(2, 0); LB(1, 0); LB(0, 0);
  UNR for (int bb = 0; bb < 4; ++bb) {
    const int nb = bb < 3 ? 1 : 0;
    LA(2, 1, bb); LA(3, 6, bb);
    asm volatile("s_waitcnt lgkmcnt(9)" : "+v"(Al[0]), "+v"(Ah[0]), "+v"(Al[1]), "+v"(Ah[1]), "+v"(B[7]), "+v"(B[6]), "+v"(B[5]));
    SB();
    MM(0, 0, 7); if (nb) LB(7, bb + 1);
    MM(0, 0, 6); MM(0, 0, 5);
    WB(8 + nb, 4); MM(0, 0, 4); WB(7 + nb, 3); MM(0, 0, 3); WB(6 + nb, 2); MM(0, 0, 2);
    WB(5 + nb, 1); MM(0, 0, 1); WB(4 + nb, 0); MM(0, 0, 0); MM(1, 7, 0);
    LA(0, 2, bb); LA(1, 5, bb); WA(4 + nb, 2); SB();
    MM(2, 1, 5); MM(2, 1, 6); if (nb) LB(6, bb + 1);
    MM(2, 1, 4); MM(2, 1, 3); MM(2, 1, 2); MM(2, 1, 1); MM(2, 1, 0); MM(3, 6, 1); MM(3, 6, 0);
    LA(2, 3, bb); LA(3, 4, bb); WA(4 + nb, 0); SB();
    MM(0, 2, 5); if (nb) LB(5, bb + 1);
    MM(0, 2, 4); MM(0, 2, 3); MM(0, 2, 2); MM(0, 2, 1); MM(0, 2, 0); MM(1, 5, 2); MM(1, 5, 1); MM(1, 5, 0);
    if (nb) { LA(0, 0, bb + 1); LA(1, 7, bb + 1); }
    WA(5 * nb, 2); SB();
    MM(2, 3, 4); if (nb) LB(4, bb + 1);
    MM(2, 3, 3); MM(2, 3, 2); MM(2, 3, 1); MM(2, 3, 0);
    MM(3, 4, 3); if (nb) LB(3, bb + 1);
    MM(3, 4, 2); if (nb) LB(2, bb + 1);
    MM(3, 4, 1); if (nb) LB(1, bb + 1);
    MM(3, 4, 0); if (nb) LB(0, bb + 1);
    if (bb == CLB) { vmwait0(); if (h == 2) lset(CNT(C_PU1), 2); if (ladd_rtn(CNT(C_CLR), lane) == 3) setflag(ws, fboff, tag, lane); SB(); }
    STAMPD(17 + bb);

  }
  lwait(CNT(C_PUB), 4, dead);
  UNR for (int i = 0; i < 4; ++i) {
    keep[i] = e ? acc[4 + i] : acc[i];
    st<f4>(L_STG + h * 4096 + i * 1024 + lane * 16, e ? acc[i] : acc[4 + i]);
  }
  FENCE();
  ladd(cB, lane);
}
DEV void conv_pair_out(rsrc_t ws, u32 lU, int ch, int h, u32 cB, float dv, const f4 (&keep)[4], int lane, int& dead) {
  const int q = lane >> 4, i16 = lane & 15, e = h & 1;
  lwait(cB, 2, dead);
  u2 Y[4];
  UNR for (int i = 0; i < 4; ++i) Y[i] = y_pack(keep[i] + ld<f4>(L_STG + (h ^ 1) * 4096 + i * 1024 + lane * 16), lU, 4 * e + i, dv, q, i16);
  UNR for (int i = 0; i < 2; ++i) {
    auto r0 = SW16(Y[2 * i].x, Y[2 * i + 1].x);
    auto r1 = SW16(Y[2 * i].y, Y[2 * i + 1].y);
    u4 o = {r0[0], r1[0], r0[1], r1[1]};
    gst4(o, ws, O_YT + ch * 4096 + 2 * (256 * (4 * e + 2 * i + (q & 1)) + 16 * i16 + 8 * (q >> 1)), 0);
  }
}
DEV void conv_quarter(int iq, int h, int lane) {
  const int q = lane >> 4, i16 = lane & 15;
  const u32 aB = L_U2 + 2 * (2032 - 16 * i16 + 8 * q) + 128 * h;
  const u32 aA = L_H2 + 4 * (((i16 + 1) >> 1) + 4 * q) + 128 * h;
  const u32 sh = (i16 & 1) ? 0 : 16;
  f4 m[5];
  UNR for (int i = 0; i < 5; ++i) m[i] = f4{0, 0, 0, 0};
  u2 a0[3][2], a1[3][2];
  u32 a2[3][2];
  u4 bq[3][2];
#define LQ(n)                                                                                                                     \
  {                                                                                                                               \
    const int t_ = (n) <= iq ? (n) : (n)-iq - 1, v_ = (n) <= iq ? iq - (n) : 8 - (n);                                             \
    const u32 pa = aA + 512 * t_, pb = aB - 512 * v_;                                                                             \
    asm volatile("ds_read2_b32 %0, %8 offset0:0 offset1:1\n\tds_read2_b32 %1, %8 offset0:2 offset1:3\n\tds_read_b32 %2, %8 offset:16\n\t" \
                 "ds_read_b128 %3, %9\n\tds_read2_b32 %4, %8 offset0:16 offset1:17\n\tds_read2_b32 %5, %8 offset0:18 offset1:19\n\t"  \
                 "ds_read_b32 %6, %8 offset:80\n\tds_read_b128 %7, %9 offset:64"                                                  \
                 : "=&v"(a0[(n) % 3][0]), "=&v"(a1[(n) % 3][0]), "=&v"(a2[(n) % 3][0]), "=&v"(bq[(n) % 3][0]), "=&v"(a0[(n) % 3][1]), \
                   "=&v"(a1[(n) % 3][1]), "=&v"(a2[(n) % 3][1]), "=&v"(bq[(n) % 3][1])                                            \
                 : "v"(pa), "v"(pb)                                                                                               \
                 : "memory");                                                                                                     \
  }
  LQ(0) LQ(1)
  UNR for (int n = 0; n < 9; ++n) {
    const int s = n % 3;
    asm volatile("s_waitcnt lgkmcnt(%8)" : "+v"(a0[s][0]), "+v"(a1[s][0]), "+v"(a2[s][0]), "+v"(bq[s][0]), "+v"(a0[s][1]), "+v"(a1[s][1]), "+v"(a2[s][1]), "+v"(bq[s][1]) : "n"(n < 8 ? 8 : 0));
    if (n < 7) LQ(n + 2)
    UNR for (int k = 0; k < 2; ++k) {
      u4 A = {__builtin_amdgcn_alignbit(a0[s][k].y, a0[s][k].x, sh), __builtin_amdgcn_alignbit(a1[s][k].x, a0[s][k].y, sh),
              __builtin_amdgcn_alignbit(a1[s][k].y, a1[s][k].x, sh), __builtin_amdgcn_alignbit(a2[s][k], a1[s][k].y, sh)};
      m[n < 4 ? n : 4] = mfma(A, bq[s][k], m[n < 4 ? n : 4]);
    }
    SB();
  }
  f4 s1 = m[0], s2 = m[4];
  UNR for (int n = 1; n < 4; ++n) { if (n <= iq) s1 += m[n]; else s2 += m[n]; }
  st<f4>(L_STG + 16384 + h * 2048 + lane * 16, s1);
  st<f4>(L_STG + 16384 + h * 2048 + 1024 + lane * 16, s2);
  FENCE();
  ladd(CNT(C_QB), lane);
}
DEV void conv_quarter_out(rsrc_t ws, int ch, int iq, int h, float dv, int lane, int& dead) {
  const int q = lane >> 4, i16 = lane & 15;
  lwait(CNT(C_QB), 4, dead);
  f4 s = ld<f4>(L_STG + 16384 + h * 1024 + lane * 16);
  UNR for (int i = 1; i < 4; ++i) s += ld<f4>(L_STG + 16384 + i * 2048 + h * 1024 + lane * 16);
  const int j = h ? 7 - iq : iq;
  u2 Y = y_pack(s, L_U2, j, dv, q, i16);
  auto r0 = SW16(Y.x, Y.x);
  auto r1 = SW16(Y.y, Y.y);
  gst4(u4{r0[0], r1[0], r0[1], r1[1]}, ws, (q & 1) ? 0x80000000u : O_YT + ch * 4096 + 2 * (256 * j + 16 * i16 + 8 * (q >> 1)), 0);
}

template <bool HELP> struct Keep { u2 x[HELP ? 4 : 1], y[HELP ? 4 : 1]; };
template <bool HELP, int L>
DEV void tail(const Args& a, rsrc_t ws, int w, int rt, int c, u32 tag, const u4 (&wo)[HELP ? 36 : 18], Keep<HELP>& kp, int& dead STARG) {
  constexpr int NB = HELP ? 2 : 1, NU = HELP ? 4 : 1;
  const int lane = lane_id(), q = lane >> 4, i16 = lane & 15;
  const int bl0 = HELP ? 2 * (w - 4) : 8, slot = HELP ? w - 4 : 4 + w;
  if (L == 0 && HELP) {
    rsrc_t rx = mkrs(a.x, 2359296);
    const u32 o = (32 * rt + i16) * 1152 + (144 * c + 4 * q) * 2;
    UNR for (int u = 0; u < NU; ++u) kp.x[u] = gld2(rx, o, (bl0 + (u >> 1)) * 32 + (u & 1) * 16 * 1152, 0);
  }
  u32 fyoff = 0, fy = tag;
  if (L == 0) {
    const int chl = 144 * c + 16 * bl0 + (lane & (16 * NB - 1)), j = rt >> 3;
    fyoff = O_FY + (chl < 512 ? chl >> 1 : 4 * (chl - 512) + (j < 4 ? j : 7 - j)) * 128;
    pin(fyoff);
  }
  u32 bo0 = L_STG + (HELP ? 0 : w * 18432) + lane * 16, bo1 = bo0 + 18432;
  u32 rwa = slot * 256 + (16 * (lane >> 5) + i16) * 8 + (q & 1) * 4 + L_REDA, rra = i16 * 16 + L_REDA;
  u32 sdst = lane < 16 ? O_ST + L * 2 * 65536 + rt * 1024 + c * 256 + lane * 16 : 0x80000000u;
  u32 stoff = O_ST + L * 2 * 65536 + rt * 1024 + (q & 1) * 512 + (16 * (q >> 1) + i16) * 8;
  u32 odst;
  {
    const int tb = HELP ? q & 1 : w;
    const bool on = HELP || !(q & 1);
    if (L == 0) odst = on ? O_X2 + tb * 18432 + (q >> 1) * 256 + i16 * 16 + 2 * rt * 18432 + (9 * c + bl0) * 512 : 0x80000000u;
    else odst = on ? (32 * rt + 16 * tb + i16) * 1152 + (q >> 1) * 16 + 288 * c + 32 * bl0 : 0x80000000u;
  }
  pin(bo0); pin(rwa); pin(rra); pin(sdst); pin(stoff); pin(odst);
  if (HELP) pin(bo1);
  bar();
  STAMPL(13);
  if (L == 0) fy = gld1(ws, fyoff, 0, 1);
  f4 acc[NU];
  UNR for (int u = 0; u < NU; ++u) acc[u] = f4{0, 0, 0, 0};
  if (HELP) {
    u4 R[5];
    UNR for (int n = 0; n < 5; ++n) R[n] = ld<u4>(((n & 1) ? bo1 : bo0) + (n >> 1) * 1024);
    SB();
    UNR for (int n = 0; n < 36; ++n) {
      const int ks = n >> 1, tb = n & 1, nn = n + 5;
      acc[tb] = mfma(wo[ks], R[n % 5], acc[tb]);
      acc[NU - 2 + tb] = mfma(wo[(NB - 1) * 18 + ks], R[n % 5], acc[NU - 2 + tb]);
      SB();
      if (nn < 36) R[n % 5] = ld<u4>(((nn & 1) ? bo1 : bo0) + (nn >> 1) * 1024);
      SB();
    }
  } else {
    f4 a2 = {0, 0, 0, 0};
    const u32 bo = bo0;
    u4 R[18];
    UNR for (int ks = 0; ks < 12; ++ks) R[ks] = ld<u4>(bo + ks * 1024);
    SB();
    UNR for (int ks = 0; ks < 18; ks += 2) {
      acc[0] = mfma(wo[ks], R[ks], acc[0]); a2 = mfma(wo[ks + 1], R[ks + 1], a2);
      if (ks < 6) { R[ks + 12] = ld<u4>(bo + (ks + 12) * 1024); R[ks + 13] = ld<u4>(bo + (ks + 13) * 1024); }
      SB();
    }
    acc[0] += a2;
  }
  FENCE();
  if (L == 0) ladd(CNT(C_G2), lane);
  u4 yv[NB];
  if (L == 0) {
    if (anyl(fy != tag)) poll(ws, fyoff, tag, dead);
    FENCE();
    UNR for (int i = 0; i < NB; ++i) yv[i] = gld4(ws, O_YT + (lane >> 2) * 4096 + (lane & 3) * 16, (144 * c + 16 * (bl0 + i)) * 4096 + rt * 64, 1);
  }
  f2 x1[NU][2];
  float S[2] = {0, 0}, Q[2] = {0, 0};
  UNR for (int u = 0; u < NU; ++u) {
    f2 lo = {acc[u].x, acc[u].y}, hi = {acc[u].z, acc[u].w};
    x1[u][0] = rnd(rnd(lo) + up(kp.x[u].x)); x1[u][1] = rnd(rnd(hi) + up(kp.x[u].y));
  }
  if (HELP) {
    UNR for (int tb = 0; tb < 2; ++tb) {
      f2 s = (x1[tb][0] + x1[tb][1]) + (x1[NU - 2 + tb][0] + x1[NU - 2 + tb][1]);
      f2 sq = x1[tb][0] * x1[tb][0] + x1[tb][1] * x1[tb][1] + x1[NU - 2 + tb][0] * x1[NU - 2 + tb][0] + x1[NU - 2 + tb][1] * x1[NU - 2 + tb][1];
      S[tb] = s.x + s.y; Q[tb] = sq.x + sq.y;
    }
  } else {
    f2 s = x1[0][0] + x1[0][1], sq = x1[0][0] * x1[0][0] + x1[0][1] * x1[0][1];
    S[0] = w ? 0.0f : s.x + s.y; S[1] = w ? s.x + s.y : 0.0f; Q[0] = w ? 0.0f : sq.x + sq.y; Q[1] = w ? sq.x + sq.y : 0.0f;
  }
  pub_stats(ws, S[0], S[1], Q[0], Q[1], rwa, rra, CNT(C_REDA), 6 * L + 5, sdst);
  STAMPL(14);
  u4 sv = ld_stats(ws, stoff);
  if (L == 0) {
    const u32 yb = L_Y + (HELP ? bl0 : 8 + w) * 1024;
    UNR for (int i = 0; i < NB; ++i) st<u4>(yb + i * 1024 + lane * 16, yv[i]);
    FENCE();
    const u32 ya = yb + (4 * q + (i16 >> 2)) * 64 + (i16 & 3) * 8;
    UNR for (int u = 0; u < NU; ++u) kp.y[u] = trd(ya + (u >> 1) * 1024 + (HELP ? u & 1 : w) * 32);
  }
  sv = poll_stats(ws, stoff, sv, dead);
  STAMPL(15);
  float m[2], rs[2];
  stats_from(sv, m, rs);
  UNR for (int u = 0; u < NU; ++u) {
    const float mu = HELP ? m[u & 1] : (w ? m[1] : m[0]), ru = HELP ? rs[u & 1] : (w ? rs[1] : rs[0]);
    UNR for (int h = 0; h < 2; ++h) {
      f2 xn = rnd(rnd(x1[u][h] - mu) * ru);
      kp.x[u][h] = pk(x1[u][h] + rnd(xn * up(kp.y[u][h])));
    }
  }
  UNR for (int i = 0; i < NB; ++i) {
    auto r0 = SW16(kp.x[2 * i].x, kp.x[HELP ? 2 * i + 1 : 0].x);
    auto r1 = SW16(kp.x[2 * i].y, kp.x[HELP ? 2 * i + 1 : 0].y);
    const u4 o = {r0[0], r1[0], r0[1], r1[1]};
    if (L == 0) gst4(san(o), ws, odst, i * 512);
    else __builtin_amdgcn_raw_buffer_store_b128(o, mkrs(a.out, 2359296), odst + 32 * i, 0, 0);
  }
  if (L == 0) {
    f2 s[NU], sq[NU];
    UNR for (int u = 0; u < NU; ++u) { f2 lo = up(kp.x[u].x), hi = up(kp.x[u].y); s[u] = lo + hi; sq[u] = lo * lo + hi * hi; }
    if (HELP) {
      UNR for (int tb = 0; tb < 2; ++tb) { f2 t = s[tb] + s[NU - 2 + tb], tq = sq[tb] + sq[NU - 2 + tb]; S[tb] = t.x + t.y; Q[tb] = tq.x + tq.y; }
    } else {
      S[0] = w ? 0.0f : s[0].x + s[0].y; S[1] = w ? s[0].x + s[0].y : 0.0f; Q[0] = w ? 0.0f : sq[0].x + sq[0].y; Q[1] = w ? sq[0].x + sq[0].y : 0.0f;
    }
    pub_stats(ws, S[0], S[1], Q[0], Q[1], rwa + (L_REDB - L_REDA), rra + (L_REDB - L_REDA), CNT(C_REDB), 5, sdst + 65536);
    if (HELP) STAMP(16);
  }
}

DEV void w_slot(rsrc_t ws, int j, int rot, int g, int lane) {
  int kr = j + rot; kr -= kr >= 9 ? 9 : 0;
  UNR for (int i = 0; i < 9; ++i) dma<SC1>(ws, j * SLOT + i * 1024, O_WT + lane * 16, g * WT_G + kr * SLOT + i * 1024);
  if (lane < 32) dma<SC1>(ws, j * SLOT + 9216, O_WT + lane * 16, g * WT_G + kr * SLOT + 9216);
}

template <int L>
DEV void main_layer(const Args& a, rsrc_t ws, int b, int w, int p, int g, int rt, int c, int rot, u32 tag, u4 (&B)[36], float (&mS)[2], float (&mQ)[2], u32& wp, int& dead STARG) {
    constexpr float LOG2E = 1.4426950408889634f;
    int lane = lane_id(), q = lane >> 4, i16 = lane & 15;
    {
      f4 acc[5][2];
      u4 sv = {0, 0, 0, 0};
      auto stoff = [&]() -> u32 { return O_ST + 65536 + (4 * p + w) * 1024 + (q & 1) * 512 + (16 * (q >> 1) + i16) * 8; };
      u32 so[9], xo0 = 0, xo1 = 0, sto = 0;
      auto b_load = [&](int j) INL { UNR for (int i = 0; i < 4; ++i) B[4 * j + i] = gld4(ws, ((i & 1) ? xo1 : xo0) + (i >> 1) * 1024, so[j], 1); };
      auto b_addr = [&]() INL {
        lane = lane_id(); q = lane >> 4; i16 = lane & 15;
        xo0 = O_X2 + lane * 16 + (8 * p + 2 * w) * 18432; xo1 = xo0 + 18432; sto = stoff();
        pin(xo0); pin(xo1); pin(sto);
      };
      if (L == 1) {
        UNR for (int j = 0; j < 9; ++j) { int kr = j + rot; kr -= kr >= 9 ? 9 : 0; so[j] = kr * 2048; pins(so[j]); }
        b_addr();
        u32 G = lane < 36 ? lane : (lane < 40 ? 9 * (lane - 36) + 8 : 0);
        u32 po = O_X2 + (8 * p + 2 * w + (lane >= 36 && lane < 40 ? 1 : 0)) * 18432 + G * 512;
        NOUNR for (int i = dead ? SPIN_G : 0; i < SPIN_G; ++i) {
          FENCE();
          u32 v = gld1(ws, po, 0, 1);
          UNR for (int j = 0; j < PF; ++j) b_load(j);
          if (!anyl(v == SENT)) break;
        }
        FENCE();
        STAMP(16);
      }
      f4 cs[5];
      bool svbad = false;
      for (int pass = 0;; ++pass) {
        if (L == 1) {
          if (pass) { UNR for (int j = 0; j < PF; ++j) b_load(j); }
          UNR for (int j = PF; j < 9; ++j) { b_load(j); if (j == PF) sv = ld_stats(ws, sto); }
          SB();
        }
        lane = lane_id(); q = lane >> 4; i16 = lane & 15;
        UNR for (int i = 0; i < 5; ++i) acc[i][0] = acc[i][1] = f4{0, 0, 0, 0};
        u32 vmx = 0, fl = 0;
        if (L == 0) { wp = lwait(CNT(C_WPROG), 1, dead); STAMP(3); }
        u32 b0[2], b4[2];
        const int r4 = 64 + 3 * (i16 >> 2) + ((i16 & 3) < 2 ? (i16 & 3) : 2);
        UNR for (int u = 0; u < 2; ++u) {
          b0[u] = i16 * 128 + (((4 * u + q) ^ (i16 >> 1)) << 4);
          b4[u] = r4 * 128 + (((4 * u + q) ^ ((r4 >> 1) & 7)) << 4);
        }
        u4 A[NA];
        UNR for (int n = 0; n < NA; ++n) A[n] = ld<u4>(n % 5 < 4 ? b0[n / 5] + (n % 5) * 2048 : b4[n / 5]);
        SB();
        sfor<90>([&](auto ic) INL {
          constexpr int n = decltype(ic)::v, s = n / 5, i = n % 5, m = n + NA, j = m / 10, u = (m / 5) & 1, im = m % 5, jo = j < 5 ? j : j - 5;
          if (L == 0 && n == 5 * KVS) { vmwait0(); ladd(CNT(C_KVC), lane_id()); }
          acc[i][0] = mfma(A[n % NA], B[2 * s], acc[i][0]);
          acc[i][1] = mfma(A[n % NA], B[2 * s + 1], acc[i][1]);
          SB();
          if (m == 50) { UNR for (int k = 0; k < 2; ++k) { b0[k] = opq(b0[k] + 5 * SLOT); b4[k] = opq(b4[k] + 5 * SLOT); } }
          if (m % 10 == 0 && m < 90 && (L == 0 || j == 5)) {
            const u32 need = L ? 11u : (u32)(j + 1);
            if (m - 5 >= NA) wp = umax(wp, rfl(fl));
            if (wp < need) wp = lwait(CNT(C_WPROG), need, dead);
          }
          if (m < 90) A[n % NA] = ld<u4>((im < 4 ? b0[u] + im * 2048 : b4[u]) + jo * SLOT);
          else if (m < 95) {
            if (L == 0 && m == 90) lwait(CNT(C_TBL), 2, dead);
            if (m == 90) q = lane_id() >> 4;
            cs[m >= 90 && m < 95 ? m - 90 : 0] = ld<f4>(L_CSL + (m - 90) * 64 + q * 16);
          }
          if (m % 10 == 5 && m < 80 && (L == 0 || j == 4)) fl = *(volatile LAS u32*)(uintptr_t)CNT(C_WPROG);
          if (L == 1 && (i == 1 || i == 3)) { vmx = vm(vmx, B[2 * s + (i >> 1)]); asm volatile("" : "+v"(vmx)); }
          if (L == 1 && s == 4 && i == 3) { svbad = anyl(vm(0, sv) == SENT); stats_from(sv, mS, mQ); }
          SB();
        });
        if (L == 0 || !anyl(vmx == SENT) || dead) break;
        if (pass >= 20000) { dead = 1; break; }
        FENCE();
        b_addr();
      }
      FENCE();
      lane = lane_id(); q = lane >> 4; i16 = lane & 15;
      ladd(CNT(C_GEM), lane);
      STAMPL(4);
      if (L == 1 && svbad) { sv = poll_stats(ws, stoff(), sv, dead); stats_from(sv, mS, mQ); }
      u32 ql[2];
      {
        const bool val = (i16 >> 2) == q;
        const u32 tk = L_TK + (val ? ((q & 1) * 4 + (i16 & 3)) * 16 : 128), tk2 = L_TK2 + (val ? ((q & 1) * 4 + (i16 & 3)) * 4 : 32);
        u4 t0[5], t1[5];
        u32 t2[5];
        u32 kv[2][9];
        UNR for (int i = 0; i < 5; ++i) {
          f2 c01 = {cs[i].x, cs[i].y}, c23 = {cs[i].z, cs[i].w};
          UNR for (int tb = 0; tb < 2; ++tb) {
            f2 lo = {acc[i][tb].x, acc[i][tb].y}, hi = {acc[i][tb].z, acc[i][tb].w};
            kv[tb][i < 4 ? 2 * i : 8] = pk((lo - c01 * mS[tb]) * mQ[tb]);
            if (i < 4) kv[tb][2 * i + 1] = pk((hi - c23 * mS[tb]) * mQ[tb]);
            else ql[tb] = pk((hi.x - c23.x * mS[tb]) * mQ[tb], 0.0f);
          }
          t0[i] = ld<u4>(tk + i * 288); t1[i] = ld<u4>(tk + i * 288 + 144); t2[i] = ld<u32>(tk2 + i * 36);
          SB();
        }
        u32 K[9], V[9];
        UNR for (int i = 0; i < 9; ++i) { auto t = SW32(kv[0][i], kv[1][i]); K[i] = t[0]; V[i] = t[1]; }
        const u4 k0 = {K[0], K[1], K[2], K[3]}, k1 = {K[4], K[5], K[6], K[7]}, k2 = {K[8], 0, 0, 0};
        f4 P[5];
        SB();
        UNR for (int m = 0; m < 5; ++m) P[m] = mfma(t0[m], k0, f4{0, 0, 0, 0});
        SB();
        UNR for (int m = 0; m < 5; ++m) P[m] = mfma(t1[m], k1, P[m]);
        SB();
        UNR for (int m = 0; m < 5; ++m) P[m] = mfma(u4{t2[m], 0, 0, 0}, k2, P[m]);
        SB();
        const u32 row = L_STG + (32 * w + 16 * (q >> 1) + i16) * 232;
        lwait(CNT(C_GEM), 4 * (L + 1), dead);
        {
          u32 vo = row + 144 + (q & 1) * 40;
          UNR for (int i = 0; i < 4; ++i) st<u2>(vo + 8 * i, u2{V[2 * i], V[2 * i + 1]});
          st<u2>(vo + 32, u2{V[8], 0x00003F80u});
        }
        SB();
        f2 z[9];
        UNR for (int i = 0; i < 9; ++i) z[i] = f2{P[i >> 1][2 * (i & 1)], P[i >> 1][2 * (i & 1) + 1]};
        float mx = fmaxf(z[0].x, z[0].y), mn = fminf(z[0].x, z[0].y);
        UNR for (int i = 1; i < 9; ++i) { mx = fmaxf(fmaxf(mx, z[i].x), z[i].y); mn = fminf(fminf(mn, z[i].x), z[i].y); }
        const float ma = -mx * LOG2E, mb = mn * LOG2E;
        f2 e1[9], e2[9], s1 = {0, 0}, s2 = {0, 0};
        UNR for (int i = 0; i < 9; ++i) {
          f2 t1 = z[i] * LOG2E + ma, t2 = mb - z[i] * LOG2E;
          e1[i] = f2{__builtin_amdgcn_exp2f(t1.x), __builtin_amdgcn_exp2f(t1.y)};
          e2[i] = f2{__builtin_amdgcn_exp2f(t2.x), __builtin_amdgcn_exp2f(t2.y)};
          s1 += e1[i]; s2 += e2[i];
        }
        const float r1 = __builtin_amdgcn_rcpf(s1.x + s1.y), r2 = __builtin_amdgcn_rcpf(s2.x + s2.y);
        u32 fo = row + (q & 1) * 72;
        UNR for (int i = 0; i < 4; ++i) {
          st<u2>(fo + 8 * i, u2{pk(e1[2 * i] * r1), pk(e1[2 * i + 1] * r1)});
          st<u2>(fo + 32 + 8 * i, u2{pk(e2[2 * i] * r2), pk(e2[2 * i + 1] * r2)});
        }
        st<u2>(fo + 64, u2{pk(e1[8] * r1), pk(e2[8] * r2)});
        FENCE();
        ladd(CNT(C_PHI), lane);
        lwait(CNT(C_PHI), 4 * (L + 1), dead);
        STAMPL(5);
        lane = lane_id(); q = lane >> 4; i16 = lane & 15;
        const int hd = w >> 1, db = w & 1;
        const u32 ta = L_STG + (16 * (q & 1) + 4 * (i16 >> 2) + (q >> 1)) * 232 + (i16 & 3) * 8;
        f4 Dd[3] = {f4{0, 0, 0, 0}, f4{0, 0, 0, 0}, f4{0, 0, 0, 0}};
        const u32 tf = ta + 72 * hd, tv = ta + 144 + 40 * hd + 32 * db;
        u4 bv[4], af[4][3];
        UNR for (int ks = 0; ks < 4; ++ks) {
          bv[ks] = trd2(tv + ks * 7424, tv + ks * 7424 + 464);
          UNR for (int fb = 0; fb < 3; ++fb) af[ks][fb] = trd2(tf + ks * 7424 + fb * 32, tf + ks * 7424 + fb * 32 + 464);
          if (ks & 1) SB();
          if (ks == 1) { UNR for (int fb = 0; fb < 3; ++fb) Dd[fb] = mfma(af[0][fb], bv[0], Dd[fb]); SB(); }
        }
        UNR for (int ks = 1; ks < 4; ++ks) UNR for (int fb = 0; fb < 3; ++fb) Dd[fb] = mfma(af[ks][fb], bv[ks], Dd[fb]);
        auto s0 = SW16(pk(Dd[0].x, Dd[0].y), pk(Dd[1].x, Dd[1].y));
        auto s1b = SW16(pk(Dd[0].z, Dd[0].w), pk(Dd[1].z, Dd[1].w));
        u4 o = {s0[0], s1b[0], s0[1], s1b[1]};
        const int d = 16 * db + i16;
        const u32 so = L * SP_L + g * SP_G + p * SP_P;
        if (L == 0) lwait(CNT(C_WOK), 1, dead);
        gst4(san(o), ws, d < 19 ? O_SP + (hd * 19 + d) * 64 + (q & 1) * 32 + (q >> 1) * 16 : 0x80000000u, so);
        gst2(u2{san(pk(Dd[2].x, Dd[2].y)), san(pk(Dd[2].z, Dd[2].w))}, ws, (d < 19 && q == 0) ? O_SP + 2432 + (hd * 19 + d) * 8 : 0x80000000u, so);
      }
      ladd(CNT(C_PUB), lane);
      STAMPL(6);
      u32 fq[2][9];
      u4 v[12];
      u32 off[3];
      auto sp_round = [&]() INL -> bool {
        u64 bad[4];
        UNR for (int pp = 0; pp < 4; ++pp) { bad[pp] = __builtin_amdgcn_ballot_w64(vm(vm(vm(0, v[pp]), v[4 + pp]), v[8 + pp]) == SENT); pins(bad[pp]); }
        if (!((bad[0] | bad[1]) | (bad[2] | bad[3]))) return false;
        UNR for (int pp = 0; pp < 4; ++pp)
          if (bad[pp]) { UNR for (int gi = 0; gi < 3; ++gi) v[4 * gi + pp] = gld4(ws, off[gi], L * SP_L + g * SP_G + 4 * pp * SP_P, 1); }
        return true;
      };
      {
        if (wp < 10) wp = lwait(CNT(C_WPROG), 10, dead);
        f4 aq[2][2];
        UNR for (int i = 0; i < 2; ++i) aq[i][0] = aq[i][1] = f4{0, 0, 0, 0};
        lane = lane_id(); q = lane >> 4; i16 = lane & 15;
        const u32 bq[2] = {(u32)opq(L_WQ + i16 * 128 + ((q ^ (i16 >> 1)) << 4)), (u32)opq(L_WQ + i16 * 128 + (((4 + q) ^ (i16 >> 1)) << 4))};
        const bool val = (i16 >> 3) == (q >> 1);
        const u32 tq = L_TQ + (val ? (q * 4 + (i16 & 3)) * 16 : 256), tq2 = L_TQ2 + (val ? (q * 4 + (i16 & 3)) * 4 : 64);
        u4 A[4], t0[5];
        u32 t1[5];
        f4 c5, c6;
        UNR for (int n = 0; n < 4; ++n) A[n] = ld<u4>(bq[n >> 1] + (n & 1) * 2048);
        SB();
        UNR for (int n = 0; n < 36; ++n) {
          const int s = n >> 1, i = n & 1, sn = s + 2;
          aq[i][0] = mfma(A[n & 3], B[2 * s], aq[i][0]);
          aq[i][1] = mfma(A[n & 3], B[2 * s + 1], aq[i][1]);
          SB();
          if (n < 32) A[n & 3] = ld<u4>(bq[sn & 1] + (sn >> 1) * 4096 + i * 2048);
          if (n == 30) { c5 = ld<f4>(L_CSL + 5 * 64 + q * 16); c6 = ld<f4>(L_CSL + 6 * 64 + q * 16); }
          if (n >= 31) { t0[n - 31] = ld<u4>(tq + (n - 31) * 272); t1[n - 31] = ld<u32>(tq2 + (n - 31) * 68); }
          SB();
        }
        STAMPL(7);
        UNR for (int gi = 0; gi < 3; ++gi) off[gi] = O_SP + q * SP_P + umin(16 * umin(w + 4 * gi, 10) + i16, 170) * 16;
        if (L == 0) lwait(CNT(C_FAOK), 1, dead);
        UNR for (int i = 0; i < 12; ++i) v[4 * (i % 3) + i / 3] = gld4(ws, off[i % 3], L * SP_L + g * SP_G + 4 * (i / 3) * SP_P, 1);
        SB();
        u4 q0[2];
        UNR for (int tb = 0; tb < 2; ++tb) {
          q0[tb].x = pk((f2{aq[0][tb].x, aq[0][tb].y} - f2{c5.x, c5.y} * mS[tb]) * mQ[tb]);
          q0[tb].y = pk((f2{aq[0][tb].z, aq[0][tb].w} - f2{c5.z, c5.w} * mS[tb]) * mQ[tb]);
          q0[tb].z = pk((f2{aq[1][tb].x, aq[1][tb].y} - f2{c6.x, c6.y} * mS[tb]) * mQ[tb]);
          q0[tb].w = pk((f2{aq[1][tb].z, aq[1][tb].w} - f2{c6.z, c6.w} * mS[tb]) * mQ[tb]);
        }
        f4 P[2][5];
        SB();
        UNR for (int tb = 0; tb < 2; ++tb) {
          UNR for (int m = 0; m < 5; ++m) P[tb][m] = mfma(u4{t1[m], 0, 0, 0}, u4{ql[tb], 0, 0, 0}, f4{0, 0, 0, 0});
          SB();
          UNR for (int m = 0; m < 5; ++m) P[tb][m] = mfma(t0[m], q0[tb], P[tb][m]);
          SB();
        }
        const float ls = (q & 1) ? -LOG2E : LOG2E;
        UNR for (int tb = 0; tb < 2; ++tb) {
          if (tb == 0 || GR3) {
            UNR for (int m = 0; m < 5; ++m) { pin(P[0][m]); pin(P[1][m]); }
            FENCE();
            if (tb == 0) STAMPD(18 + 4 * L);
            bool bd = sp_round();
            FENCE();
            if (tb == 0) STAMPD(19 + 4 * L);
          }
          f2 z[9];
          UNR for (int i = 0; i < 9; ++i) z[i] = f2{P[tb][i >> 1][2 * (i & 1)], P[tb][i >> 1][2 * (i & 1) + 1]} * ls;
          float mx = fmaxf(z[0].x, z[0].y);
          UNR for (int i = 1; i < 9; ++i) mx = fmaxf(fmaxf(mx, z[i].x), z[i].y);
          f2 s1 = {0, 0};
          UNR for (int i = 0; i < 9; ++i) { f2 t = z[i] - mx; z[i] = f2{__builtin_amdgcn_exp2f(t.x), __builtin_amdgcn_exp2f(t.y)}; s1 += z[i]; }
          const float r1 = __builtin_amdgcn_rcpf(s1.x + s1.y);
          UNR for (int i = 0; i < 9; ++i) { fq[tb][i] = pk(z[i] * r1); pin(fq[tb][i]); }
        }
      }
      STAMPL(8);
      lane = lane_id(); q = lane >> 4; i16 = lane & 15;
      u32 m01, m2, l01, l2, oo, lo_;
      {
        const bool val = (i16 >> 3) == (q >> 1);
        const int d01 = (q >> 1) * 19 + 8 * ((i16 >> 2) & 1) + (i16 & 3), d2 = (q >> 1) * 19 + 16 + ((i16 & 3) < 2 ? (i16 & 3) : 2);
        m01 = val ? L_STB + d01 * 64 + (q & 1) * 32 : L_ZERO; m2 = val ? L_STB + d2 * 64 + (q & 1) * 32 : L_ZERO;
        l01 = val ? L_STB + 2432 + d01 * 8 + (q & 1) * 4 : L_ZERO; l2 = val ? L_STB + 2432 + d2 * 8 + (q & 1) * 4 : L_ZERO;
        oo = O_OB + lane * 16 + L * OB_L + p * OB_P + 2 * w * 16384 + g * 1024;
        lo_ = (q < 2 && !(i16 & 1)) ? O_OB + 131072 + (16 * q + i16) * 8 + L * OB_L + p * OB_P + g * 1024 + w * 256 : 0x80000000u;
        pin(m01); pin(m2); pin(l01); pin(l2); pin(oo); pin(lo_);
      }
      u32 fb = tag;
      u32 fbo = O_FB + wg_of(p, i16) * 128;
      if (L == 0) pin(fbo);
      {
        u4 sel;
        UNR for (int d = 0; d < 4; ++d) sel[d] = ((i16 & 7) >> 1) == d ? ((i16 & 1) ? 0x3F800000u : 0x00003F80u) : 0u;
        u32 sa[3];
        UNR for (int gi = 0; gi < 3; ++gi) { sa[gi] = L_STB + (16 * umin(w + 4 * gi, 10) + i16) * 16 + q * 8; pin(sa[gi]); }
        pin(sel);
        STAMPD(21 + 5 * L);
        NOUNR for (int it = dead ? SPIN_G - 1 : 0;; ++it) {
          FENCE();
          if (!sp_round()) break;
          if (it >= SPIN_G) { dead = 1; break; }
        }
        FENCE();
        STAMPL(9);
        f4 s[3];
        UNR for (int pp = 0; pp < 4; ++pp) UNR for (int gi = 0; gi < 3; ++gi) s[gi] = mfma(sel, v[4 * gi + pp], pp ? s[gi] : f4{0, 0, 0, 0});
        SB();
        if (q < 2) { UNR for (int gi = 0; gi < 3; ++gi) st<u2>(sa[gi], u2{pk(s[gi].x, s[gi].y), pk(s[gi].z, s[gi].w)}); }
        FENCE();
        ladd(CNT(C_STB), lane);
      }
      if (L == 0) fb = gld1(ws, fbo, 0, 1);
      u32 clr = 4;
      if (L == 0) clr = *(volatile LAS u32*)(uintptr_t)CNT(C_CLR);
      lwait(CNT(C_STB), 4 * (L + 1), dead);
      STAMPL(10);
      {
        f4 O[2][3];
        u4 t0[3], t1[3];
        u32 t2[3];
        UNR for (int k = 0; k < 3; ++k) { const int m = (k + 2) % 3; t0[m] = ld<u4>(m < 2 ? m01 + m * 256 : m2); }
        UNR for (int k = 0; k < 3; ++k) { const int m = (k + 2) % 3; t1[m] = ld<u4>((m < 2 ? m01 + m * 256 : m2) + 16); }
        UNR for (int k = 0; k < 3; ++k) { const int m = (k + 2) % 3; t2[m] = ld<u32>(m < 2 ? l01 + m * 32 : l2); }
        SB();
        UNR for (int k = 0; k < 3; ++k) UNR for (int tb = 0; tb < 2; ++tb) { const int m = (k + 2) % 3; O[tb][m] = mfma(t0[m], u4{fq[tb][0], fq[tb][1], fq[tb][2], fq[tb][3]}, f4{0, 0, 0, 0}); }
        SB();
        UNR for (int k = 0; k < 3; ++k) UNR for (int tb = 0; tb < 2; ++tb) { const int m = (k + 2) % 3; O[tb][m] = mfma(t1[m], u4{fq[tb][4], fq[tb][5], fq[tb][6], fq[tb][7]}, O[tb][m]); }
        SB();
        UNR for (int k = 0; k < 3; ++k) UNR for (int tb = 0; tb < 2; ++tb) { const int m = (k + 2) % 3; O[tb][m] = mfma(u4{t2[m], 0, 0, 0}, u4{fq[tb][8], 0, 0, 0}, O[tb][m]); }
        SB();
        u32 lo[2];
        if (L == 0 && rfl(clr) < 4) lwait(CNT(C_CLR), 4, dead);
        UNR for (int tb = 0; tb < 2; ++tb) {
          const float inv = __builtin_amdgcn_rcpf(O[tb][2].z + 1e-6f);
          u4 o = {pk(f2{O[tb][0].x, O[tb][0].y} * inv), pk(f2{O[tb][0].z, O[tb][0].w} * inv), pk(f2{O[tb][1].x, O[tb][1].y} * inv), pk(f2{O[tb][1].z, O[tb][1].w} * inv)};
          gst4(san(o), ws, oo, tb * 16384);
          lo[tb] = pk(f2{O[tb][2].x, O[tb][2].y} * inv);
        }
        const u32 lv = san((q & 1) ? lo[1] : lo[0]);
        auto hh = SW32(lv, lv);
        const u32 n0 = __builtin_amdgcn_update_dpp(0u, hh[0], 0xB1, 0xF, 0xF, true), n1 = __builtin_amdgcn_update_dpp(0u, hh[1], 0xB1, 0xF, 0xF, true);
        gst4(u4{hh[0], hh[1], n0, n1}, ws, lo_, 0);
        ladd(CNT(C_PUB), lane);
      }
      STAMPL(11);
      lane = lane_id(); q = lane >> 4; i16 = lane & 15;
      u4 wo[18];
      Keep<false> kp;
      if (w < 2) {
        UNR for (int i = 0; i < 18; ++i) wo[i] = gld4(ws, O_WOT + lane * 16, (9 * c + 8) * 18432 + i * 1024, 1);
        if (L == 0) kp.x[0] = gld2(mkrs(a.x, 2359296), (32 * rt + i16) * 1152 + (144 * c + 4 * q) * 2, 256 + w * 16 * 1152, 0);
        else { u4 t = ld<u4>(L_KEEP + w * 1024 + lane * 16); kp.x[0] = u2{t.x, t.y}; kp.y[0] = u2{t.z, t.w}; }
      }
      {
        const u32 pb = L * OB_L + p * OB_P, tbg = 2 * (rt & 3) + (w >> 1);
        if (L == 0 && anyl(fb != tag)) poll(ws, fbo, tag, dead);
        auto o_ld = [&](int i) INL {
          if (i < 8) v[i] = gld4(ws, O_OB + lane * 16, pb + tbg * 16384 + (8 * (w & 1) + i) * 1024, 1);
          else {
            u32 o = O_OB + 131072 + q * 2048 + i16 * 8, so = pb + (w & 1) * 8192 + tbg * 128;
            u2 l0 = gld2(ws, o, so, 1), l1 = gld2(ws, o + 1024, so, 1);
            v[8] = u4{l0.x, l0.y, l1.x, l1.y};
          }
        };
        FENCE();
        UNR for (int i = 0; i < 9; ++i) o_ld(i);
        NOUNR for (int it = dead ? SPIN_G - 1 : 0;; ++it) {
          FENCE();
          u64 bad[9], any = 0;
          UNR for (int i = 0; i < 9; ++i) { bad[i] = __builtin_amdgcn_ballot_w64(vm(0, v[i]) == SENT); pins(bad[i]); any |= bad[i]; }
          if (!any) break;
          UNR for (int i = 0; i < 9; ++i) if (bad[i]) o_ld(i);
          if (it >= SPIN_G) { dead = 1; break; }
        }
        FENCE();
        STAMPL(12);
        lwait(CNT(C_PUB), 8 * L + 8, dead);
        if (L == 0) lwait(CNT(C_QA), 4, dead);
        const u32 sb = L_STG + (w >> 1) * 18432 + lane * 16;
        UNR for (int i = 0; i < 8; ++i) st<u4>(sb + (8 * (w & 1) + i) * 1024, v[i]);
        st<u4>(sb + (16 + (w & 1)) * 1024, v[8]);
      }
      if (w >= 2) { bar(); STAMPL(13); }
      if (w < 2) {
        tail<false, L>(a, ws, w, rt, c, tag, wo, kp, dead STPASS);
        if (L == 0) st<u4>(L_KEEP + w * 1024 + lane_id() * 16, u4{kp.x[0].x, kp.x[0].y, kp.y[0].x, kp.y[0].y});
      }
      else if (L == 0 && w == 2) {
        lwait(CNT(C_G2), 6, dead);
        UNR for (int j = 5; j < 9; ++j) w_slot(ws, j, rot, g, lane);
        vmwait0();
        lset(CNT(C_WPROG), 11);
      }
    }
}

__global__ void __launch_bounds__(512, 2) fused_kernel(Args a) {
  const int tid = threadIdx.x, lane = tid & 63, w = rfl(tid >> 6), b = blockIdx.x;
  u64 pc0;
  u32 clen;
  asm volatile("s_getpc_b64 %0\n.Lpf_here:\n\ts_mov_b32 %1, fused_code_end-.Lpf_here" : "=s"(pc0), "=s"(clen));
  clen = clen < 131072 ? clen : 0;
  u32 cpf = gld1(mkrs((const void*)pc0, clen), (((b >> 3) & 1) * 8 + w) * 8192 + lane * 128, 0, 0);
  FENCE();
  asm volatile("" : "+s"(a.x), "+s"(a.wqkv), "+s"(a.wphi), "+s"(a.wo), "+s"(a.bk), "+s"(a.bv), "+s"(a.sk), "+s"(a.ssm), "+s"(a.dv), "+s"(a.out), "+s"(a.ws));
  const int q = lane >> 4, i16 = lane & 15;
  const int xcd = b & 7, r = b >> 3;
  const int p = 4 * (xcd >> 1) + (r >> 3), g = 8 * (xcd & 1) + (r & 7), rot = ((r & 7) + 2 * (r >> 3)) % 9;
  const int rt = 4 * p + (g >> 2), c = g & 3;
  rsrc_t ws = mkrs(a.ws, WS_BYTES);
  int dead = 0;
  STAMP(0); STAMPC(29);
  u32 tag = *(const CAS u32*)a.ws;
#define BAR0() do { bar(); asm volatile("" : "+s"(tag)); tag += 1; } while (0)

  if (w < 4) {
    __builtin_amdgcn_s_setprio(2);
    u4 B[36];
    float mS[2], mQ[2];
    {
      rsrc_t rx = mkrs(a.x, 2359296);
      u32 vo[2];
      UNR for (int par = 0; par < 2; ++par) vo[par] = (8 * par + (lane >> 3)) * 1152 + (((lane & 7) ^ (4 * par + (lane >> 4))) << 4);
      auto x_step = [&](int j) {
        int kr = j + rot; kr -= kr >= 9 ? 9 : 0;
        UNR for (int rg = 0; rg < 4; ++rg) dma<0>(rx, (j & 7) * 16384 + w * 4096 + rg * 1024, vo[rg & 1], (128 * p + 32 * w + 16 * (rg >> 1)) * 1152 + kr * 128);
      };
      const u4 sw = kv_w(a, tid, c);
      SB();
      STAMPD(26);
      BAR0();
      STAMPD(27);
      SB();
      UNR for (int j = 0; j < XLA; ++j) x_step(j);
      FENCE();
      SB();
      STAMP(1);
      u32 a0 = w * 4096 + (i16 >> 3) * 1024 + (i16 & 7) * 128 + ((q ^ (i16 >> 1)) << 4), a1 = a0 ^ 64, pa = CNT(C_PROG) + 4 * w;
      f4 gS[2] = {f4{0, 0, 0, 0}, f4{0, 0, 0, 0}}, gQ[2] = {f4{0, 0, 0, 0}, f4{0, 0, 0, 0}};
      const u4 ones = {0x3F803F80u, 0x3F803F80u, 0x3F803F80u, 0x3F803F80u};
#define X0STEP(j)                                                                                                                      \
  {                                                                                                                                    \
    u32 pv = j + 1;                                                                                                                    \
    asm volatile("s_waitcnt vmcnt(%8)\n\tds_read_b128 %0, %4\n\tds_read_b128 %1, %4 offset:2048\n\tds_read_b128 %2, %5\n\t"            \
                 "ds_read_b128 %3, %5 offset:2048\n\ts_waitcnt lgkmcnt(0)\n\tds_write_b32 %6, %7"                                       \
                 : "=&v"(B[4 * j]), "=&v"(B[4 * j + 1]), "=&v"(B[4 * j + 2]), "=&v"(B[4 * j + 3])                                        \
                 : "v"(a0 + (j & 7) * 16384), "v"(a1 + (j & 7) * 16384), "v"(pa), "v"(pv), "n"(4 * ((j + XLA - 1 < 8 ? j + XLA - 1 : 8) - j))                                                 \
                 : "memory");                                                                                                          \
    if (j + XLA < 9) { x_step(j + XLA); FENCE(); }                                                                                                \
    UNR for (int i = 0; i < 4; ++i) { gS[i & 1] = mfma(ones, B[4 * j + i], gS[i & 1]); gQ[i & 1] = mfma(B[4 * j + i], B[4 * j + i], gQ[i & 1]); } \
  }
      X0STEP(0)
      pin(cpf);
      X0STEP(1) X0STEP(2) X0STEP(3) X0STEP(4) X0STEP(5) X0STEP(6) X0STEP(7) X0STEP(8)
      STAMP(2);
      UNR for (int tb = 0; tb < 2; ++tb) {
        float d = (i16 & 2) ? ((i16 & 1) ? gQ[tb].w : gQ[tb].z) : ((i16 & 1) ? gQ[tb].y : gQ[tb].x);
        d = (q == (i16 >> 2)) ? d : 0.0f;
        d = sw16add(d, d); d = sw32add(d, d);
        f2 t = ln_stats(gS[tb].x, d);
        mS[tb] = t.x; mQ[tb] = t.y;
      }
      lwait(CNT(C_KVL), 2, dead);
      STAMPD(28);
      KVin in;
      kv_read_asm(tid, in);
      ladd(CNT(C_KVR), lane);
      kv_compute(ws, tid, rt, c, in, sw, true);
    }
    u32 wp = 0;
    main_layer<0>(a, ws, b, w, p, g, rt, c, rot, tag, B, mS, mQ, wp, dead STPASS);
    main_layer<1>(a, ws, b, w, p, g, rt, c, rot, tag, B, mS, mQ, wp, dead STPASS);
  } else {
    const int pr = (w - 4) >> 1, e = w & 1;
    const u32 lH = pr ? L_H1 : L_H0, lU = pr ? L_U1 : L_U0;
    if (w == 4) {
      const int cl = b % 27, kr0 = b / 27;
      if (b < 243) {
        rsrc_t rw = mkrs(a.wqkv, 576 * 3456);
        UNR for (int rg = 0; rg < 8; ++rg)
          dma<0>(rw, L_TILE + rg * 1024, ((lane >> 3) ^ (rg & 1)) * 3456 + (((lane & 7) ^ (4 * ((rg >> 1) & 1))) << 4), (64 * kr0 + 8 * rg) * 3456 + cl * 128);
      }
      FENCE();
      SB();
      STAMPD(26);
      BAR0();
      STAMPD(27);
      SB();
      __builtin_amdgcn_s_setprio(3);
      if (b < 243) {
        const int cg = lane >> 3, rg = lane & 7, sec = cl / 9;
        u32 dst[8];
        UNR for (int j = 0; j < 8; ++j) {
          int cc = 64 * (cl - 9 * sec) + 8 * cg + j, h = cc / 18, d = cc - 18 * h, hl = h & 1;
          int hi = d >= 9, qq = sec ? 2 * (sec - 1) + hl : 2 * hl + hi, s = sec ? d : d - 9 * hi;
          bool left = s >= (sec ? 16 : 8);
          int row = left ? 64 + 3 * qq + (sec ? s - 16 : 2) : 16 * (s >> 2) + 4 * qq + (s & 3);
          u32 o = (sec == 0 && !left) ? L_WQ + kr0 * 4096 : kr0 * SLOT;
          dst[j] = O_WT + (h >> 1) * WT_G + o + row * 128 + ((rg ^ ((row >> 1) & 7)) << 4);
        }
        SB();
        vmwait0();
        pin(cpf);
        STAMPD(28);
        u4 R[8];
        const u32 rb = L_TILE + rg * 1024 + ((cg ^ (4 * ((rg >> 1) & 1))) << 4);
        UNR for (int i = 0; i < 8; ++i) R[i] = ld<u4>(rb + ((i ^ (rg & 1)) << 7));
        FENCE();
        lset(CNT(C_WT), 1);
        u4 o[8];
        UNR for (int j = 0; j < 8; ++j) {
          UNR for (int d = 0; d < 4; ++d) o[j][d] = __builtin_amdgcn_perm(R[2 * d + 1][j >> 1], R[2 * d][j >> 1], (j & 1) ? 0x07060302u : 0x05040100u);
          gst4(o[j], ws, dst[j], 0);
        }
        SB();
        float sel = 0;
        UNR for (int j = 0; j < 8; ++j) {
          float v = dot2(o[j].x, 0x3F803F80u, dot2(o[j].y, 0x3F803F80u, 0.0f)) + dot2(o[j].z, 0x3F803F80u, dot2(o[j].w, 0x3F803F80u, 0.0f));
          v += __uint_as_float(__builtin_amdgcn_update_dpp(0, __float_as_uint(v), 0xB1, 0xF, 0xF, true));
          v += __uint_as_float(__builtin_amdgcn_update_dpp(0, __float_as_uint(v), 0x4E, 0xF, 0xF, true));
          v += __uint_as_float(__builtin_amdgcn_update_dpp(0, __float_as_uint(v), 0x141, 0xF, 0xF, true));
          sel = rg == j ? v : sel;
        }
        gst1(__float_as_uint(sel), ws, O_CS + lane * 4, kr0 * 6912 + cl * 256);
        vmwait0();
      } else {
        pin(cpf);
        lset(CNT(C_WT), 1);
      }
      if (b < 243) setflag(ws, O_FW + (cl * 9 + kr0) * 128, tag, lane);
      STAMP(1);
      auto need_x = [&]() {
      NOUNR for (int i = dead ? SPIN_L : 0; i < SPIN_L; ++i) {
        u4 v;
        asm volatile("ds_read_b128 %0, %1\n\ts_waitcnt lgkmcnt(0)" : "=v"(v) : "v"(CNT(C_PROG)) : "memory");
        if (rfl(umin(umin(v.x, v.y), umin(v.z, v.w))) >= 9) break;
        if (i == SPIN_L - 1) dead = 1;
        __builtin_amdgcn_s_sleep(1);
      }
      };
      if (PLATE) need_x();
      {
        int l6 = lane < 54 ? lane : 0, kr = l6 / 6, i = l6 - 6 * kr;
        int ln = 9 * (i >> 1) + ((36 * g + 35 * (i & 1)) >> 6);
        poll(ws, O_FW + (ln * 9 + kr) * 128, tag, dead);
      }
      STAMP(2);
      u32 cv[2][9];
      UNR for (int rd = 0; rd < 2; ++rd) {
        int blk = rd * 4 + q, qq = i16 >> 2, ee = i16 & 3;
        int kvc = 576 + 576 * (qq >> 1) + 36 * g + 18 * (qq & 1), qc = 36 * g + 9 * qq;
        int col = rd == 0 ? kvc + 4 * blk + ee : (blk == 4 ? (ee < 2 ? kvc + 16 + ee : qc + 8) : qc + 4 * ((blk - 5) & 1) + ee);
        UNR for (int k = 0; k < 9; ++k) asm volatile("buffer_load_dword %0, %1, %2, %3 offen sc1" : "=v"(cv[rd][k]) : "v"(O_CS + col * 4), "s"(ws), "s"(k * 6912) : "memory");
      }
      if (!PLATE) need_x();
      UNR for (int j = 0; j < 9; ++j) {
        w_slot(ws, j, rot, g, lane);
        if (j == 3) {
          asm volatile("s_waitcnt vmcnt(40)"
                       : "+v"(cv[0][0]), "+v"(cv[0][1]), "+v"(cv[0][2]), "+v"(cv[0][3]), "+v"(cv[0][4]), "+v"(cv[0][5]), "+v"(cv[0][6]), "+v"(cv[0][7]), "+v"(cv[0][8]),
                         "+v"(cv[1][0]), "+v"(cv[1][1]), "+v"(cv[1][2]), "+v"(cv[1][3]), "+v"(cv[1][4]), "+v"(cv[1][5]), "+v"(cv[1][6]), "+v"(cv[1][7]), "+v"(cv[1][8])
                       :
                       : "memory");
          lwait(CNT(C_TBL), 1, dead);
          UNR for (int rd = 0; rd < 2; ++rd) {
            float s = 0;
            UNR for (int k = 0; k < 9; ++k) s += __uint_as_float(cv[rd][k]);
            lset((rd == 0 || q < 3) ? L_CSL + rd * 256 + lane * 4 : CNT(31), __float_as_uint(s));
          }
          lset(CNT(C_TBL), 2);
        }
        if (j >= 4) { vmwait<40>(); lset(CNT(C_WPROG), j - 3); if (j == 4) STAMP(8); if (j == 6) STAMP(9); if (j == 8) STAMP(10); }
        if (j == 3) STAMP(7);
      }
      UNR for (int j = 0; j < 9; ++j) {
        int kr = j + rot; kr -= kr >= 9 ? 9 : 0;
        UNR for (int i = 0; i < 4; ++i) dma<SC1>(ws, L_WQ + j * 4096 + i * 1024, O_WT + L_WQ + lane * 16, g * WT_G + kr * 4096 + i * 1024);
        if (j == 1) { vmwait<38>(); lset(CNT(C_WPROG), 6); }
        if (j == 3) { vmwait<36>(); lset(CNT(C_WPROG), 7); }
        if (j == 6) { vmwait<38>(); lset(CNT(C_WPROG), 8); }
      }
      vmwait<36>(); lset(CNT(C_WPROG), 9);
      STAMP(12);
      vmwait0();
      lset(CNT(C_WPROG), 10);
      __builtin_amdgcn_s_setprio(0);
      STAMP(3);
    } else if (w < 7) {
      const int chn = 2 * b + (w - 5);
      const u32 mH = w == 5 ? L_H0 : L_H1, mU = w == 5 ? L_U0 : L_U1;
      auto kv_dma = [&]() {
        const int r3 = lane / 18;
        const u32 vo = r3 * 1152 + (lane - 18 * r3) * 16 + 288 * c;
        if (w == 5) {
          rsrc_t rk = mkrs(a.bk, 2359296);
          if (lane < 54) {
            dma<0>(rk, L_KT, rt ? vo + (32 * rt - 3) * 1152 : 0x80000000u, 0);
            UNR for (int i = 1; i < 11; ++i) dma<0>(rk, L_KT + i * 864, vo, (32 * rt - 3 + 3 * i) * 1152);
          }
          if (lane < 36) dma<0>(rk, L_KT + 11 * 864, vo, (32 * rt + 30) * 1152);
        } else {
          rsrc_t rv = mkrs(a.bv, 2359296);
          if (lane < 54) { UNR for (int i = 0; i < 10; ++i) dma<0>(rv, L_VT + i * 864, vo, (32 * rt + 3 * i) * 1152); }
          if (lane < 36) dma<0>(rv, L_VT + 10 * 864, vo, (32 * rt + 30) * 1152);
        }
      };
      FENCE();
      SB();
      STAMPD(26);
      BAR0();
      STAMPD(27);
      SB();
      lwait(CNT(C_PROG) + 4 * (w - 5), KVAT, dead);
      kv_dma();
      STAMP(9);
      vmwait0();
      pin(cpf);
      ladd(CNT(C_KVL), lane);
      STAMP(10);
      lwait(CNT(C_WPROG), HAT, dead);
      Hin hin;
      h_load(a, chn, lane, hin);
      FENCE();
      if (w == 5) {
        lwait(CNT(C_KVC), 5, dead);
        setflag(ws, O_FK + (c * 64 + rt) * 128, tag, lane);
        STAMP(7);
      }
      lwait(CNT(C_KVR), 5, dead);
      h_stage(mH, mU, lane, hin);
      FENCE();
      ladd(CNT(C_PU0), lane);
      STAMP(8);
      if (w == 6) {
        lwait(CNT(C_WPROG), PAT, dead);
        const u32 f0 = O_FK + ((2 * b) / 144 * 64 + lane) * 128, f1 = O_FK + (192 + lane) * 128;
        const u32 v0 = gld1(ws, f0, 0, 1), v1 = gld1(ws, f1, 0, 1);
        if (anyl(v0 != tag)) poll(ws, f0, tag, dead);
        FENCE();
        STAMP(2);
        u_stage(ws, L_U0, 2 * b, lane);
        u_stage(ws, L_U1, 2 * b + 1, lane);
        if (anyl(v1 != tag)) { poll(ws, f1, tag, dead); }
        FENCE();
        lwait(CNT(C_PU0), 3, dead);
        u_stage(ws, L_U2, 512 + (b >> 2), lane);
        vmwait<4>();
        lset(CNT(C_PU1), 1);
        STAMP(3);
      }
    } else {
      if (lane < 32) lset(L_CNT + lane * 4, 0);
      const int cl = b % 9, gg = b / 9;
      rsrc_t rp = mkrs(a.wphi, 32 * 648);
      u4 p0 = gld4(rp, lane * 16, g * 1296, 0), p1 = gld4(rp, lane < 17 ? 1024 + lane * 16 : 0x80000000u, g * 1296, 0);
      const u4 sw = kv_w(a, 256 + (lane & 31), c);
      const u4 s4 = {SENT, SENT, SENT, SENT};
      UNR for (int i = 0; i < 3; ++i) gst4(s4, ws, (i < 2 || lane < 48) ? O_SP + i * 1024 + lane * 16 : 0x80000000u, g * SP_G + p * SP_P);
      FENCE();
      SB();
      STAMPD(26);
      BAR0();
      STAMPD(27);
      SB();
      st<u4>(L_PT + lane * 16, p0);
      if (lane < 17) st<u4>(L_PT + 1024 + lane * 16, p1);
      FENCE();
      auto wp16 = [&](int hd, int d, int f) -> u32 { u32 v = ld<u16>(L_PT + (hd * 324 + d * 18 + (f < 18 ? f : 0)) * 2); return f < 18 ? v : 0u; };
      UNR for (int rd = 0; rd < 2; ++rd) {
        int t = rd * 64 + lane, tt = t < 80 ? t : 79, m = tt >> 4, cc = (tt >> 3) & 1, hd = (tt >> 2) & 1, f = 4 * m + (tt & 3);
        u4 v;
        UNR for (int d = 0; d < 4; ++d) v[d] = wp16(hd, 8 * cc + 2 * d, f) | (wp16(hd, 8 * cc + 2 * d + 1, f) << 16);
        st<u4>(L_TK + (tt >> 3) * 144 + (tt & 7) * 16, v);
        int hq = (tt >> 3) & 1, dh = (tt >> 2) & 1;
        UNR for (int d = 0; d < 4; ++d) v[d] = wp16(hq, 9 * dh + 2 * d, f) | (wp16(hq, 9 * dh + 2 * d + 1, f) << 16);
        st<u4>(L_TQ + m * 272 + (tt & 15) * 16, v);
        st<u32>(L_TQ2 + m * 68 + (tt & 15) * 4, wp16(hq, 9 * dh + 8, f));
      }
      {
        int tt = lane < 40 ? lane : 39, m = tt >> 3, hd = (tt >> 2) & 1, f = 4 * m + (tt & 3);
        st<u32>(L_TK2 + m * 36 + (tt & 7) * 4, wp16(hd, 16, f) | (wp16(hd, 17, f) << 16));
        u4 z = {0, 0, 0, 0};
        if (lane < 10) st<u4>(L_TK + lane * 144 + 128, z);
        if (lane < 5) { st<u4>(L_TQ + lane * 272 + 256, z); st<u32>(L_TK2 + lane * 36 + 32, 0u); st<u32>(L_TQ2 + lane * 68 + 64, 0u); }
      }
      FENCE();
      lset(CNT(C_TBL), 1);
      vmwait0();
      pin(cpf);
      lset(CNT(C_WOK), 1);
      setflag(ws, O_FA + b * 128, tag, lane);
      STAMP(7);
      lwait(CNT(C_WT), 1, dead);
      lwait(CNT(C_PROG) + 8, KVAT, dead);
      if (b < 144) {
        rsrc_t rw = mkrs(a.wo, 576 * 1152);
        UNR for (int m = 0; m < 5; ++m)
          if (m < 4 || lane < 32) dma<0>(rw, L_TILE + m * 1024, (lane >> 3) * 1152 + (lane & 7) * 16, (36 * gg + 8 * m) * 1152 + cl * 128);
      }
      vmwait0();
      lwait(CNT(C_KVL), 2, dead);
      {
        KVin in;
        kv_read(256 + (lane & 31), in);
        FENCE();
        ladd(CNT(C_KVR), lane);
        kv_compute(ws, 256 + (lane & 31), rt, c, in, sw, lane < 32);
      }
      if (b < 144) {
        const u32 ta = L_TILE + (18 * (q >> 1) + 8 * (q & 1) + (i16 >> 2)) * 128 + (i16 & 3) * 8;
        UNR for (int G = 0; G < 4; ++G) gst4(trd2(ta + G * 32, ta + G * 32 + 512), ws, O_WOT + lane * 16, (4 * cl + G) * 18432 + gg * 1024);
        u2 lf = trd(L_TILE + (16 + ((i16 >> 2) & 1) + 18 * (i16 >> 3)) * 128 + q * 32 + (i16 & 3) * 8);
        gst2(lf, ws, O_WOT + q * 18432 + i16 * 16, 4 * cl * 18432 + (16 + (gg >> 3)) * 1024 + ((gg >> 1) & 3) * 256 + (gg & 1) * 8);
      }
      FENCE();
      vmwait0();
      ladd(CNT(C_KVC), lane);
      setflag(ws, O_FO + b * 128, tag, lane);
      STAMP(8);
      if (lane < 32) st<u4>(lane < 2 ? L_H2 + lane * 16 : L_ZERO + (lane - 2) * 16, u4{0, 0, 0, 0});
      FENCE();
      lwait(CNT(C_WPROG), HAT, dead);
      UNR for (int m = 0; m < 4; ++m) dma<0>(mkrs(a.ssm, 2359296), L_H2 + 32 + m * 1024, lane * 16, (512 + (b >> 2)) * 4096 + m * 1024);
      lwait(CNT(C_WPROG), FAT, dead);
      poll(ws, lane < 16 ? O_FA + wg_of(lane, g) * 128 : O_FO + (i16 * 9 + 2 * c + q - 1) * 128, tag, dead);
      lset(CNT(C_FAOK), 1);
      ladd(CNT(C_PU0), lane);
      STAMP(3);
    }
    u4 wo[36];
    auto wo_load = [&]() { UNR for (int i = 0; i < 36; ++i) wo[i] = gld4(ws, O_WOT + lane * 16, (9 * c + 2 * (w - 4)) * 18432 + i * 1024, 1); };
    lwait(CNT(C_FAOK), 1, dead);
    if (w != 6) wo_load();
    FENCE();
    const float dv = load_dv(a, 2 * b + pr), dq = load_dv(a, 512 + (b >> 2));
    STAMP(11);
    lwait(CNT(C_PU0), 3, dead);
    lwait(CNT(C_PU1), 1, dead);
    STAMP(4);
    late_fill(ws, w - 4, p, g, rt, c, lane);
    FENCE();
    {
      f4 keep[4];
      conv_pair(ws, lH, lU, w - 4, pr ? CNT(C_PB1) : CNT(C_PB0), O_FB + b * 128, tag, keep, lane, dead STPASS);
      STAMP(5);
      lwait(CNT(C_PU1), 2, dead);
      conv_quarter(b & 3, w - 4, lane);
      STAMPD(22);
      conv_pair_out(ws, lU, 2 * b + pr, w - 4, pr ? CNT(C_PB1) : CNT(C_PB0), dv, keep, lane, dead);
      STAMPD(23);
    }
    if (w < 6) conv_quarter_out(ws, 512 + (b >> 2), b & 3, w - 4, dq, lane, dead);
    FENCE();
    ladd(CNT(C_QA), lane);
    STAMPD(24);
    vmwait0();
    if (ladd_rtn(CNT(C_FYC), lane) == 3) setflag(ws, O_FY + b * 128, tag, lane);
    STAMP(6);
    if (w == 6) wo_load();
    __builtin_amdgcn_s_setprio(3);
    Keep<true> kp;
    tail<true, 0>(a, ws, w, rt, c, tag, wo, kp, dead STPASS);
    tail<true, 1>(a, ws, w, rt, c, tag, wo, kp, dead STPASS);
    if (b == 0 && w == 7 && lane_id() == 0) gst1(tag, ws, 0, 0);
  }
  STAMP(31); STAMPC(30);
}

extern "C" __device__ __attribute__((used, noinline)) void fused_code_end() {}

extern "C" void kernel_launch(void* const* d_in, const int* in_sizes, int n_in, void* d_out, int out_size, void* d_ws, size_t ws_size, hipStream_t stream) {
  Args a;
  a.x = (const u16*)d_in[0]; a.wqkv = (const u16*)d_in[1]; a.wphi = (const u16*)d_in[2]; a.wo = (const u16*)d_in[3];
  a.bk = (const u16*)d_in[5]; a.bv = (const u16*)d_in[6]; a.sk = (const u16*)d_in[7]; a.ssm = (const u16*)d_in[8]; a.dv = (const u16*)d_in[9];
  a.out = (u16*)d_out; a.ws = (unsigned char*)d_ws;
  (void)hipFuncSetAttribute((const void*)fused_kernel, hipFuncAttributeMaxDynamicSharedMemorySize, LDS_BYTES);
  fused_kernel<<<256, 512, LDS_BYTES, stream>>>(a);
}
```

```cpp
#include <hip/hip_runtime.h>
#include <hip/hip_bf16.h>
#include <utility>

typedef unsigned u32;
typedef unsigned short u16;
typedef unsigned long long u64;
typedef float f2 __attribute__((ext_vector_type(2)));
typedef float f4 __attribute__((ext_vector_type(4)));
typedef u32 u2 __attribute__((ext_vector_type(2)));
typedef u32 u4 __attribute__((ext_vector_type(4)));
typedef u32 u2a __attribute__((ext_vector_type(2), aligned(4)));
typedef __bf16 bf2 __attribute__((ext_vector_type(2)));
typedef __bf16 bf8 __attribute__((ext_vector_type(8)));
typedef short s4v __attribute__((ext_vector_type(4)));
typedef __amdgpu_buffer_rsrc_t rsrc_t;
#define LAS __attribute__((address_space(3)))
#define CAS __attribute__((address_space(4)))
#define DEV __device__ __forceinline__
#define UNR _Pragma("unroll")
#define NOUNR _Pragma("unroll 1")
#define FENCE() asm volatile("" ::: "memory")
#define SC1 16

#define WV3 1
constexpr u32 O_FW = 4096, O_FA = O_FW + 32768, O_FK = O_FA + 32768, O_FY = O_FK + 32768, O_FO = O_FY + 32768, O_FB = O_FO + 32768, O_FC = O_FB + 32768;
constexpr u32 O_WT = 262144, WT_G = 124416;
constexpr u32 O_CS = O_WT + 16 * WT_G;
constexpr u32 O_WOT = O_CS + 62464;
constexpr u32 O_KVT = O_WOT + 663552;
constexpr u32 O_YT = O_KVT + 2359296;
constexpr u32 O_SP = O_YT + 2359296, SP_L = 720896, SP_G = 45056, SP_P = 2816;
constexpr u32 O_OB = O_SP + 2 * SP_L, OB_L = 2359296, OB_P = 147456;
constexpr u32 O_ST = O_OB + 2 * OB_L;
constexpr u32 O_X2 = O_ST + 3 * 65536;
constexpr u32 O_STAMP = O_X2 + 2359296;
constexpr u32 WS_BYTES = O_STAMP + 256 * 8 * 32 * 8;
constexpr u32 SLOT = 9728, L_WQ = 9 * SLOT, L_STG = 5 * SLOT, L_STB = L_STG + 30720;
constexpr u32 L_H0 = 124416, L_H1 = 132736, L_U0 = 141056, L_U1 = 145632, L_H2 = 150528, L_U2 = 154688, L_ZERO = L_U2 + 4096, L_CSL = 159264;
constexpr u32 L_KT = 131072, L_VT = 141152, L_TILE = 150528, L_PT = 158720;
constexpr u32 L_REDA = 124416, L_REDB = 126464, L_Y = 128512, L_KEEP = 139264;
constexpr u32 L_TK = 160256, L_TK2 = 161696, L_TQ = 161888, L_TQ2 = 163248, L_CNT = 163712, LDS_BYTES = 163840;
enum { C_PROG = 0, C_WPROG = 4, C_WOK, C_TBL, C_KVC, C_PHI, C_STB, C_PU0, C_PU1, C_PU2, C_PA0, C_PA1, C_PB0, C_PB1, C_QA, C_QB, C_REDA, C_REDB, C_FYC, C_FAOK, C_GEM, C_PUB, C_KVL, C_KVR, C_WT, C_CLR, C_G2, C_FBOK };
#define CNT(i) (L_CNT + 4 * (i))
constexpr u32 SENT = 0xFFFFFFFFu;
constexpr int SPIN_G = 1 << 17, SPIN_L = 1 << 21;

#define CLRAT 7
#define KVAT 4
#define XLA 5
#define HAT 7
#define FAT 10
#define CLB 2
#define PAT 10
#define PLATE 1
#define KVS 8
#define PF 0
#define CPF 3
#define KAP 1
#define GR3 0
#define NA 5
#define DO0 26
#define DO1 14
#define DOA 12
#define DOR 6
#define DST 10
#define DSR 0
#define DXW 1
#define DX 5
#define DXR 0
#define DXA 0
#define SPE 1
#define SPE1 SPE
#define DW 8
#define DWR 4
#define FYL 0
#define WST 1
#define DK 0
#define DKR 1
#define DPR 0
#define DST1 DST
#define BLA 3
#define DOR1 DOR
#define RST 1
#define DRS 0
#define SVL 39
#define HA2 5
#define DCS 8
#define ES 1
#define FAW 1
#define WOAT 2
#define MAP 1
__device__ __forceinline__ int wg_of(int p, int g) {
  return (p >> 2) * 2 + (g >> 3) + 8 * ((p & 3) * 8 + (g & 7));
}
struct Args {
  const u16 *x, *wqkv, *wphi, *wo, *bk, *bv, *sk, *ssm, *dv;
  u16* out;
  unsigned char* ws;
};

template <int I> struct IC { static constexpr int v = I; };
template <class F, int... I> DEV void sfor_(F&& f, std::integer_sequence<int, I...>) { (f(IC<I>{}), ...); }
template <int N, class F> DEV void sfor(F&& f) { sfor_(f, std::make_integer_sequence<int, N>{}); }
#define INL __attribute__((always_inline))
template <class T> DEV void pin(T& v) { asm volatile("" : "+v"(v)); }
template <class T> DEV void pins(T& v) { asm volatile("" : "+s"(v)); }
DEV int opq(int v) { asm volatile("" : "+v"(v)); return v; }
DEV int lane_id() { u32 m = ~0u; asm volatile("" : "+s"(m)); return __builtin_amdgcn_mbcnt_hi(m, __builtin_amdgcn_mbcnt_lo(m, 0)); }
DEV u32 rfl(u32 v) { return __builtin_amdgcn_readfirstlane(v); }
DEV rsrc_t mkrs(const void* p, u32 n) { return __builtin_amdgcn_make_buffer_rsrc((void*)p, 0, n, 0x00020000); }
DEV u32 pk(float a, float b) { f2 v = {a, b}; return __builtin_bit_cast(u32, __builtin_convertvector(v, bf2)); }
DEV u32 pk(f2 v) { return __builtin_bit_cast(u32, __builtin_convertvector(v, bf2)); }
DEV f2 up(u32 p) { f2 r = {__uint_as_float(p << 16), __uint_as_float(p & 0xffff0000u)}; return r; }
DEV f2 rnd(f2 v) { return up(pk(v)); }
DEV float rnd1(float v) { return __uint_as_float(pk(v, v) & 0xffff0000u); }
DEV f4 mfma(u4 a, u4 b, f4 c) { return __builtin_amdgcn_mfma_f32_16x16x32_bf16(__builtin_bit_cast(bf8, a), __builtin_bit_cast(bf8, b), c, 0, 0, 0); }
DEV u32 umin(u32 a, u32 b) { return a < b ? a : b; }
DEV u32 umax(u32 a, u32 b) { return a > b ? a : b; }
DEV u32 umax3(u32 a, u32 b, u32 c) { return umax(umax(a, b), c); }
DEV u32 san(u32 v) { return umin(v, 0xFFFFFFFEu); }
DEV u4 san(u4 v) { u4 r = {san(v.x), san(v.y), san(v.z), san(v.w)}; return r; }
DEV u32 vm(u32 m, u4 v) { return umax3(umax3(m, v.x, v.y), v.z, v.w); }
DEV bool anyl(bool c) { return __builtin_amdgcn_ballot_w64(c) != 0; }
DEV float dot2(u32 a, u32 b, float c) { return __builtin_amdgcn_fdot2_f32_bf16(__builtin_bit_cast(bf2, a), __builtin_bit_cast(bf2, b), c, false); }
template <class T> DEV T ld(u32 o) { return *(LAS T*)(uintptr_t)o; }
template <class T> DEV void st(u32 o, T v) { *(LAS T*)(uintptr_t)o = v; }
DEV u2 trd(u32 o) { return __builtin_bit_cast(u2, __builtin_amdgcn_ds_read_tr16_b64_v4i16((LAS s4v*)(uintptr_t)o)); }
DEV u4 trd2(u32 o0, u32 o1) { u2 a = trd(o0), b = trd(o1); u4 r = {a.x, a.y, b.x, b.y}; return r; }
DEV u4 gld4(rsrc_t r, u32 v, u32 s, int aux) { return aux ? __builtin_amdgcn_raw_buffer_load_b128(r, v, s, SC1) : __builtin_amdgcn_raw_buffer_load_b128(r, v, s, 0); }
DEV u2 gld2(rsrc_t r, u32 v, u32 s, int aux) { return aux ? __builtin_amdgcn_raw_buffer_load_b64(r, v, s, SC1) : __builtin_amdgcn_raw_buffer_load_b64(r, v, s, 0); }
DEV u32 gld1(rsrc_t r, u32 v, u32 s, int aux) { return aux ? __builtin_amdgcn_raw_buffer_load_b32(r, v, s, SC1) : __builtin_amdgcn_raw_buffer_load_b32(r, v, s, 0); }
DEV void gst4(u4 d, rsrc_t r, u32 v, u32 s) { __builtin_amdgcn_raw_buffer_store_b128(d, r, v + s, 0, SC1); }
DEV void gst2(u2 d, rsrc_t r, u32 v, u32 s) { __builtin_amdgcn_raw_buffer_store_b64(d, r, v, s, SC1); }
DEV void gst1(u32 d, rsrc_t r, u32 v, u32 s) { __builtin_amdgcn_raw_buffer_store_b32(d, r, v, s, SC1); }
template <int AUX> DEV void dma(rsrc_t r, u32 l, u32 v, u32 s) {
#if defined(__HIP_DEVICE_COMPILE__)
  __builtin_amdgcn_raw_ptr_buffer_load_lds(r, (LAS void*)(uintptr_t)l, 16, v, s, 0, AUX);
#endif
}
DEV void vmwait0() { asm volatile("s_waitcnt vmcnt(0)" ::: "memory"); }
template <int N> DEV void vmwait() { asm volatile("s_waitcnt vmcnt(%0)" ::"n"(N) : "memory"); }
template <int N> DEV void nap() { if (N > 0) __builtin_amdgcn_s_sleep(N); }
DEV void bar() { asm volatile("s_waitcnt lgkmcnt(0)\n\ts_barrier" ::: "memory"); }
DEV void lset(u32 a, u32 v) { asm volatile("ds_write_b32 %0, %1" ::"v"(a), "v"(v) : "memory"); }
DEV void ladd(u32 a, int lane) { if (lane == 0) { u32 one = 1; asm volatile("ds_add_u32 %0, %1" ::"v"(a), "v"(one) : "memory"); } }
DEV u32 ladd_rtn(u32 a, int lane) {
  u32 r = 0;
  if (lane == 0) { u32 one = 1; asm volatile("ds_add_rtn_u32 %0, %1, %2\n\ts_waitcnt lgkmcnt(0)" : "=v"(r) : "v"(a), "v"(one) : "memory"); }
  return rfl(r);
}
DEV u32 lget(u32 a) { u32 r; asm volatile("ds_read_b32 %0, %1\n\ts_waitcnt lgkmcnt(0)" : "=v"(r) : "v"(a) : "memory"); return rfl(r); }
DEV u32 lwait(u32 a, u32 t, int& dead) {
  u32 r = lget(a);
  if (__builtin_expect(r < t, 0)) {
    NOUNR for (int i = dead ? SPIN_L : 0; i < SPIN_L; ++i) { r = lget(a); if (r >= t) return r; __builtin_amdgcn_s_sleep(1); }
    dead = 1;
  }
  return r;
}
template <int R = 1> DEV void poll(rsrc_t ws, u32 off, u32 tag, int& dead) {
  NOUNR for (int i = dead ? SPIN_G : 0; i < SPIN_G; ++i) {
    FENCE();
    u32 v = gld1(ws, off, 0, 1);
    if (!anyl(v != tag)) { FENCE(); return; }
    nap<R>();
  }
  dead = 1;
}
DEV void setflag(rsrc_t ws, u32 off, u32 tag, int lane) { if (lane == 0) gst1(tag, ws, off, 0); }
#define SW16(a, b) __builtin_amdgcn_permlane16_swap(a, b, false, false)
#define SW32(a, b) __builtin_amdgcn_permlane32_swap(a, b, false, false)
DEV float sw16add(float a, float b) { auto r = SW16(__float_as_uint(a), __float_as_uint(b)); return __uint_as_float(r[0]) + __uint_as_float(r[1]); }
DEV float sw32add(float a, float b) { auto r = SW32(__float_as_uint(a), __float_as_uint(b)); return __uint_as_float(r[0]) + __uint_as_float(r[1]); }

#define STAMP(i)
#define STAMPC(i)
#define STARG
#define STPASS

#define STAMPD(i)
#define STAMPL(i) STAMP(i + L * 13)
DEV f2 ln_stats(float s, float sq) {
  float mean = s * (1.0f / 576.0f);
  float var = fmaxf(sq * (1.0f / 576.0f) - mean * mean, 0.0f);
  float sd = rnd1(__builtin_amdgcn_sqrtf(rnd1(rnd1(var) + 1e-5f)));
  f2 r = {rnd1(mean), __builtin_amdgcn_rcpf(sd)};
  return r;
}
DEV void stats_from(u4 v, float (&m)[2], float (&rs)[2]) {
  float s = __uint_as_float(v.x) + __uint_as_float(v.z), sq = __uint_as_float(v.y) + __uint_as_float(v.w);
  s = sw16add(s, s); sq = sw16add(sq, sq);
  f2 t = ln_stats(s, sq);
  auto r0 = SW32(__float_as_uint(t.x), __float_as_uint(t.x));
  auto r1 = SW32(__float_as_uint(t.y), __float_as_uint(t.y));
  m[0] = __uint_as_float(r0[0]); m[1] = __uint_as_float(r0[1]); rs[0] = __uint_as_float(r1[0]); rs[1] = __uint_as_float(r1[1]);
}
DEV u4 ld_stats(rsrc_t ws, u32 off) { u2 a = gld2(ws, off, 0, 1), b = gld2(ws, off + 256, 0, 1); return u4{a.x, a.y, b.x, b.y}; }
DEV u4 poll_stats(rsrc_t ws, u32 off, u4 v, int& dead) {
  if (anyl(vm(0, v) == SENT)) {
    NOUNR for (int i = dead ? SPIN_G : 0; i < SPIN_G; ++i) {
      nap<DSR>();
      FENCE();
      v = ld_stats(ws, off);
      if (!anyl(vm(0, v) == SENT)) { FENCE(); return v; }
    }
    dead = 1;
  }
  FENCE();
  return v;
}

DEV void pub_stats(rsrc_t ws, float s0, float s1, float q0, float q1, u32 wa, u32 ra, u32 cnt, u32 last, u32 dst) {
  float t = sw16add(sw32add(s0, s1), sw32add(q0, q1));
  u32 n, one = 1;
  u64 ex;
  f4 r[6];
  asm volatile("ds_write_b32 %8, %9\n\ts_mov_b64 %7, exec\n\ts_mov_b64 exec, 1\n\tds_add_rtn_u32 %6, %10, %11\n\ts_mov_b64 exec, %7\n\t"
               "ds_read_b128 %0, %12\n\tds_read_b128 %1, %12 offset:256\n\tds_read_b128 %2, %12 offset:512\n\tds_read_b128 %3, %12 offset:768\n\t"
               "ds_read_b128 %4, %12 offset:1024\n\tds_read_b128 %5, %12 offset:1280\n\ts_waitcnt lgkmcnt(0)"
               : "=&v"(r[0]), "=&v"(r[1]), "=&v"(r[2]), "=&v"(r[3]), "=&v"(r[4]), "=&v"(r[5]), "=&v"(n), "=&s"(ex)
               : "v"(wa), "v"(t), "v"(cnt), "v"(one), "v"(ra)
               : "memory");
  if (rfl(n) == last) gst4(san(__builtin_bit_cast(u4, ((r[0] + r[1]) + (r[2] + r[3])) + (r[4] + r[5]))), ws, dst, 0);
}

struct KVin { u32 k[11], v[8]; };
DEV u4 kv_w(const Args& a, int T, int c) { return gld4(mkrs(a.sk, 4608), (144 * c + 2 * (T >> 2)) * 8, 0, 0); }
DEV void kv_read_asm(int T, KVin& in) {
  const u32 base = (T & 3) * 2304 + (T >> 2) * 4;
  UNR for (int i = 0; i < 11; ++i) asm volatile("ds_read_b32 %0, %1 offset:%2" : "=v"(in.k[i]) : "v"(L_KT + base), "n"(i * 288) : "memory");
  UNR for (int i = 0; i < 8; ++i) asm volatile("ds_read_b32 %0, %1 offset:%2" : "=v"(in.v[i]) : "v"(L_VT + base), "n"(i * 288) : "memory");
  asm volatile("s_waitcnt lgkmcnt(0)"
               : "+v"(in.k[0]), "+v"(in.k[1]), "+v"(in.k[2]), "+v"(in.k[3]), "+v"(in.k[4]), "+v"(in.k[5]), "+v"(in.k[6]), "+v"(in.k[7]), "+v"(in.k[8]),
                 "+v"(in.k[9]), "+v"(in.k[10]), "+v"(in.v[0]), "+v"(in.v[1]), "+v"(in.v[2]), "+v"(in.v[3]), "+v"(in.v[4]), "+v"(in.v[5]), "+v"(in.v[6]),
                 "+v"(in.v[7])
               :
               : "memory");
}
DEV void kv_read(int T, KVin& in) {
  const u32 base = (T & 3) * 2304 + (T >> 2) * 4;
  UNR for (int i = 0; i < 11; ++i) in.k[i] = ld<u32>(L_KT + base + i * 288);
  UNR for (int i = 0; i < 8; ++i) in.v[i] = ld<u32>(L_VT + base + i * 288);
}
#define SB() __builtin_amdgcn_sched_barrier(0)
DEV void add_rnd8(float (&t)[8], float (&a)[8]) {
  asm volatile("v_cvt_pk_bf16_f32 %8, %0, %8\n\tv_cvt_pk_bf16_f32 %9, %1, %9\n\tv_cvt_pk_bf16_f32 %10, %2, %10\n\tv_cvt_pk_bf16_f32 %11, %3, %11\n\t"
               "v_cvt_pk_bf16_f32 %12, %4, %12\n\tv_cvt_pk_bf16_f32 %13, %5, %13\n\tv_cvt_pk_bf16_f32 %14, %6, %14\n\tv_cvt_pk_bf16_f32 %15, %7, %15\n\t"
               "v_dot2_f32_bf16 %0, %8, %16, 0\n\tv_dot2_f32_bf16 %1, %9, %16, 0\n\tv_dot2_f32_bf16 %2, %10, %16, 0\n\tv_dot2_f32_bf16 %3, %11, %16, 0\n\t"
               "v_dot2_f32_bf16 %4, %12, %16, 0\n\tv_dot2_f32_bf16 %5, %13, %16, 0\n\tv_dot2_f32_bf16 %6, %14, %16, 0\n\tv_dot2_f32_bf16 %7, %15, %16, 0\n\ts_nop 2"
               : "+v"(t[0]), "+v"(t[1]), "+v"(t[2]), "+v"(t[3]), "+v"(t[4]), "+v"(t[5]), "+v"(t[6]), "+v"(t[7]), "+v"(a[0]), "+v"(a[1]), "+v"(a[2]),
                 "+v"(a[3]), "+v"(a[4]), "+v"(a[5]), "+v"(a[6]), "+v"(a[7])
               : "s"(0x3F803F80u));
}
DEV void kv_compute(rsrc_t ws, int T, int rt, int c, const KVin& in, u4 w, bool on) {
  const int ch = 144 * c + 2 * (T >> 2), s0 = 32 * rt + 8 * (T & 3);
  f2 w0 = {__uint_as_float(w.x << 16), __uint_as_float(w.z << 16)}, w1 = {__uint_as_float(w.x & 0xffff0000u), __uint_as_float(w.z & 0xffff0000u)};
  f2 w2 = {__uint_as_float(w.y << 16), __uint_as_float(w.w << 16)}, w3 = {__uint_as_float(w.y & 0xffff0000u), __uint_as_float(w.w & 0xffff0000u)};
  f2 kf[11];
  UNR for (int i = 0; i < 11; ++i) kf[i] = up(in.k[i]);
  float t0[8], t1[8], a0[8], a1[8];
  UNR for (int s = 0; s < 8; ++s) { const f2 x = kf[s] * w3, y = kf[s + 1] * w2; t0[s] = x.x; t1[s] = x.y; a0[s] = y.x; a1[s] = y.y; }
  add_rnd8(t0, a0); add_rnd8(t1, a1);
  UNR for (int s = 0; s < 8; ++s) { const f2 y = kf[s + 2] * w1; a0[s] = y.x; a1[s] = y.y; }
  add_rnd8(t0, a0); add_rnd8(t1, a1);
  UNR for (int s = 0; s < 8; ++s) { const f2 y = kf[s + 3] * w0; a0[s] = y.x; a1[s] = y.y; }
  add_rnd8(t0, a0); add_rnd8(t1, a1);
  u32 P[8];
  UNR for (int s = 0; s < 8; ++s) P[s] = pk(t0[s], t1[s]);
  SB();
  UNR for (int s = 0; s < 8; ++s) kf[s] = up(P[s]) * up(in.v[s]);
  SB();
  UNR for (int s = 0; s < 8; ++s) P[s] = pk(kf[s]);
  u4 o0, o1;
  UNR for (int d = 0; d < 4; ++d) {
    o0[d] = __builtin_amdgcn_perm(P[6 - 2 * d], P[7 - 2 * d], 0x05040100u);
    o1[d] = __builtin_amdgcn_perm(P[6 - 2 * d], P[7 - 2 * d], 0x07060302u);
  }
  const u32 off = on ? O_KVT + ch * 4096 + (2040 - s0) * 2 : 0x80000000u;
  gst4(o0, ws, off, 0);
  gst4(o1, ws, off, 4096);
}

struct Hin { u4 h[4]; };
DEV void h_load(const Args& a, int ch, int lane, Hin& in) {
  rsrc_t rh = mkrs(a.ssm, 2359296);
  UNR for (int m = 0; m < 4; ++m) in.h[m] = gld4(rh, ch * 4096 + lane * 16, m * 1024, 0);
}
DEV void h_stage(u32 lH, u32 lU, int lane, const Hin& in) {
  UNR for (int m = 0; m < 4; ++m) {
    const u4 h = in.h[m];
    const u32 nx = __builtin_amdgcn_update_dpp(m < 3 ? rfl(in.h[m < 3 ? m + 1 : m].x) : 0u, h.x, 0x130, 0xF, 0xF, false);
    u4 s = {__builtin_amdgcn_alignbit(h.y, h.x, 16), __builtin_amdgcn_alignbit(h.z, h.y, 16), __builtin_amdgcn_alignbit(h.w, h.z, 16), __builtin_amdgcn_alignbit(nx, h.w, 16)};
    st<u4>(lH + 32 + m * 1024 + lane * 16, h);
    st<u4>(lH + 4160 + 32 + m * 1024 + lane * 16, s);
  }
  u32 h0 = rfl(in.h[0].x) << 16;
  u4 z = {0, 0, 0, lane == 3 ? h0 : 0u};
  if (lane < 4) st<u4>(lH + (lane >> 1) * 4160 + (lane & 1) * 16, z);
  u4 zz = {0, 0, 0, 0};
  if (lane < 30) st<u4>(lU + 4096 + lane * 16, zz);
}
DEV void u_stage(rsrc_t ws, u32 lU, int ch, int lane) {
  UNR for (int m = 0; m < 4; ++m) dma<SC1>(ws, lU + m * 1024, O_KVT + ch * 4096 + lane * 16, m * 1024);
}
DEV float load_dv(const Args& a, int ch) {
  const u32 v = ((const CAS u32*)a.dv)[ch >> 1];
  return __uint_as_float((ch & 1) ? v & 0xffff0000u : v << 16);
}
DEV void late_fill(rsrc_t ws, int h, int p, int g, int rt, int c, int lane) {
  const u4 s4 = {SENT, SENT, SENT, SENT};
  if (h < 2) {
    UNR for (int i = 0; i < 6; ++i)
      if (h == 0 || i < 3) gst4(s4, ws, O_OB + lane * 16, OB_L + p * OB_P + (6 * h + i) * 16384 + g * 1024);
      else gst4(s4, ws, (i < 5 || lane < 48) ? O_SP + (i - 3) * 1024 + lane * 16 : 0x80000000u, SP_L + g * SP_G + p * SP_P);
  } else {
    UNR for (int i = 0; i < 5; ++i) {
      u32 o = (5 * (h - 2) + i) * 1024 + lane * 16;
      o += o >= 4608 ? 18432 - 4608 : 0;
      if (h == 2 || i < 4) gst4(s4, ws, O_X2 + o, 2 * rt * 18432 + c * 4608);
      else gst4(s4, ws, lane < 48 ? O_ST + (lane >> 4) * 65536 + (lane & 15) * 16 : 0x80000000u, rt * 1024 + c * 256);
    }
  }
}
DEV u2 y_pack(f4 s, u32 lU, int j, float dv, int q, int i16) {
  u2 uu = ld<u2>(lU + 2 * (2044 - 256 * j - 16 * i16 - 4 * q));
  f2 u32_ = up(uu.x), u10 = up(uu.y);
  f2 g01 = {u10.y * dv, u10.x * dv}, g23 = {u32_.y * dv, u32_.x * dv};
  f2 s01 = {s.x, s.y}, s23 = {s.z, s.w};
  return u2{pk(rnd(s01) + rnd(g01)), pk(rnd(s23) + rnd(g23))};
}
DEV void conv_pair(rsrc_t ws, u32 lH, u32 lU, int h, u32 cB, u32 fboff, u32 tag, f4 (&keep)[4], int lane, int& dead STARG) {
  const int q = lane >> 4, i16 = lane & 15, e = h & 1;
  f4 acc[8];
  UNR for (int i = 0; i < 8; ++i) acc[i] = f4{0, 0, 0, 0};
  const u32 aB = lU + 2 * (2032 - 16 * i16 + 8 * q) - 3584 + 256 * e;
  const u32 aA = lH + ((i16 & 1) ? 0 : 4160) + 4 * (((i16 + 1) >> 1) + 4 * q) + 256 * e;
  const u32 aA4[4] = {aA, aA + 1024, aA + 2048, aA + 3072};
  u4 B[8];
  u2 Al[4], Ah[4];
#define LA(s, t, bb)                                                                                                     \
  asm volatile("ds_read2_b32 %0, %2 offset0:%3 offset1:%4\n\tds_read2_b32 %1, %2 offset0:%5 offset1:%6"                  \
               : "=&v"(Al[s]), "=&v"(Ah[s])                                                                              \
               : "v"(aA4[(t) >> 1]), "n"(16 * (bb) + 128 * ((t) & 1)), "n"(16 * (bb) + 128 * ((t) & 1) + 1),              \
                 "n"(16 * (bb) + 128 * ((t) & 1) + 2), "n"(16 * (bb) + 128 * ((t) & 1) + 3)                               \
               : "memory")
#define LB(v, bb) asm volatile("ds_read_b128 %0, %1 offset:%2" : "=&v"(B[v]) : "v"(aB), "n"(512 * (7 - (v)) + 64 * (bb)) : "memory")
#define WA(n, s) asm volatile("s_waitcnt lgkmcnt(%4)" : "+v"(Al[s]), "+v"(Ah[s]), "+v"(Al[(s) + 1]), "+v"(Ah[(s) + 1]) : "n"(n))
#define WB(n, v) asm volatile("s_waitcnt lgkmcnt(%1)" : "+v"(B[v]) : "n"(n))
#define MM(s, t, v) do { acc[(t) + (v)] = mfma(u4{Al[s].x, Al[s].y, Ah[s].x, Ah[s].y}, B[v], acc[(t) + (v)]); SB(); } while (0)
  LB(7, 0); LB(6, 0); LB(5, 0); LA(0, 0, 0); LA(1, 7, 0); LB(4, 0); LB(3, 0); LB(2, 0); LB(1, 0); LB(0, 0);
  UNR for (int bb = 0; bb < 4; ++bb) {
    const int nb = bb < 3 ? 1 : 0;
    LA(2, 1, bb); LA(3, 6, bb);
    asm volatile("s_waitcnt lgkmcnt(9)" : "+v"(Al[0]), "+v"(Ah[0]), "+v"(Al[1]), "+v"(Ah[1]), "+v"(B[7]), "+v"(B[6]), "+v"(B[5]));
    SB();
    MM(0, 0, 7); if (nb) LB(7, bb + 1);
    MM(0, 0, 6); MM(0, 0, 5);
    WB(8 + nb, 4); MM(0, 0, 4); WB(7 + nb, 3); MM(0, 0, 3); WB(6 + nb, 2); MM(0, 0, 2);
    WB(5 + nb, 1); MM(0, 0, 1); WB(4 + nb, 0); MM(0, 0, 0); MM(1, 7, 0);
    LA(0, 2, bb); LA(1, 5, bb); WA(4 + nb, 2); SB();
    MM(2, 1, 5); MM(2, 1, 6); if (nb) LB(6, bb + 1);
    MM(2, 1, 4); MM(2, 1, 3); MM(2, 1, 2); MM(2, 1, 1); MM(2, 1, 0); MM(3, 6, 1); MM(3, 6, 0);
    LA(2, 3, bb); LA(3, 4, bb); WA(4 + nb, 0); SB();
    MM(0, 2, 5); if (nb) LB(5, bb + 1);
    MM(0, 2, 4); MM(0, 2, 3); MM(0, 2, 2); MM(0, 2, 1); MM(0, 2, 0); MM(1, 5, 2); MM(1, 5, 1); MM(1, 5, 0);
    if (nb) { LA(0, 0, bb + 1); LA(1, 7, bb + 1); }
    WA(5 * nb, 2); SB();
    MM(2, 3, 4); if (nb) LB(4, bb + 1);
    MM(2, 3, 3); MM(2, 3, 2); MM(2, 3, 1); MM(2, 3, 0);
    MM(3, 4, 3); if (nb) LB(3, bb + 1);
    MM(3, 4, 2); if (nb) LB(2, bb + 1);
    MM(3, 4, 1); if (nb) LB(1, bb + 1);
    MM(3, 4, 0); if (nb) LB(0, bb + 1);
    if (bb == CLB) { vmwait0(); if (h == 2) lset(CNT(C_PU1), 2); if (ladd_rtn(CNT(C_CLR), lane) == 3) setflag(ws, fboff, tag, lane); SB(); }
    STAMPD(17 + bb);

  }
  lwait(CNT(C_PUB), 4, dead);
  UNR for (int i = 0; i < 4; ++i) {
    keep[i] = e ? acc[4 + i] : acc[i];
    st<f4>(L_STG + h * 4096 + i * 1024 + lane * 16, e ? acc[i] : acc[4 + i]);
  }
  FENCE();
  ladd(cB, lane);
}
DEV void conv_pair_out(rsrc_t ws, u32 lU, int ch, int h, u32 cB, float dv, const f4 (&keep)[4], int lane, int& dead) {
  const int q = lane >> 4, i16 = lane & 15, e = h & 1;
  lwait(cB, 2, dead);
  u2 Y[4];
  UNR for (int i = 0; i < 4; ++i) Y[i] = y_pack(keep[i] + ld<f4>(L_STG + (h ^ 1) * 4096 + i * 1024 + lane * 16), lU, 4 * e + i, dv, q, i16);
  UNR for (int i = 0; i < 2; ++i) {
    auto r0 = SW16(Y[2 * i].x, Y[2 * i + 1].x);
    auto r1 = SW16(Y[2 * i].y, Y[2 * i + 1].y);
    u4 o = {r0[0], r1[0], r0[1], r1[1]};
    gst4(o, ws, O_YT + ch * 4096 + 2 * (256 * (4 * e + 2 * i + (q & 1)) + 16 * i16 + 8 * (q >> 1)), 0);
  }
}
DEV void conv_quarter(int iq, int h, int lane) {
  const int q = lane >> 4, i16 = lane & 15;
  const u32 aB = L_U2 + 2 * (2032 - 16 * i16 + 8 * q) + 128 * h;
  const u32 aA = L_H2 + 4 * (((i16 + 1) >> 1) + 4 * q) + 128 * h;
  const u32 sh = (i16 & 1) ? 0 : 16;
  f4 m[5];
  UNR for (int i = 0; i < 5; ++i) m[i] = f4{0, 0, 0, 0};
  u2 a0[3][2], a1[3][2];
  u32 a2[3][2];
  u4 bq[3][2];
#define LQ(n)                                                                                                                     \
  {                                                                                                                               \
    const int t_ = (n) <= iq ? (n) : (n)-iq - 1, v_ = (n) <= iq ? iq - (n) : 8 - (n);                                             \
    const u32 pa = aA + 512 * t_, pb = aB - 512 * v_;                                                                             \
    asm volatile("ds_read2_b32 %0, %8 offset0:0 offset1:1\n\tds_read2_b32 %1, %8 offset0:2 offset1:3\n\tds_read_b32 %2, %8 offset:16\n\t" \
                 "ds_read_b128 %3, %9\n\tds_read2_b32 %4, %8 offset0:16 offset1:17\n\tds_read2_b32 %5, %8 offset0:18 offset1:19\n\t"  \
                 "ds_read_b32 %6, %8 offset:80\n\tds_read_b128 %7, %9 offset:64"                                                  \
                 : "=&v"(a0[(n) % 3][0]), "=&v"(a1[(n) % 3][0]), "=&v"(a2[(n) % 3][0]), "=&v"(bq[(n) % 3][0]), "=&v"(a0[(n) % 3][1]), \
                   "=&v"(a1[(n) % 3][1]), "=&v"(a2[(n) % 3][1]), "=&v"(bq[(n) % 3][1])                                            \
                 : "v"(pa), "v"(pb)                                                                                               \
                 : "memory");                                                                                                     \
  }
  LQ(0) LQ(1)
  UNR for (int n = 0; n < 9; ++n) {
    const int s = n % 3;
    asm volatile("s_waitcnt lgkmcnt(%8)" : "+v"(a0[s][0]), "+v"(a1[s][0]), "+v"(a2[s][0]), "+v"(bq[s][0]), "+v"(a0[s][1]), "+v"(a1[s][1]), "+v"(a2[s][1]), "+v"(bq[s][1]) : "n"(n < 8 ? 8 : 0));
    if (n < 7) LQ(n + 2)
    UNR for (int k = 0; k < 2; ++k) {
      u4 A = {__builtin_amdgcn_alignbit(a0[s][k].y, a0[s][k].x, sh), __builtin_amdgcn_alignbit(a1[s][k].x, a0[s][k].y, sh),
              __builtin_amdgcn_alignbit(a1[s][k].y, a1[s][k].x, sh), __builtin_amdgcn_alignbit(a2[s][k], a1[s][k].y, sh)};
      m[n < 4 ? n : 4] = mfma(A, bq[s][k], m[n < 4 ? n : 4]);
    }
    SB();
  }
  f4 s1 = m[0], s2 = m[4];
  UNR for (int n = 1; n < 4; ++n) { if (n <= iq) s1 += m[n]; else s2 += m[n]; }
  st<f4>(L_STG + 16384 + h * 2048 + lane * 16, s1);
  st<f4>(L_STG + 16384 + h * 2048 + 1024 + lane * 16, s2);
  FENCE();
  ladd(CNT(C_QB), lane);
}
DEV void conv_quarter_out(rsrc_t ws, int ch, int iq, int h, float dv, int lane, int& dead) {
  const int q = lane >> 4, i16 = lane & 15;
  lwait(CNT(C_QB), 4, dead);
  f4 s = ld<f4>(L_STG + 16384 + h * 1024 + lane * 16);
  UNR for (int i = 1; i < 4; ++i) s += ld<f4>(L_STG + 16384 + i * 2048 + h * 1024 + lane * 16);
  const int j = h ? 7 - iq : iq;
  u2 Y = y_pack(s, L_U2, j, dv, q, i16);
  auto r0 = SW16(Y.x, Y.x);
  auto r1 = SW16(Y.y, Y.y);
  gst4(u4{r0[0], r1[0], r0[1], r1[1]}, ws, (q & 1) ? 0x80000000u : O_YT + ch * 4096 + 2 * (256 * j + 16 * i16 + 8 * (q >> 1)), 0);
}

template <bool HELP> struct Keep { u2 x[HELP ? 4 : 1], y[HELP ? 4 : 1]; };
template <bool HELP, int L>
DEV void tail(const Args& a, rsrc_t ws, int w, int rt, int c, u32 tag, const u4 (&wo)[HELP ? 36 : 18], Keep<HELP>& kp, int& dead STARG) {
  constexpr int NB = HELP ? 2 : 1, NU = HELP ? 4 : 1;
  const int lane = lane_id(), q = lane >> 4, i16 = lane & 15;
  const int bl0 = HELP ? 2 * (w - 4) : 8, slot = HELP ? w - 4 : 4 + w;
  if (L == 0 && HELP) {
    rsrc_t rx = mkrs(a.x, 2359296);
    const u32 o = (32 * rt + i16) * 1152 + (144 * c + 4 * q) * 2;
    UNR for (int u = 0; u < NU; ++u) kp.x[u] = gld2(rx, o, (bl0 + (u >> 1)) * 32 + (u & 1) * 16 * 1152, 0);
  }
  u32 fyoff = 0, fy = tag;
  if (L == 0) {
    const int chl = 144 * c + 16 * bl0 + (lane & (16 * NB - 1)), j = rt >> 3;
    fyoff = O_FY + (chl < 512 ? chl >> 1 : 4 * (chl - 512) + (j < 4 ? j : 7 - j)) * 128;
    pin(fyoff);
  }
  u32 bo0 = L_STG + (HELP ? 0 : w * 18432) + lane * 16, bo1 = bo0 + 18432;
  u32 rwa = slot * 256 + (16 * (lane >> 5) + i16) * 8 + (q & 1) * 4 + L_REDA, rra = i16 * 16 + L_REDA;
  u32 sdst = lane < 16 ? O_ST + L * 2 * 65536 + rt * 1024 + c * 256 + lane * 16 : 0x80000000u;
  u32 stoff = O_ST + L * 2 * 65536 + rt * 1024 + (q & 1) * 512 + (16 * (q >> 1) + i16) * 8;
  u32 odst;
  {
    const int tb = HELP ? q & 1 : w;
    const bool on = HELP || !(q & 1);
    if (L == 0) odst = on ? O_X2 + tb * 18432 + (q >> 1) * 256 + i16 * 16 + 2 * rt * 18432 + (9 * c + bl0) * 512 : 0x80000000u;
    else odst = on ? (32 * rt + 16 * tb + i16) * 1152 + (q >> 1) * 16 + 288 * c + 32 * bl0 : 0x80000000u;
  }
  pin(bo0); pin(rwa); pin(rra); pin(sdst); pin(stoff); pin(odst);
  if (HELP) pin(bo1);
  bar();
  STAMPL(13);
  if (L == 0 && FYL == 0) fy = gld1(ws, fyoff, 0, 1);
  f4 acc[NU];
  UNR for (int u = 0; u < NU; ++u) acc[u] = f4{0, 0, 0, 0};
  if (HELP) {
    u4 R[5];
    UNR for (int n = 0; n < 5; ++n) R[n] = ld<u4>(((n & 1) ? bo1 : bo0) + (n >> 1) * 1024);
    SB();
    UNR for (int n = 0; n < 36; ++n) {
      const int ks = n >> 1, tb = n & 1, nn = n + 5;
      acc[tb] = mfma(wo[ks], R[n % 5], acc[tb]);
      acc[NU - 2 + tb] = mfma(wo[(NB - 1) * 18 + ks], R[n % 5], acc[NU - 2 + tb]);
      SB();
      if (nn < 36) R[n % 5] = ld<u4>(((nn & 1) ? bo1 : bo0) + (nn >> 1) * 1024);
      if (L == 0 && FYL && n == FYL) fy = gld1(ws, fyoff, 0, 1);
      SB();
    }
  } else {
    f4 a2 = {0, 0, 0, 0};
    const u32 bo = bo0;
    u4 R[18];
    UNR for (int ks = 0; ks < 12; ++ks) R[ks] = ld<u4>(bo + ks * 1024);
    SB();
    UNR for (int ks = 0; ks < 18; ks += 2) {
      acc[0] = mfma(wo[ks], R[ks], acc[0]); a2 = mfma(wo[ks + 1], R[ks + 1], a2);
      if (ks < 6) { R[ks + 12] = ld<u4>(bo + (ks + 12) * 1024); R[ks + 13] = ld<u4>(bo + (ks + 13) * 1024); }
      SB();
    }
    acc[0] += a2;
    if (L == 0 && FYL) { nap<4>(); FENCE(); fy = gld1(ws, fyoff, 0, 1); }
  }
  FENCE();
  if (L == 0) ladd(CNT(C_G2), lane);
  u4 yv[NB];
  if (L == 0) {
    if (anyl(fy != tag)) poll(ws, fyoff, tag, dead);
    FENCE();
    UNR for (int i = 0; i < NB; ++i) yv[i] = gld4(ws, O_YT + (lane >> 2) * 4096 + (lane & 3) * 16, (144 * c + 16 * (bl0 + i)) * 4096 + rt * 64, 1);
  }
  f2 x1[NU][2];
  float S[2] = {0, 0}, Q[2] = {0, 0};
  UNR for (int u = 0; u < NU; ++u) {
    f2 lo = {acc[u].x, acc[u].y}, hi = {acc[u].z, acc[u].w};
    x1[u][0] = rnd(rnd(lo) + up(kp.x[u].x)); x1[u][1] = rnd(rnd(hi) + up(kp.x[u].y));
  }
  if (HELP) {
    UNR for (int tb = 0; tb < 2; ++tb) {
      f2 s = (x1[tb][0] + x1[tb][1]) + (x1[NU - 2 + tb][0] + x1[NU - 2 + tb][1]);
      f2 sq = x1[tb][0] * x1[tb][0] + x1[tb][1] * x1[tb][1] + x1[NU - 2 + tb][0] * x1[NU - 2 + tb][0] + x1[NU - 2 + tb][1] * x1[NU - 2 + tb][1];
      S[tb] = s.x + s.y; Q[tb] = sq.x + sq.y;
    }
  } else {
    f2 s = x1[0][0] + x1[0][1], sq = x1[0][0] * x1[0][0] + x1[0][1] * x1[0][1];
    S[0] = w ? 0.0f : s.x + s.y; S[1] = w ? s.x + s.y : 0.0f; Q[0] = w ? 0.0f : sq.x + sq.y; Q[1] = w ? sq.x + sq.y : 0.0f;
  }
  pub_stats(ws, S[0], S[1], Q[0], Q[1], rwa, rra, CNT(C_REDA), 6 * L + 5, sdst);
  STAMPL(14);
  nap<L ? DST1 : DST>();
  FENCE();
  u4 sv = ld_stats(ws, stoff);
  if (L == 0) {
    const u32 yb = L_Y + (HELP ? bl0 : 8 + w) * 1024;
    UNR for (int i = 0; i < NB; ++i) st<u4>(yb + i * 1024 + lane * 16, yv[i]);
    FENCE();
    const u32 ya = yb + (4 * q + (i16 >> 2)) * 64 + (i16 & 3) * 8;
    UNR for (int u = 0; u < NU; ++u) kp.y[u] = trd(ya + (u >> 1) * 1024 + (HELP ? u & 1 : w) * 32);
  }
  sv = poll_stats(ws, stoff, sv, dead);
  STAMPL(15);
  float m[2], rs[2];
  stats_from(sv, m, rs);
  UNR for (int u = 0; u < NU; ++u) {
    const float mu = HELP ? m[u & 1] : (w ? m[1] : m[0]), ru = HELP ? rs[u & 1] : (w ? rs[1] : rs[0]);
    UNR for (int h = 0; h < 2; ++h) {
      f2 xn = rnd(rnd(x1[u][h] - mu) * ru);
      kp.x[u][h] = pk(x1[u][h] + rnd(xn * up(kp.y[u][h])));
    }
  }
  UNR for (int i = 0; i < NB; ++i) {
    auto r0 = SW16(kp.x[2 * i].x, kp.x[HELP ? 2 * i + 1 : 0].x);
    auto r1 = SW16(kp.x[2 * i].y, kp.x[HELP ? 2 * i + 1 : 0].y);
    const u4 o = {r0[0], r1[0], r0[1], r1[1]};
    if (L == 0) gst4(san(o), ws, odst, i * 512);
    else __builtin_amdgcn_raw_buffer_store_b128(o, mkrs(a.out, 2359296), odst + 32 * i, 0, 0);
  }
  if (L == 0) {
    f2 s[NU], sq[NU];
    UNR for (int u = 0; u < NU; ++u) { f2 lo = up(kp.x[u].x), hi = up(kp.x[u].y); s[u] = lo + hi; sq[u] = lo * lo + hi * hi; }
    if (HELP) {
      UNR for (int tb = 0; tb < 2; ++tb) { f2 t = s[tb] + s[NU - 2 + tb], tq = sq[tb] + sq[NU - 2 + tb]; S[tb] = t.x + t.y; Q[tb] = tq.x + tq.y; }
    } else {
      S[0] = w ? 0.0f : s[0].x + s[0].y; S[1] = w ? s[0].x + s[0].y : 0.0f; Q[0] = w ? 0.0f : sq[0].x + sq[0].y; Q[1] = w ? sq[0].x + sq[0].y : 0.0f;
    }
    pub_stats(ws, S[0], S[1], Q[0], Q[1], rwa + (L_REDB - L_REDA), rra + (L_REDB - L_REDA), CNT(C_REDB), 5, sdst + 65536);
    if (HELP) STAMP(16);
  }
}

DEV void w_slot(rsrc_t ws, int j, int rot, int g, int lane) {
  int kr = j + rot; kr -= kr >= 9 ? 9 : 0;
  UNR for (int i = 0; i < 9; ++i) dma<SC1>(ws, j * SLOT + i * 1024, O_WT + lane * 16, g * WT_G + kr * SLOT + i * 1024);
  if (lane < 32) dma<SC1>(ws, j * SLOT + 9216, O_WT + lane * 16, g * WT_G + kr * SLOT + 9216);
}

template <int L>
DEV void main_layer(const Args& a, rsrc_t ws, int b, int w, int p, int g, int rt, int c, int rot, u32 tag, u4 (&B)[36], float (&mS)[2], float (&mQ)[2], u32& wp, int& dead STARG) {
    constexpr float LOG2E = 1.4426950408889634f;
    int lane = lane_id(), q = lane >> 4, i16 = lane & 15;
    {
      f4 acc[5][2];
      u4 sv = {0, 0, 0, 0};
      auto stoff = [&]() -> u32 { return O_ST + 65536 + (4 * p + w) * 1024 + (q & 1) * 512 + (16 * (q >> 1) + i16) * 8; };
      u32 so[9], xo0 = 0, xo1 = 0, sto = 0;
      auto b_load = [&](int j) INL { UNR for (int i = 0; i < 4; ++i) B[4 * j + i] = gld4(ws, ((i & 1) ? xo1 : xo0) + (i >> 1) * 1024, so[j], 1); };
      auto b_addr = [&]() INL {
        lane = lane_id(); q = lane >> 4; i16 = lane & 15;
        xo0 = O_X2 + lane * 16 + (8 * p + 2 * w) * 18432; xo1 = xo0 + 18432; sto = stoff();
        pin(xo0); pin(xo1); pin(sto);
      };
      if (L == 1) {
        UNR for (int j = 0; j < 9; ++j) { int kr = j + rot; kr -= kr >= 9 ? 9 : 0; so[j] = kr * 2048; pins(so[j]); }
        b_addr();
        u32 G = lane < 36 ? lane : (lane < 40 ? 9 * (lane - 36) + 8 : 0);
        u32 po = O_X2 + (8 * p + 2 * w + (lane >= 36 && lane < 40 ? 1 : 0)) * 18432 + G * 512;
        if (w >= 2) {
          lwait(CNT(C_REDB), 6, dead);
          nap<DX>();
          NOUNR for (int i = dead ? SPIN_G : 0; i < SPIN_G; ++i) {
            FENCE();
            u32 v = gld1(ws, po, 0, 1), v2 = gld1(ws, po - 4 * 18432, 0, 1);
            if (!anyl(umax(v, v2) == SENT)) break;
            nap<DXR>();
          }
          lset(CNT(C_KVL) + 4 * (w - 2), 64);
        } else lwait(CNT(C_KVL) + 4 * w, 64, dead);
        nap<DXA>();
        FENCE();
        STAMP(16);
      }
      f4 cs[5];
      bool svbad = false;
      for (int pass = 0;; ++pass) {
        if (L == 1) {
          if (pass) { UNR for (int j = 0; j < PF; ++j) b_load(j); }
          UNR for (int j = PF; j < BLA; ++j) { b_load(j); if (j == PF && !SVL) sv = ld_stats(ws, sto); }
          SB();
        }
        lane = lane_id(); q = lane >> 4; i16 = lane & 15;
        UNR for (int i = 0; i < 5; ++i) acc[i][0] = acc[i][1] = f4{0, 0, 0, 0};
        u32 vmx = 0, fl = 0;
        if (L == 0) { wp = lwait(CNT(C_WPROG), 1, dead); STAMP(3); }
        u32 b0[2], b4[2];
        const int r4 = 64 + 3 * (i16 >> 2) + ((i16 & 3) < 2 ? (i16 & 3) : 2);
        UNR for (int u = 0; u < 2; ++u) {
          b0[u] = i16 * 128 + (((4 * u + q) ^ (i16 >> 1)) << 4);
          b4[u] = r4 * 128 + (((4 * u + q) ^ ((r4 >> 1) & 7)) << 4);
        }
        u4 A[NA];
        UNR for (int n = 0; n < NA; ++n) A[n] = ld<u4>(n % 5 < 4 ? b0[n / 5] + (n % 5) * 2048 : b4[n / 5]);
        SB();
        sfor<90>([&](auto ic) INL {
          constexpr int n = decltype(ic)::v, s = n / 5, i = n % 5, m = n + NA, j = m / 10, u = (m / 5) & 1, im = m % 5, jo = j < 5 ? j : j - 5;
          if (L == 0 && n == 5 * KVS) { vmwait0(); ladd(CNT(C_KVC), lane_id()); }
          acc[i][0] = mfma(A[n % NA], B[2 * s], acc[i][0]);
          acc[i][1] = mfma(A[n % NA], B[2 * s + 1], acc[i][1]);
          SB();
          if (L == 1 && n % 10 < 8 && (n & 1) == 0 && n / 10 + BLA < 9) {
            constexpr int jb = n / 10 + BLA < 9 ? n / 10 + BLA : 0, ib = (n % 10) / 2;
            B[4 * jb + ib] = gld4(ws, ((ib & 1) ? xo1 : xo0) + (ib >> 1) * 1024, so[jb], 1);
          }
          if (m == 50) { UNR for (int k = 0; k < 2; ++k) { b0[k] = opq(b0[k] + 5 * SLOT); b4[k] = opq(b4[k] + 5 * SLOT); } }
          if (m % 10 == 0 && m < 90 && (L == 0 || j == 5)) {
            const u32 need = L ? 3u : (u32)((j + 1) / 2 + 1);
            if (rfl(fl) < need) lwait(CNT(L ? C_PA1 : (j & 1) ? C_PA0 : C_WPROG), need, dead);
          }
          if (m < 90) A[n % NA] = ld<u4>((im < 4 ? b0[u] + im * 2048 : b4[u]) + jo * SLOT);
          else if (m < 95) {
            if (L == 0 && m == 90) lwait(CNT(C_TBL), 2, dead);
            if (m == 90) q = lane_id() >> 4;
            cs[m >= 90 && m < 95 ? m - 90 : 0] = ld<f4>(L_CSL + (m - 90) * 64 + q * 16);
          }
          if (m % 10 == 5 && m < 80 && (L == 0 || j == 4)) fl = *(volatile LAS u32*)(uintptr_t)CNT(L ? C_PA1 : (j & 1) ? C_WPROG : C_PA0);
          if (L == 1 && (i == 1 || i == 3)) { vmx = vm(vmx, B[2 * s + (i >> 1)]); asm volatile("" : "+v"(vmx)); }
          if (L == 1 && SVL && n == SVL) sv = ld_stats(ws, sto);
          if (L == 1 && s == (SVL ? 14 : 4) && i == 3) { svbad = anyl(vm(0, sv) == SENT); stats_from(sv, mS, mQ); }
          SB();
        });
        if (L == 0 || !anyl(vmx == SENT) || dead) break;
        if (pass >= 20000) { dead = 1; break; }
        FENCE();
        b_addr();
      }
      FENCE();
      lane = lane_id(); q = lane >> 4; i16 = lane & 15;
      ladd(CNT(C_GEM), lane);
      STAMPL(4);
      if (L == 1 && svbad) { sv = poll_stats(ws, stoff(), sv, dead); stats_from(sv, mS, mQ); }
      u32 ql[2];
      {
        const bool val = (i16 >> 2) == q;
        const u32 tk = L_TK + (val ? ((q & 1) * 4 + (i16 & 3)) * 16 : 128), tk2 = L_TK2 + (val ? ((q & 1) * 4 + (i16 & 3)) * 4 : 32);
        u4 t0[5], t1[5];
        u32 t2[5];
        u32 kv[2][9];
        UNR for (int i = 0; i < 5; ++i) {
          f2 c01 = {cs[i].x, cs[i].y}, c23 = {cs[i].z, cs[i].w};
          UNR for (int tb = 0; tb < 2; ++tb) {
            f2 lo = {acc[i][tb].x, acc[i][tb].y}, hi = {acc[i][tb].z, acc[i][tb].w};
            kv[tb][i < 4 ? 2 * i : 8] = pk((lo - c01 * mS[tb]) * mQ[tb]);
            if (i < 4) kv[tb][2 * i + 1] = pk((hi - c23 * mS[tb]) * mQ[tb]);
            else ql[tb] = pk((hi.x - c23.x * mS[tb]) * mQ[tb], 0.0f);
          }
          t0[i] = ld<u4>(tk + i * 288); t1[i] = ld<u4>(tk + i * 288 + 144); t2[i] = ld<u32>(tk2 + i * 36);
          SB();
        }
        u32 K[9], V[9];
        UNR for (int i = 0; i < 9; ++i) { auto t = SW32(kv[0][i], kv[1][i]); K[i] = t[0]; V[i] = t[1]; }
        const u4 k0 = {K[0], K[1], K[2], K[3]}, k1 = {K[4], K[5], K[6], K[7]}, k2 = {K[8], 0, 0, 0};
        f4 P[5];
        SB();
        UNR for (int m = 0; m < 5; ++m) P[m] = mfma(t0[m], k0, f4{0, 0, 0, 0});
        SB();
        UNR for (int m = 0; m < 5; ++m) P[m] = mfma(t1[m], k1, P[m]);
        SB();
        UNR for (int m = 0; m < 5; ++m) P[m] = mfma(u4{t2[m], 0, 0, 0}, k2, P[m]);
        SB();
        const u32 row = L_STG + (32 * w + 16 * (q >> 1) + i16) * 232;
        lwait(CNT(C_GEM), 4 * (L + 1), dead);
        {
          u32 vo = row + 144 + (q & 1) * 40;
          UNR for (int i = 0; i < 4; ++i) st<u2>(vo + 8 * i, u2{V[2 * i], V[2 * i + 1]});
          st<u2>(vo + 32, u2{V[8], 0x00003F80u});
        }
        SB();
        f2 z[9];
        UNR for (int i = 0; i < 9; ++i) z[i] = f2{P[i >> 1][2 * (i & 1)], P[i >> 1][2 * (i & 1) + 1]};
        float mx = fmaxf(z[0].x, z[0].y), mn = fminf(z[0].x, z[0].y);
        UNR for (int i = 1; i < 9; ++i) { mx = fmaxf(fmaxf(mx, z[i].x), z[i].y); mn = fminf(fminf(mn, z[i].x), z[i].y); }
        const float ma = -mx * LOG2E, mb = mn * LOG2E;
        f2 e1[9], e2[9], s1 = {0, 0}, s2 = {0, 0};
        UNR for (int i = 0; i < 9; ++i) {
          f2 t1 = z[i] * LOG2E + ma, t2 = mb - z[i] * LOG2E;
          e1[i] = f2{__builtin_amdgcn_exp2f(t1.x), __builtin_amdgcn_exp2f(t1.y)};
          e2[i] = f2{__builtin_amdgcn_exp2f(t2.x), __builtin_amdgcn_exp2f(t2.y)};
          s1 += e1[i]; s2 += e2[i];
        }
        const float r1 = __builtin_amdgcn_rcpf(s1.x + s1.y), r2 = __builtin_amdgcn_rcpf(s2.x + s2.y);
        u32 fo = row + (q & 1) * 72;
        UNR for (int i = 0; i < 4; ++i) {
          st<u2>(fo + 8 * i, u2{pk(e1[2 * i] * r1), pk(e1[2 * i + 1] * r1)});
          st<u2>(fo + 32 + 8 * i, u2{pk(e2[2 * i] * r2), pk(e2[2 * i + 1] * r2)});
        }
        st<u2>(fo + 64, u2{pk(e1[8] * r1), pk(e2[8] * r2)});
        FENCE();
        ladd(CNT(C_PHI), lane);
        lwait(CNT(C_PHI), 4 * (L + 1), dead);
        STAMPL(5);
        lane = lane_id(); q = lane >> 4; i16 = lane & 15;
        const int hd = w >> 1, db = w & 1;
        const u32 ta = L_STG + (16 * (q & 1) + 4 * (i16 >> 2) + (q >> 1)) * 232 + (i16 & 3) * 8;
        f4 Dd[3] = {f4{0, 0, 0, 0}, f4{0, 0, 0, 0}, f4{0, 0, 0, 0}};
        const u32 tf = ta + 72 * hd, tv = ta + 144 + 40 * hd + 32 * db;
        u4 bv[4], af[4][3];
        UNR for (int ks = 0; ks < 4; ++ks) {
          bv[ks] = trd2(tv + ks * 7424, tv + ks * 7424 + 464);
          UNR for (int fb = 0; fb < 3; ++fb) af[ks][fb] = trd2(tf + ks * 7424 + fb * 32, tf + ks * 7424 + fb * 32 + 464);
          if (ks & 1) SB();
          if (ks == 1) { UNR for (int fb = 0; fb < 3; ++fb) Dd[fb] = mfma(af[0][fb], bv[0], Dd[fb]); SB(); }
        }
        UNR for (int ks = 1; ks < 4; ++ks) UNR for (int fb = 0; fb < 3; ++fb) Dd[fb] = mfma(af[ks][fb], bv[ks], Dd[fb]);
        auto s0 = SW16(pk(Dd[0].x, Dd[0].y), pk(Dd[1].x, Dd[1].y));
        auto s1b = SW16(pk(Dd[0].z, Dd[0].w), pk(Dd[1].z, Dd[1].w));
        u4 o = {s0[0], s1b[0], s0[1], s1b[1]};
        const int d = 16 * db + i16;
        const u32 so = L * SP_L + g * SP_G + p * SP_P;
        if (L == 0) lwait(CNT(C_WOK), 1, dead);
        gst4(san(o), ws, d < 19 ? O_SP + (hd * 19 + d) * 64 + (q & 1) * 32 + (q >> 1) * 16 : 0x80000000u, so);
        gst2(u2{san(pk(Dd[2].x, Dd[2].y)), san(pk(Dd[2].z, Dd[2].w))}, ws, (d < 19 && q == 0) ? O_SP + 2432 + (hd * 19 + d) * 8 : 0x80000000u, so);
      }
      ladd(CNT(C_PUB), lane);
      STAMPL(6);
      u32 fq[2][9];
      u4 v[12];
      u32 off[3];
      auto sp_round = [&]() INL -> bool {
        u64 bad[4];
        UNR for (int pp = 0; pp < 4; ++pp) { bad[pp] = __builtin_amdgcn_ballot_w64(vm(vm(vm(0, v[pp]), v[4 + pp]), v[8 + pp]) == SENT); pins(bad[pp]); }
        if (!((bad[0] | bad[1]) | (bad[2] | bad[3]))) return false;
        nap<DPR>();
        UNR for (int pp = 0; pp < 4; ++pp)
          if (bad[pp]) { UNR for (int gi = 0; gi < 3; ++gi) v[4 * gi + pp] = gld4(ws, off[gi], L * SP_L + g * SP_G + 4 * pp * SP_P, 1); }
        return true;
      };
      {
        if (L == 0) lwait(CNT(C_PA1), 2, dead);
        f4 aq[2][2];
        UNR for (int i = 0; i < 2; ++i) aq[i][0] = aq[i][1] = f4{0, 0, 0, 0};
        lane = lane_id(); q = lane >> 4; i16 = lane & 15;
        const u32 bq[2] = {(u32)opq(L_WQ + i16 * 128 + ((q ^ (i16 >> 1)) << 4)), (u32)opq(L_WQ + i16 * 128 + (((4 + q) ^ (i16 >> 1)) << 4))};
        const bool val = (i16 >> 3) == (q >> 1);
        const u32 tq = L_TQ + (val ? (q * 4 + (i16 & 3)) * 16 : 256), tq2 = L_TQ2 + (val ? (q * 4 + (i16 & 3)) * 4 : 64);
        u4 A[4], t0[5];
        u32 t1[5];
        f4 c5, c6;
        UNR for (int n = 0; n < 4; ++n) A[n] = ld<u4>(bq[n >> 1] + (n & 1) * 2048);
        SB();
        UNR for (int n = 0; n < 36; ++n) {
          const int s = n >> 1, i = n & 1, sn = s + 2;
          aq[i][0] = mfma(A[n & 3], B[2 * s], aq[i][0]);
          aq[i][1] = mfma(A[n & 3], B[2 * s + 1], aq[i][1]);
          SB();
          if (n < 32) A[n & 3] = ld<u4>(bq[sn & 1] + (sn >> 1) * 4096 + i * 2048);
          if (n == 30) { c5 = ld<f4>(L_CSL + 5 * 64 + q * 16); c6 = ld<f4>(L_CSL + 6 * 64 + q * 16); }
          if (n >= 31) { t0[n - 31] = ld<u4>(tq + (n - 31) * 272); t1[n - 31] = ld<u32>(tq2 + (n - 31) * 68); }
          SB();
        }
        STAMPL(7);
        UNR for (int gi = 0; gi < 3; ++gi) off[gi] = O_SP + q * SP_P + umin(16 * umin(w + 4 * gi, 10) + i16, 170) * 16;
        auto sp_req = [&]() INL {
        if (L == 0) lwait(CNT(C_FAOK), 1, dead);
        UNR for (int i = 0; i < 12; ++i) v[4 * (i % 3) + i / 3] = gld4(ws, off[i % 3], L * SP_L + g * SP_G + 4 * (i / 3) * SP_P, 1);
        };
        if ((L ? SPE1 : SPE) <= 0) sp_req();
        SB();
        u4 q0[2];
        UNR for (int tb = 0; tb < 2; ++tb) {
          q0[tb].x = pk((f2{aq[0][tb].x, aq[0][tb].y} - f2{c5.x, c5.y} * mS[tb]) * mQ[tb]);
          q0[tb].y = pk((f2{aq[0][tb].z, aq[0][tb].w} - f2{c5.z, c5.w} * mS[tb]) * mQ[tb]);
          q0[tb].z = pk((f2{aq[1][tb].x, aq[1][tb].y} - f2{c6.x, c6.y} * mS[tb]) * mQ[tb]);
          q0[tb].w = pk((f2{aq[1][tb].z, aq[1][tb].w} - f2{c6.z, c6.w} * mS[tb]) * mQ[tb]);
        }
        f4 P[2][5];
        SB();
        UNR for (int tb = 0; tb < 2; ++tb) {
          UNR for (int m = 0; m < 5; ++m) P[tb][m] = mfma(u4{t1[m], 0, 0, 0}, u4{ql[tb], 0, 0, 0}, f4{0, 0, 0, 0});
          SB();
          UNR for (int m = 0; m < 5; ++m) P[tb][m] = mfma(t0[m], q0[tb], P[tb][m]);
          SB();
        }
        const float ls = (q & 1) ? -LOG2E : LOG2E;
        UNR for (int tb = 0; tb < 2; ++tb) {
          if (tb == 0 || GR3) {
            UNR for (int m = 0; m < 5; ++m) { pin(P[0][m]); pin(P[1][m]); }
            FENCE();
            if (tb == 0) STAMPD(18 + 4 * L);
            bool bd = false;
            if ((L ? SPE1 : SPE) == 0) bd = sp_round();
            if ((L ? SPE1 : SPE) == 1) sp_req();
            FENCE();
            if (tb == 0) STAMPD(19 + 4 * L);
          }
          f2 z[9];
          UNR for (int i = 0; i < 9; ++i) z[i] = f2{P[tb][i >> 1][2 * (i & 1)], P[tb][i >> 1][2 * (i & 1) + 1]} * ls;
          float mx = fmaxf(z[0].x, z[0].y);
          UNR for (int i = 1; i < 9; ++i) mx = fmaxf(fmaxf(mx, z[i].x), z[i].y);
          f2 s1 = {0, 0};
          UNR for (int i = 0; i < 9; ++i) { f2 t = z[i] - mx; z[i] = f2{__builtin_amdgcn_exp2f(t.x), __builtin_amdgcn_exp2f(t.y)}; s1 += z[i]; }
          const float r1 = __builtin_amdgcn_rcpf(s1.x + s1.y);
          UNR for (int i = 0; i < 9; ++i) { fq[tb][i] = pk(z[i] * r1); pin(fq[tb][i]); }
          if ((L ? SPE1 : SPE) == 2 + tb) { FENCE(); SB(); sp_req(); SB(); FENCE(); }
        }
      }
      STAMPL(8);
      lane = lane_id(); q = lane >> 4; i16 = lane & 15;
      u32 m01, m2, l01, l2, oo, lo_;
      {
        const bool val = (i16 >> 3) == (q >> 1);
        const int d01 = (q >> 1) * 19 + 8 * ((i16 >> 2) & 1) + (i16 & 3), d2 = (q >> 1) * 19 + 16 + ((i16 & 3) < 2 ? (i16 & 3) : 2);
        m01 = val ? L_STB + d01 * 64 + (q & 1) * 32 : L_ZERO; m2 = val ? L_STB + d2 * 64 + (q & 1) * 32 : L_ZERO;
        l01 = val ? L_STB + 2432 + d01 * 8 + (q & 1) * 4 : L_ZERO; l2 = val ? L_STB + 2432 + d2 * 8 + (q & 1) * 4 : L_ZERO;
        oo = O_OB + lane * 16 + L * OB_L + p * OB_P + 2 * w * 16384 + g * 1024;
        lo_ = (q < 2 && !(i16 & 1)) ? O_OB + 131072 + (16 * q + i16) * 8 + L * OB_L + p * OB_P + g * 1024 + w * 256 : 0x80000000u;
        pin(m01); pin(m2); pin(l01); pin(l2); pin(oo); pin(lo_);
      }
      u32 fb = tag;
      u32 fbo = O_FC + wg_of(p, i16) * 128;
      if (L == 0) pin(fbo);
      {
        u4 sel;
        UNR for (int d = 0; d < 4; ++d) sel[d] = ((i16 & 7) >> 1) == d ? ((i16 & 1) ? 0x3F800000u : 0x00003F80u) : 0u;
        u32 sa[3];
        UNR for (int gi = 0; gi < 3; ++gi) { sa[gi] = L_STB + (16 * umin(w + 4 * gi, 10) + i16) * 16 + q * 8; pin(sa[gi]); }
        pin(sel);
        STAMPD(21 + 5 * L);
        NOUNR for (int it = dead ? SPIN_G - 1 : 0;; ++it) {
          FENCE();
          if (!sp_round()) break;
          if (it >= SPIN_G) { dead = 1; break; }
        }
        FENCE();
        STAMPL(9);
        f4 s[3];
        UNR for (int pp = 0; pp < 4; ++pp) UNR for (int gi = 0; gi < 3; ++gi) s[gi] = mfma(sel, v[4 * gi + pp], pp ? s[gi] : f4{0, 0, 0, 0});
        SB();
        if (q < 2) { UNR for (int gi = 0; gi < 3; ++gi) st<u2>(sa[gi], u2{pk(s[gi].x, s[gi].y), pk(s[gi].z, s[gi].w)}); }
        FENCE();
        ladd(CNT(C_STB), lane);
      }
      if (L == 0) fb = gld1(ws, fbo, 0, 1);
      u32 clr = 1;
      if (L == 0) clr = *(volatile LAS u32*)(uintptr_t)CNT(C_PU2);
      lwait(CNT(C_STB), 4 * (L + 1), dead);
      STAMPL(10);
      {
        f4 O[2][3];
        u4 t0[3], t1[3];
        u32 t2[3];
        UNR for (int k = 0; k < 3; ++k) { const int m = (k + 2) % 3; t0[m] = ld<u4>(m < 2 ? m01 + m * 256 : m2); }
        UNR for (int k = 0; k < 3; ++k) { const int m = (k + 2) % 3; t1[m] = ld<u4>((m < 2 ? m01 + m * 256 : m2) + 16); }
        UNR for (int k = 0; k < 3; ++k) { const int m = (k + 2) % 3; t2[m] = ld<u32>(m < 2 ? l01 + m * 32 : l2); }
        SB();
        UNR for (int k = 0; k < 3; ++k) UNR for (int tb = 0; tb < 2; ++tb) { const int m = (k + 2) % 3; O[tb][m] = mfma(t0[m], u4{fq[tb][0], fq[tb][1], fq[tb][2], fq[tb][3]}, f4{0, 0, 0, 0}); }
        SB();
        UNR for (int k = 0; k < 3; ++k) UNR for (int tb = 0; tb < 2; ++tb) { const int m = (k + 2) % 3; O[tb][m] = mfma(t1[m], u4{fq[tb][4], fq[tb][5], fq[tb][6], fq[tb][7]}, O[tb][m]); }
        SB();
        UNR for (int k = 0; k < 3; ++k) UNR for (int tb = 0; tb < 2; ++tb) { const int m = (k + 2) % 3; O[tb][m] = mfma(u4{t2[m], 0, 0, 0}, u4{fq[tb][8], 0, 0, 0}, O[tb][m]); }
        SB();
        u32 lo[2];
        if (L == 0 && rfl(clr) < 1) lwait(CNT(C_PU2), 1, dead);
        UNR for (int tb = 0; tb < 2; ++tb) {
          const float inv = __builtin_amdgcn_rcpf(O[tb][2].z + 1e-6f);
          u4 o = {pk(f2{O[tb][0].x, O[tb][0].y} * inv), pk(f2{O[tb][0].z, O[tb][0].w} * inv), pk(f2{O[tb][1].x, O[tb][1].y} * inv), pk(f2{O[tb][1].z, O[tb][1].w} * inv)};
          gst4(san(o), ws, oo, tb * 16384);
          lo[tb] = pk(f2{O[tb][2].x, O[tb][2].y} * inv);
        }
        const u32 lv = san((q & 1) ? lo[1] : lo[0]);
        auto hh = SW32(lv, lv);
        const u32 n0 = __builtin_amdgcn_update_dpp(0u, hh[0], 0xB1, 0xF, 0xF, true), n1 = __builtin_amdgcn_update_dpp(0u, hh[1], 0xB1, 0xF, 0xF, true);
        gst4(u4{hh[0], hh[1], n0, n1}, ws, lo_, 0);
        ladd(CNT(C_PUB), lane);
      }
      STAMPL(11);
      lane = lane_id(); q = lane >> 4; i16 = lane & 15;
      if (L == 0 && anyl(fb != tag)) poll(ws, fbo, tag, dead);
      FENCE();
      u4 wo[18];
      auto wo_at = [&](int i) INL -> u32 { return (i < 10 ? L_Y + i * 1024 : L_KEEP + 2048 + (i - 10) * 1024) + lane * 16; };
      Keep<false> kp;
      if (w < 2) {
        UNR for (int i = 0; i < 18; ++i) wo[i] = (L == 1 && WST) ? ld<u4>(wo_at(i)) : gld4(ws, O_WOT + lane * 16, (9 * c + 8) * 18432 + i * 1024, 1);
        if (L == 0) kp.x[0] = gld2(mkrs(a.x, 2359296), (32 * rt + i16) * 1152 + (144 * c + 4 * q) * 2, 256 + w * 16 * 1152, 0);
        else { u4 t = ld<u4>(L_KEEP + w * 1024 + lane * 16); kp.x[0] = u2{t.x, t.y}; kp.y[0] = u2{t.z, t.w}; }
      }
      {
        const u32 pb = L * OB_L + p * OB_P, tbg = 2 * (rt & 3) + (w >> 1);
        auto o_ld = [&](int i) INL {
          if (i < 8) v[i] = gld4(ws, O_OB + lane * 16, pb + tbg * 16384 + (8 * (w & 1) + i) * 1024, 1);
          else {
            u32 o = O_OB + 131072 + q * 2048 + i16 * 8, so = pb + (w & 1) * 8192 + tbg * 128;
            u2 l0 = gld2(ws, o, so, 1), l1 = gld2(ws, o + 1024, so, 1);
            v[8] = u4{l0.x, l0.y, l1.x, l1.y};
          }
        };
        FENCE();
        const u32 fb2o = w == 3 ? fbo - (O_FC - O_FB) : 0x80000000u;
        if (L == 0) fb = gld1(ws, fb2o, 0, 1);
        if (w >= 2 || (L == 1 && WST)) nap<L ? DO1 : DO0>(); else nap<DOA>();
        FENCE();
        UNR for (int i = 0; i < 9; ++i) o_ld(i);
        NOUNR for (int it = dead ? SPIN_G - 1 : 0;; ++it) {
          FENCE();
          u64 bad[9], any = 0;
          UNR for (int i = 0; i < 9; ++i) { bad[i] = __builtin_amdgcn_ballot_w64(vm(0, v[i]) == SENT); pins(bad[i]); any |= bad[i]; }
          if (!any) break;
          nap<L ? DOR1 : DOR>();
          UNR for (int i = 0; i < 9; ++i) if (bad[i]) o_ld(i);
          if (it >= SPIN_G) { dead = 1; break; }
        }
        FENCE();
        STAMPL(12);
        if (L == 0 && w == 3 && anyl(fb != tag)) poll(ws, fb2o, tag, dead);
        lwait(CNT(C_PUB), 8 * L + 8, dead);
        if (L == 0) lwait(CNT(C_QA), 4, dead);
        const u32 sb = L_STG + (w >> 1) * 18432 + lane * 16;
        UNR for (int i = 0; i < 8; ++i) st<u4>(sb + (8 * (w & 1) + i) * 1024, v[i]);
        st<u4>(sb + (16 + (w & 1)) * 1024, v[8]);
      }
      if (w >= 2) { bar(); STAMPL(13); }
      if (w < 2) {
        tail<false, L>(a, ws, w, rt, c, tag, wo, kp, dead STPASS);
        if (L == 0) {
          lane = lane_id();
          st<u4>(L_KEEP + w * 1024 + lane * 16, u4{kp.x[0].x, kp.x[0].y, kp.y[0].x, kp.y[0].y});
          if (WST) {
            lwait(CNT(C_REDB), 6, dead);
            UNR for (int i = 0; i < 9; ++i) st<u4>(wo_at(2 * i) + w * 1024, w ? wo[2 * i + 1] : wo[2 * i]);
          }
        }
      }
      else if (RST != 1 && L == 0 && w == 2) {
        if (RST == 2) lwait(CNT(C_REDA), 6, dead); else lwait(CNT(C_G2), 6, dead);
        nap<DRS>();
        UNR for (int j = 5; j < 9; ++j) w_slot(ws, j, rot, g, lane);
        vmwait0();
        lset(CNT(C_PA1), 3);
      }
    }
}

__global__ void __launch_bounds__(512, 2) fused_kernel(Args a) {
  const int tid = threadIdx.x, lane = tid & 63, w = rfl(tid >> 6), b = blockIdx.x;
  u64 pc0;
  u32 clen;
  asm volatile("s_getpc_b64 %0\n.Lpf_here:\n\ts_mov_b32 %1, fused_code_end-.Lpf_here" : "=s"(pc0), "=s"(clen));
  clen = clen < 131072 ? clen : 0;
  u32 cpf = gld1(mkrs((const void*)pc0, clen), (((b >> 3) & 1) * 8 + w) * 8192 + lane * 128, 0, 0);
  FENCE();
  asm volatile("" : "+s"(a.x), "+s"(a.wqkv), "+s"(a.wphi), "+s"(a.wo), "+s"(a.bk), "+s"(a.bv), "+s"(a.sk), "+s"(a.ssm), "+s"(a.dv), "+s"(a.out), "+s"(a.ws));
  const int q = lane >> 4, i16 = lane & 15;
  const int xcd = b & 7, r = b >> 3;
  const int p = 4 * (xcd >> 1) + (r >> 3), g = 8 * (xcd & 1) + (r & 7), rot = ((r & 7) + 2 * (r >> 3)) % 9;
  const int rt = 4 * p + (g >> 2), c = g & 3;
  rsrc_t ws = mkrs(a.ws, WS_BYTES);
  int dead = 0;
  STAMP(0); STAMPC(29);
  u32 tag = *(const CAS u32*)a.ws;
#define BAR0() do { bar(); asm volatile("" : "+s"(tag)); tag += 1; } while (0)

  if (w < 4) {
    __builtin_amdgcn_s_setprio(2);
    u4 B[36];
    float mS[2], mQ[2];
    {
      rsrc_t rx = mkrs(a.x, 2359296);
      u32 vo[2];
      UNR for (int par = 0; par < 2; ++par) vo[par] = (8 * par + (lane >> 3)) * 1152 + (((lane & 7) ^ (4 * par + (lane >> 4))) << 4);
      auto x_step = [&](int j) {
        int kr = j + rot; kr -= kr >= 9 ? 9 : 0;
        UNR for (int rg = 0; rg < 4; ++rg) dma<0>(rx, (j & 7) * 16384 + w * 4096 + rg * 1024, vo[rg & 1], (128 * p + 32 * w + 16 * (rg >> 1)) * 1152 + kr * 128);
      };
      const u4 sw = kv_w(a, tid, c);
      SB();
      STAMPD(26);
      BAR0();
      STAMPD(27);
      SB();
      UNR for (int j = 0; j < XLA; ++j) x_step(j);
      FENCE();
      SB();
      STAMP(1);
      u32 a0 = w * 4096 + (i16 >> 3) * 1024 + (i16 & 7) * 128 + ((q ^ (i16 >> 1)) << 4), a1 = a0 ^ 64, pa = CNT(C_PROG) + 4 * w;
      f4 gS[2] = {f4{0, 0, 0, 0}, f4{0, 0, 0, 0}}, gQ[2] = {f4{0, 0, 0, 0}, f4{0, 0, 0, 0}};
      const u4 ones = {0x3F803F80u, 0x3F803F80u, 0x3F803F80u, 0x3F803F80u};
#define X0STEP(j)                                                                                                                      \
  {                                                                                                                                    \
    u32 pv = j + 1;                                                                                                                    \
    asm volatile("s_waitcnt vmcnt(%8)\n\tds_read_b128 %0, %4\n\tds_read_b128 %1, %4 offset:2048\n\tds_read_b128 %2, %5\n\t"            \
                 "ds_read_b128 %3, %5 offset:2048\n\ts_waitcnt lgkmcnt(0)\n\tds_write_b32 %6, %7"                                       \
                 : "=&v"(B[4 * j]), "=&v"(B[4 * j + 1]), "=&v"(B[4 * j + 2]), "=&v"(B[4 * j + 3])                                        \
                 : "v"(a0 + (j & 7) * 16384), "v"(a1 + (j & 7) * 16384), "v"(pa), "v"(pv), "n"(4 * ((j + XLA - 1 < 8 ? j + XLA - 1 : 8) - j))                                                 \
                 : "memory");                                                                                                          \
    if (j + XLA < 9) { x_step(j + XLA); FENCE(); }                                                                                                \
    UNR for (int i = 0; i < 4; ++i) { gS[i & 1] = mfma(ones, B[4 * j + i], gS[i & 1]); gQ[i & 1] = mfma(B[4 * j + i], B[4 * j + i], gQ[i & 1]); } \
  }
      X0STEP(0)
      pin(cpf);
      X0STEP(1) X0STEP(2) X0STEP(3) X0STEP(4) X0STEP(5) X0STEP(6) X0STEP(7) X0STEP(8)
      STAMP(2);
      UNR for (int tb = 0; tb < 2; ++tb) {
        float d = (i16 & 2) ? ((i16 & 1) ? gQ[tb].w : gQ[tb].z) : ((i16 & 1) ? gQ[tb].y : gQ[tb].x);
        d = (q == (i16 >> 2)) ? d : 0.0f;
        d = sw16add(d, d); d = sw32add(d, d);
        f2 t = ln_stats(gS[tb].x, d);
        mS[tb] = t.x; mQ[tb] = t.y;
      }
      lwait(CNT(C_KVL), 2, dead);
      STAMPD(28);
      KVin in;
      kv_read_asm(tid, in);
      ladd(CNT(C_KVR), lane);
      kv_compute(ws, tid, rt, c, in, sw, true);
    }
    u32 wp = 0;
    main_layer<0>(a, ws, b, w, p, g, rt, c, rot, tag, B, mS, mQ, wp, dead STPASS);
    main_layer<1>(a, ws, b, w, p, g, rt, c, rot, tag, B, mS, mQ, wp, dead STPASS);
  } else {
    const int pr = (w - 4) >> 1, e = w & 1;
    const u32 lH = pr ? L_H1 : L_H0, lU = pr ? L_U1 : L_U0;
    float dv, dq;
    auto dv_ld = [&]() INL { dv = load_dv(a, 2 * b + pr); dq = load_dv(a, 512 + (b >> 2)); };
    auto wq_slot = [&](int j) INL {
      int kr = j + rot; kr -= kr >= 9 ? 9 : 0;
      UNR for (int i = 0; i < 4; ++i) dma<SC1>(ws, L_WQ + j * 4096 + i * 1024, O_WT + L_WQ + lane * 16, g * WT_G + kr * 4096 + i * 1024);
    };
    if (w == 4) {
      const int cl = b % 27, kr0 = b / 27;
      if (b < 243) {
        rsrc_t rw = mkrs(a.wqkv, 576 * 3456);
        UNR for (int rg = 0; rg < 8; ++rg)
          dma<0>(rw, L_TILE + rg * 1024, ((lane >> 3) ^ (rg & 1)) * 3456 + (((lane & 7) ^ (4 * ((rg >> 1) & 1))) << 4), (64 * kr0 + 8 * rg) * 3456 + cl * 128);
      }
      FENCE();
      SB();
      STAMPD(26);
      BAR0();
      STAMPD(27);
      SB();
      dv_ld();
      SB();
      __builtin_amdgcn_s_setprio(3);
      if (b < 243) {
        const int cg = lane >> 3, rg = lane & 7, sec = cl / 9;
        u32 dst[8];
        UNR for (int j = 0; j < 8; ++j) {
          int cc = 64 * (cl - 9 * sec) + 8 * cg + j, h = cc / 18, d = cc - 18 * h, hl = h & 1;
          int hi = d >= 9, qq = sec ? 2 * (sec - 1) + hl : 2 * hl + hi, s = sec ? d : d - 9 * hi;
          bool left = s >= (sec ? 16 : 8);
          int row = left ? 64 + 3 * qq + (sec ? s - 16 : 2) : 16 * (s >> 2) + 4 * qq + (s & 3);
          u32 o = (sec == 0 && !left) ? L_WQ + kr0 * 4096 : kr0 * SLOT;
          dst[j] = O_WT + (h >> 1) * WT_G + o + row * 128 + ((rg ^ ((row >> 1) & 7)) << 4);
        }
        SB();
        vmwait0();
        pin(cpf);
        STAMPD(28);
        u4 R[8];
        const u32 rb = L_TILE + rg * 1024 + ((cg ^ (4 * ((rg >> 1) & 1))) << 4);
        UNR for (int i = 0; i < 8; ++i) R[i] = ld<u4>(rb + ((i ^ (rg & 1)) << 7));
        FENCE();
        lset(CNT(C_WT), 1);
        u4 o[8];
        UNR for (int j = 0; j < 8; ++j) {
          UNR for (int d = 0; d < 4; ++d) o[j][d] = __builtin_amdgcn_perm(R[2 * d + 1][j >> 1], R[2 * d][j >> 1], (j & 1) ? 0x07060302u : 0x05040100u);
          gst4(o[j], ws, dst[j], 0);
        }
        SB();
        float sel = 0;
        UNR for (int j = 0; j < 8; ++j) {
          float v = dot2(o[j].x, 0x3F803F80u, dot2(o[j].y, 0x3F803F80u, 0.0f)) + dot2(o[j].z, 0x3F803F80u, dot2(o[j].w, 0x3F803F80u, 0.0f));
          v += __uint_as_float(__builtin_amdgcn_update_dpp(0, __float_as_uint(v), 0xB1, 0xF, 0xF, true));
          v += __uint_as_float(__builtin_amdgcn_update_dpp(0, __float_as_uint(v), 0x4E, 0xF, 0xF, true));
          v += __uint_as_float(__builtin_amdgcn_update_dpp(0, __float_as_uint(v), 0x141, 0xF, 0xF, true));
          sel = rg == j ? v : sel;
        }
        gst1(__float_as_uint(sel), ws, O_CS + lane * 4, kr0 * 6912 + cl * 256);
        vmwait0();
      } else {
        pin(cpf);
        lset(CNT(C_WT), 1);
      }
      if (b < 243) setflag(ws, O_FW + (cl * 9 + kr0) * 128, tag, lane);
      STAMP(1);
      auto need_x = [&]() {
      NOUNR for (int i = dead ? SPIN_L : 0; i < SPIN_L; ++i) {
        u4 v;
        asm volatile("ds_read_b128 %0, %1\n\ts_waitcnt lgkmcnt(0)" : "=v"(v) : "v"(CNT(C_PROG)) : "memory");
        if (rfl(umin(umin(v.x, v.y), umin(v.z, v.w))) >= 9) break;
        if (i == SPIN_L - 1) dead = 1;
        __builtin_amdgcn_s_sleep(1);
      }
      };
      if (PLATE) need_x();
      {
        int l6 = lane < 54 ? lane : 0, kr = l6 / 6, i = l6 - 6 * kr;
        int ln = 9 * (i >> 1) + ((36 * g + 35 * (i & 1)) >> 6);
        nap<DW>();
        poll<DWR>(ws, O_FW + (ln * 9 + kr) * 128, tag, dead);
      }
      STAMP(2);
      lset(CNT(C_PA0), 1);
      w_slot(ws, 0, rot, g, lane); w_slot(ws, 2, rot, g, lane);
      STAMP(7);
      vmwait<10>(); lset(CNT(C_WPROG), 1);
      STAMP(8);
      w_slot(ws, 4, rot, g, lane); w_slot(ws, 6, rot, g, lane);
      vmwait<20>(); lset(CNT(C_WPROG), 2);
      w_slot(ws, 8, rot, g, lane);
      wq_slot(0); vmwait<24>(); lset(CNT(C_WPROG), 3);
      STAMP(9);
      wq_slot(1); vmwait<18>(); lset(CNT(C_WPROG), 4);
      wq_slot(2); vmwait<12>(); lset(CNT(C_WPROG), 5);
      wq_slot(3);
      STAMP(12);
      const u32 fao = lane < 16 ? O_FA + wg_of(lane, g) * 128 : O_FO + (i16 * 9 + 2 * c + q - 1) * 128;
      const u32 fav = gld1(ws, fao, 0, 1);
      FENCE();
      vmwait<1>();
      ladd(CNT(C_PA1), lane);
      if (anyl(fav != tag)) poll(ws, fao, tag, dead);
      FENCE();
      if (FAW) lwait(CNT(C_PA1), 2, dead);
      lset(CNT(C_FAOK), 1);
      __builtin_amdgcn_s_setprio(0);
      STAMP(3);
    } else if (w < 7) {
      const int chn = 2 * b + (w - 5);
      const u32 mH = w == 5 ? L_H0 : L_H1, mU = w == 5 ? L_U0 : L_U1;
      auto kv_dma = [&]() {
        const int r3 = lane / 18;
        const u32 vo = r3 * 1152 + (lane - 18 * r3) * 16 + 288 * c;
        if (w == 5) {
          rsrc_t rk = mkrs(a.bk, 2359296);
          if (lane < 54) {
            dma<0>(rk, L_KT, rt ? vo + (32 * rt - 3) * 1152 : 0x80000000u, 0);
            UNR for (int i = 1; i < 11; ++i) dma<0>(rk, L_KT + i * 864, vo, (32 * rt - 3 + 3 * i) * 1152);
          }
          if (lane < 36) dma<0>(rk, L_KT + 11 * 864, vo, (32 * rt + 30) * 1152);
        } else {
          rsrc_t rv = mkrs(a.bv, 2359296);
          if (lane < 54) { UNR for (int i = 0; i < 10; ++i) dma<0>(rv, L_VT + i * 864, vo, (32 * rt + 3 * i) * 1152); }
          if (lane < 36) dma<0>(rv, L_VT + 10 * 864, vo, (32 * rt + 30) * 1152);
        }
      };
      FENCE();
      SB();
      STAMPD(26);
      BAR0();
      STAMPD(27);
      SB();
      dv_ld();
      SB();
      lwait(CNT(C_PROG) + 4 * (w - 5), KVAT, dead);
      kv_dma();
      STAMP(9);
      vmwait0();
      pin(cpf);
      ladd(CNT(C_KVL), lane);
      STAMP(10);
      lwait(CNT(C_PA0), 1, dead);
      if (w == 5) {
        __builtin_amdgcn_s_setprio(3);
        w_slot(ws, 1, rot, g, lane); w_slot(ws, 3, rot, g, lane); w_slot(ws, 5, rot, g, lane);
        vmwait<20>(); lset(CNT(C_PA0), 2);
        w_slot(ws, 7, rot, g, lane);
        wq_slot(4); vmwait<24>(); lset(CNT(C_PA0), 3);
        wq_slot(5); vmwait<18>(); lset(CNT(C_PA0), 4);
        wq_slot(6); vmwait<12>(); lset(CNT(C_PA0), 5);
        wq_slot(7);
        wq_slot(8);
        __builtin_amdgcn_s_setprio(0);
      } else {
        nap<DCS>();
        u32 cv[2][9];
        UNR for (int rd = 0; rd < 2; ++rd) {
          int blk = rd * 4 + q, qq = i16 >> 2, ee = i16 & 3;
          int kvc = 576 + 576 * (qq >> 1) + 36 * g + 18 * (qq & 1), qc = 36 * g + 9 * qq;
          int col = rd == 0 ? kvc + 4 * blk + ee : (blk == 4 ? (ee < 2 ? kvc + 16 + ee : qc + 8) : qc + 4 * ((blk - 5) & 1) + ee);
          UNR for (int k = 0; k < 9; ++k) cv[rd][k] = gld1(ws, O_CS + col * 4, k * 6912, 1);
        }
        lwait(CNT(C_TBL), 1, dead);
        UNR for (int rd = 0; rd < 2; ++rd) {
          float s = 0;
          UNR for (int k = 0; k < 9; ++k) s += __uint_as_float(cv[rd][k]);
          lset((rd == 0 || q < 3) ? L_CSL + rd * 256 + lane * 4 : CNT(31), __float_as_uint(s));
        }
        lset(CNT(C_TBL), 2);
        lwait(CNT(C_WPROG), HA2, dead);
      }
      FENCE();
      Hin hin;
      h_load(a, chn, lane, hin);
      FENCE();
      if (w == 5) {
        const u4 s4 = {SENT, SENT, SENT, SENT};
        UNR for (int i = 0; i < 9; ++i) gst4(s4, ws, O_OB + lane * 16, p * OB_P + i * 16384 + g * 1024);
        FENCE();
        vmwait<13>();
        ladd(CNT(C_PA1), lane);
      }
      FENCE();
      lwait(CNT(C_KVR), 5, dead);
      h_stage(mH, mU, lane, hin);
      FENCE();
      ladd(CNT(C_PU0), lane);
      STAMP(8);
      if (w == 5) { vmwait0(); lset(CNT(C_PU2), 1); setflag(ws, O_FC + b * 128, tag, lane); STAMP(7); }
      if (w == 6) {
        lwait(CNT(C_PA1), 2, dead);
        const u32 f0 = O_FK + ((2 * b) / 144 * 64 + lane) * 128, f1 = O_FK + (192 + lane) * 128;
        nap<DK>();
        FENCE();
        const u32 v0 = gld1(ws, f0, 0, 1), v1 = gld1(ws, f1, 0, 1);
        if (anyl(v0 != tag)) poll<DKR>(ws, f0, tag, dead);
        FENCE();
        STAMP(2);
        u_stage(ws, L_U0, 2 * b, lane);
        u_stage(ws, L_U1, 2 * b + 1, lane);
        if (anyl(v1 != tag)) { poll(ws, f1, tag, dead); }
        FENCE();
        lwait(CNT(C_PU0), 3, dead);
        u_stage(ws, L_U2, 512 + (b >> 2), lane);
        vmwait<4>();
        lset(CNT(C_PU1), 1);
        STAMP(3);
      }
    } else {
      if (lane < 32) lset(L_CNT + lane * 4, 0);
      const int cl = b % 9, gg = b / 9;
      rsrc_t rp = mkrs(a.wphi, 32 * 648);
      u4 p0 = gld4(rp, lane * 16, g * 1296, 0), p1 = gld4(rp, lane < 17 ? 1024 + lane * 16 : 0x80000000u, g * 1296, 0);
      const u4 sw = kv_w(a, 256 + (lane & 31), c);
      const u4 s4 = {SENT, SENT, SENT, SENT};
      UNR for (int i = 0; i < 3; ++i) gst4(s4, ws, (i < 2 || lane < 48) ? O_SP + i * 1024 + lane * 16 : 0x80000000u, g * SP_G + p * SP_P);
      FENCE();
      SB();
      STAMPD(26);
      BAR0();
      STAMPD(27);
      SB();
      dv_ld();
      SB();
      st<u4>(L_PT + lane * 16, p0);
      if (lane < 17) st<u4>(L_PT + 1024 + lane * 16, p1);
      FENCE();
      auto wp16 = [&](int hd, int d, int f) -> u32 { u32 v = ld<u16>(L_PT + (hd * 324 + d * 18 + (f < 18 ? f : 0)) * 2); return f < 18 ? v : 0u; };
      UNR for (int rd = 0; rd < 2; ++rd) {
        int t = rd * 64 + lane, tt = t < 80 ? t : 79, m = tt >> 4, cc = (tt >> 3) & 1, hd = (tt >> 2) & 1, f = 4 * m + (tt & 3);
        u4 v;
        UNR for (int d = 0; d < 4; ++d) v[d] = wp16(hd, 8 * cc + 2 * d, f) | (wp16(hd, 8 * cc + 2 * d + 1, f) << 16);
        st<u4>(L_TK + (tt >> 3) * 144 + (tt & 7) * 16, v);
        int hq = (tt >> 3) & 1, dh = (tt >> 2) & 1;
        UNR for (int d = 0; d < 4; ++d) v[d] = wp16(hq, 9 * dh + 2 * d, f) | (wp16(hq, 9 * dh + 2 * d + 1, f) << 16);
        st<u4>(L_TQ + m * 272 + (tt & 15) * 16, v);
        st<u32>(L_TQ2 + m * 68 + (tt & 15) * 4, wp16(hq, 9 * dh + 8, f));
      }
      {
        int tt = lane < 40 ? lane : 39, m = tt >> 3, hd = (tt >> 2) & 1, f = 4 * m + (tt & 3);
        st<u32>(L_TK2 + m * 36 + (tt & 7) * 4, wp16(hd, 16, f) | (wp16(hd, 17, f) << 16));
        u4 z = {0, 0, 0, 0};
        if (lane < 10) st<u4>(L_TK + lane * 144 + 128, z);
        if (lane < 5) { st<u4>(L_TQ + lane * 272 + 256, z); st<u32>(L_TK2 + lane * 36 + 32, 0u); st<u32>(L_TQ2 + lane * 68 + 64, 0u); }
      }
      FENCE();
      lset(CNT(C_TBL), 1);
      vmwait0();
      pin(cpf);
      lset(CNT(C_WOK), 1);
      setflag(ws, O_FA + b * 128, tag, lane);
      STAMP(7);
      lwait(CNT(C_WT), 1, dead);
      lwait(CNT(C_PROG) + 8, WOAT, dead);
      if (b < 144) {
        rsrc_t rw = mkrs(a.wo, 576 * 1152);
        UNR for (int m = 0; m < 5; ++m)
          if (m < 4 || lane < 32) dma<0>(rw, L_TILE + m * 1024, (lane >> 3) * 1152 + (lane & 7) * 16, (36 * gg + 8 * m) * 1152 + cl * 128);
      }
      vmwait0();
      if (b < 144) {
        const u32 ta = L_TILE + (18 * (q >> 1) + 8 * (q & 1) + (i16 >> 2)) * 128 + (i16 & 3) * 8;
        UNR for (int G = 0; G < 4; ++G) gst4(trd2(ta + G * 32, ta + G * 32 + 512), ws, O_WOT + lane * 16, (4 * cl + G) * 18432 + gg * 1024);
        u2 lf = trd(L_TILE + (16 + ((i16 >> 2) & 1) + 18 * (i16 >> 3)) * 128 + q * 32 + (i16 & 3) * 8);
        gst2(lf, ws, O_WOT + q * 18432 + i16 * 16, 4 * cl * 18432 + (16 + (gg >> 3)) * 1024 + ((gg >> 1) & 3) * 256 + (gg & 1) * 8);
      }
      FENCE();
      vmwait0();
      setflag(ws, O_FO + b * 128, tag, lane);
      STAMP(9);
      lwait(CNT(C_KVL), 2, dead);
      {
        KVin in;
        kv_read(256 + (lane & 31), in);
        FENCE();
        ladd(CNT(C_KVR), lane);
        kv_compute(ws, 256 + (lane & 31), rt, c, in, sw, lane < 32);
      }
      FENCE();
      vmwait0();
      ladd(CNT(C_KVC), lane);
      STAMP(8);
      lwait(CNT(C_KVC), 5, dead);
      setflag(ws, O_FK + (c * 64 + rt) * 128, tag, lane);
      STAMP(10);
      if (lane < 32) st<u4>(lane < 2 ? L_H2 + lane * 16 : L_ZERO + (lane - 2) * 16, u4{0, 0, 0, 0});
      FENCE();
      lwait(CNT(C_WPROG), HA2, dead);
      UNR for (int m = 0; m < 4; ++m) dma<0>(mkrs(a.ssm, 2359296), L_H2 + 32 + m * 1024, lane * 16, (512 + (b >> 2)) * 4096 + m * 1024);
      vmwait0();
      ladd(CNT(C_PU0), lane);
      STAMP(3);
    }
    pins(dv); pins(dq);
    u4 wo[36];
    auto wo_load = [&]() { UNR for (int i = 0; i < 36; ++i) wo[i] = gld4(ws, O_WOT + lane * 16, (9 * c + 2 * (w - 4)) * 18432 + i * 1024, 1); };
    lwait(CNT(C_FAOK), 1, dead);
    if (w != 6) wo_load();
    FENCE();
    STAMP(11);
    lwait(CNT(C_PU0), 3, dead);
    lwait(CNT(C_PU1), 1, dead);
    STAMP(4);
    late_fill(ws, w - 4, p, g, rt, c, lane);
    FENCE();
    {
      f4 keep[4];
      conv_pair(ws, lH, lU, w - 4, pr ? CNT(C_PB1) : CNT(C_PB0), O_FB + b * 128, tag, keep, lane, dead STPASS);
      STAMP(5);
      lwait(CNT(C_PU1), 2, dead);
      conv_quarter(b & 3, w - 4, lane);
      STAMPD(22);
      conv_pair_out(ws, lU, 2 * b + pr, w - 4, pr ? CNT(C_PB1) : CNT(C_PB0), dv, keep, lane, dead);
      STAMPD(23);
    }
    if (w < 6) conv_quarter_out(ws, 512 + (b >> 2), b & 3, w - 4, dq, lane, dead);
    FENCE();
    ladd(CNT(C_QA), lane);
    STAMPD(24);
    vmwait0();
    if (ladd_rtn(CNT(C_FYC), lane) == 3) setflag(ws, O_FY + b * 128, tag, lane);
    STAMP(6);
    if (w == 6) wo_load();
    __builtin_amdgcn_s_setprio(3);
    Keep<true> kp;
    tail<true, 0>(a, ws, w, rt, c, tag, wo, kp, dead STPASS);
    if (RST == 1 && w == 7) {
      const int l2 = lane_id();
      UNR for (int j = 5; j < 9; ++j) w_slot(ws, j, rot, g, l2);
      vmwait0();
      lset(CNT(C_PA1), 3);
    }
    tail<true, 1>(a, ws, w, rt, c, tag, wo, kp, dead STPASS);
    if (b == 0 && w == 7 && lane_id() == 0) gst1(tag, ws, 0, 0);
  }
  STAMP(31); STAMPC(30);
}

extern "C" __device__ __attribute__((used, noinline)) void fused_code_end() {}

extern "C" void kernel_launch(void* const* d_in, const int* in_sizes, int n_in, void* d_out, int out_size, void* d_ws, size_t ws_size, hipStream_t stream) {
  Args a;
  a.x = (const u16*)d_in[0]; a.wqkv = (const u16*)d_in[1]; a.wphi = (const u16*)d_in[2]; a.wo = (const u16*)d_in[3];
  a.bk = (const u16*)d_in[5]; a.bv = (const u16*)d_in[6]; a.sk = (const u16*)d_in[7]; a.ssm = (const u16*)d_in[8]; a.dv = (const u16*)d_in[9];
  a.out = (u16*)d_out; a.ws = (unsigned char*)d_ws;
  (void)hipFuncSetAttribute((const void*)fused_kernel, hipFuncAttributeMaxDynamicSharedMemorySize, LDS_BYTES);
  fused_kernel<<<256, 512, LDS_BYTES, stream>>>(a);
}
```
